```python
import torch

def _softmax(x, axis=-1):
    return torch.softmax(x, dim=axis)
def _gelu(x):
    return torch.nn.functional.gelu(x, approximate="tanh")
def _silu(x):
    return torch.nn.functional.silu(x)
def _sigmoid(x):
    return torch.sigmoid(x)
def _layernorm(x, axis=-1, eps=1e-5):
    m = x.mean(dim=axis, keepdim=True)
    v = x.var(dim=axis, keepdim=True, unbiased=False)
    return (x - m) / torch.sqrt(v + eps)
def _scatter_add(tgt, idx, src, axis=0):
    return tgt.clone().index_add_(axis, idx, src)
def _segment_sum(src, seg, n):
    out = torch.zeros((n,) + src.shape[1:], dtype=src.dtype)
    return out.index_add_(0, seg, src)
def _pad(x, pw):
    flat = tuple(v for pair in reversed(pw) for v in pair)
    return torch.nn.functional.pad(x, flat)

kernel_name = 'standalone__gqa_softmax__near_ridge'

def setup_inputs():

    _w = (lambda a: (lambda t: t.to(torch.bfloat16) if t.is_floating_point() else t)(torch.as_tensor(a)))
    B, Hq, Hkv, Sq, Sk, D = 16, 32, 16, 496, 512, 128
    return {
        "q": _w((lambda *s: torch.randn(*s, dtype=torch.bfloat16) * 0.8)(B, Hq, Sq, D)),
        "k": _w((lambda *s: torch.randn(*s, dtype=torch.bfloat16) * 0.8)(B, Hkv, Sk, D)),
        "v": _w((lambda *s: torch.randn(*s, dtype=torch.bfloat16) * 0.05)(B, Hkv, Sk, D)),
    }

def reference(q, k, v):
    B, Hq, Sq, D = q.shape
    Hkv = k.shape[1]
    rep = Hq // Hkv
    k = (lambda x, n, axis: torch.repeat_interleave(x, n, dim=axis))(k, rep, axis=1)
    v = (lambda x, n, axis: torch.repeat_interleave(x, n, dim=axis))(v, rep, axis=1)
    scale = D ** -0.5
    scores = torch.einsum("bhqd,bhkd->bhqk", q, k) * scale
    p = _softmax(scores, axis=-1)
    return torch.einsum("bhqk,bhkd->bhqd", p, v)
```

```cpp
#include <hip/hip_runtime.h>
#include <hip/hip_bf16.h>
#include <cstdio>
using bf16 = __hip_bfloat16;
constexpr int   D = 128, NW = 8, QBLK = 32, KVBLK = 64;
constexpr float SCALE = 0.088388347648318440f;
constexpr float THR = 8.f;
constexpr int SDEPTH = 2;
constexpr int SQ2 = 992, SKV = 512;
constexpr int LDQ = D, LDK = D, LDO = D;
constexpr size_t SHM_V = KVBLK * D * 2, SHM_K = KVBLK * D * 2, SHM_ATTN = 2 * SHM_V + 2 * SHM_K + NW * 64 * 4;
using bf16x8 = __attribute__((ext_vector_type(8))) short;
using s16x4  = __attribute__((ext_vector_type(4))) short;
using f32x16 = __attribute__((ext_vector_type(16))) float;
using u32x4  = __attribute__((ext_vector_type(4))) unsigned;
#define KSWZ(row, colB) ((row) * 256 + ((colB) ^ (((row) & 7) << 4)))
#define SBAR() __builtin_amdgcn_sched_barrier(0)
__device__ __forceinline__ int crow(int r, int hi) { return (r & 3) + 8 * (r >> 2) + 4 * hi; }
__device__ __forceinline__ unsigned cvtpk(float lo, float hi) {
  unsigned r; asm volatile("v_cvt_pk_bf16_f32 %0, %1, %2" : "=v"(r) : "v"(lo), "v"(hi)); return r;
}
__device__ __forceinline__ bf16x8 ld8(const bf16* p) { return *reinterpret_cast<const bf16x8*>(p); }
__device__ __forceinline__ void partialSM(f32x16& p0, f32x16& p1, float& m_reg, float& mn, float& alpha) {
  constexpr float C = SCALE * 1.4426950408889634f;
  float pmax = p0[0]; for (int r = 1; r < 16; ++r) pmax = fmaxf(pmax, p0[r]); for (int r = 0; r < 16; ++r) pmax = fmaxf(pmax, p1[r]);
  { auto rr = __builtin_amdgcn_permlane32_swap(__float_as_uint(pmax), __float_as_uint(pmax), false, false);
    pmax = fmaxf(__uint_as_float(rr[0]), __uint_as_float(rr[1])); }
  if (__builtin_expect(__all(pmax - m_reg <= THR / SCALE), 1)) { mn = m_reg; alpha = 1.f; }
  else { mn = fmaxf(m_reg, pmax); alpha = __builtin_amdgcn_exp2f((m_reg - mn) * C); m_reg = mn; }
  float mnC = -mn * C;
  for (int r = 0; r < 16; ++r) p0[r] = fmaf(p0[r], C, mnC); for (int r = 0; r < 16; ++r) p1[r] = fmaf(p1[r], C, mnC);
  for (int r = 0; r < 16; ++r) p0[r] = __builtin_amdgcn_exp2f(p0[r]);
}
__device__ __forceinline__ void finishSM(f32x16& p0, f32x16& p1, float alpha, float& l_reg, bf16x8& pa0, bf16x8& pa1, bf16x8& pa2, bf16x8& pa3) {
  for (int r = 0; r < 16; ++r) p1[r] = __builtin_amdgcn_exp2f(p1[r]);
  float ps = 0; for (int r = 0; r < 16; ++r) ps += p0[r]; for (int r = 0; r < 16; ++r) ps += p1[r];
  { auto rr = __builtin_amdgcn_permlane32_swap(__float_as_uint(ps), __float_as_uint(ps), false, false);
    ps = __uint_as_float(rr[0]) + __uint_as_float(rr[1]); }
  l_reg = l_reg * alpha + ps;
#define PK4(P, BASE, OUT) do { unsigned a0 = cvtpk(P[BASE + 0], P[BASE + 1]), a1 = cvtpk(P[BASE + 2], P[BASE + 3]);   \
    unsigned b0 = cvtpk(P[BASE + 4], P[BASE + 5]), b1 = cvtpk(P[BASE + 6], P[BASE + 7]);                              \
    auto r0 = __builtin_amdgcn_permlane32_swap(a0, b0, false, false); auto r1 = __builtin_amdgcn_permlane32_swap(a1, b1, false, false); \
    u32x4 w = {r0[0], r1[0], r0[1], r1[1]}; OUT = *reinterpret_cast<bf16x8*>(&w); } while (0)
  PK4(p0, 0, pa0); PK4(p0, 8, pa1); PK4(p1, 0, pa2); PK4(p1, 8, pa3);
#undef PK4
}
__device__ __forceinline__ void qkt(f32x16& p0, f32x16& p1, const bf16* Ks, const bf16x8* qr, int r32, int hi) {
  p0 = f32x16{}; p1 = f32x16{};
  for (int d0 = 0; d0 < 8; ++d0) { int cb = (d0 * 16 + hi * 8) * 2;
    bf16x8 b0 = *reinterpret_cast<const bf16x8*>((const char*)Ks + KSWZ(r32, cb));
    bf16x8 b1 = *reinterpret_cast<const bf16x8*>((const char*)Ks + KSWZ(32 + r32, cb));
    p0 = __builtin_amdgcn_mfma_f32_32x32x16_bf16(b0, qr[d0], p0, 0, 0, 0);
    p1 = __builtin_amdgcn_mfma_f32_32x32x16_bf16(b1, qr[d0], p1, 0, 0, 0); }
}
__device__ __forceinline__ int v_st(int k, int c) { const int kk = (k & ~0xC) | ((k & 4) << 1) | ((k & 8) >> 1); return ((kk >> 3) * 4 + (c >> 5)) * 512 + ((kk & 7) * 32 + (c & 31)) * 2; }
__device__ __forceinline__ int v_rd_base(int lane) { return ((lane & 3) << 3) | (((lane >> 2) & 3) << 6) | (((lane >> 4) & 1) << 5) | (((lane >> 5) & 1) << 8); }
constexpr int v_rd_off(int d0, int ks, int half) { return d0 * 512 + ks * 4096 + half * 2048; }
template <int OFF> __device__ __forceinline__ s16x4 tr_read(int vb) {
  s16x4 r; asm volatile("ds_read_b64_tr_b16 %0, %1 offset:%2" : "=&v"(r) : "v"(vb), "i"(OFF) : "memory"); return r;
}
template <int D0> __device__ __forceinline__ void pv_one(f32x16& od, int vb, bf16x8 pa0, bf16x8 pa1, bf16x8 pa2, bf16x8 pa3) {
  const s16x4 l0 = tr_read<v_rd_off(D0, 0, 0)>(vb), h0 = tr_read<v_rd_off(D0, 0, 1)>(vb), l1 = tr_read<v_rd_off(D0, 1, 0)>(vb), h1 = tr_read<v_rd_off(D0, 1, 1)>(vb);
  const s16x4 l2 = tr_read<v_rd_off(D0, 2, 0)>(vb), h2 = tr_read<v_rd_off(D0, 2, 1)>(vb), l3 = tr_read<v_rd_off(D0, 3, 0)>(vb), h3 = tr_read<v_rd_off(D0, 3, 1)>(vb);
  asm volatile("s_waitcnt lgkmcnt(0)" ::: "memory"); SBAR();
#define PK(L, H) (bf16x8){L[0], L[1], L[2], L[3], H[0], H[1], H[2], H[3]}
  od = __builtin_amdgcn_mfma_f32_32x32x16_bf16(pa0, PK(l0, h0), od, 0, 0, 0);
  od = __builtin_amdgcn_mfma_f32_32x32x16_bf16(pa1, PK(l1, h1), od, 0, 0, 0);
  od = __builtin_amdgcn_mfma_f32_32x32x16_bf16(pa2, PK(l2, h2), od, 0, 0, 0);
  od = __builtin_amdgcn_mfma_f32_32x32x16_bf16(pa3, PK(l3, h3), od, 0, 0, 0);
#undef PK
}
__device__ __forceinline__ void pv_d0(f32x16* o, int vb, bf16x8 pa0, bf16x8 pa1, bf16x8 pa2, bf16x8 pa3) {
  pv_one<0>(o[0], vb, pa0, pa1, pa2, pa3); pv_one<1>(o[1], vb, pa0, pa1, pa2, pa3); pv_one<2>(o[2], vb, pa0, pa1, pa2, pa3); pv_one<3>(o[3], vb, pa0, pa1, pa2, pa3);
}
__device__ __forceinline__ void attn_dense_body(const bf16* __restrict__ Qb, const bf16* __restrict__ Kh, const bf16* __restrict__ Vh,
                                                bf16* __restrict__ Ob, int rows, char* lds) {
  const int seq = SKV;
  const int tid = threadIdx.x, wid = tid >> 6, lane = tid & 63, r32 = lane & 31, hi = lane >> 5;
  bf16* V_lds = (bf16*)lds; bf16* K_lds = (bf16*)(lds + 2 * SHM_V);
  float* ws = (float*)(lds + 2 * SHM_V + 2 * SHM_K) + wid * 64; float* li_l = ws; float* al_l = ws + 32;
  float m_reg = -1e30f, l_reg = 0; f32x16 o[4] = {}; bf16x8 qr[8];
  const bool valid = wid * QBLK < rows;
  const int qrow = valid ? wid * QBLK + r32 : r32;
  const bf16* Qw = Qb + (long)qrow * LDQ + hi * 8;
#pragma unroll
  for (int d0 = 0; d0 < 8; ++d0) qr[d0] = ld8(Qw + d0 * 16);
  const int sr = tid >> 4, sc = (tid & 15) * 8, vst0 = v_st(sr, sc), vst1 = v_st(32 + sr, sc);
  const int vb0 = (int)(uintptr_t)V_lds + v_rd_base(lane);
  struct { bf16x8 vs0, vs1, ks0, ks1; } sr_[SDEPTH];
#define SLOAD(i, k0) do { sr_[i].vs0 = ld8(&Vh[(long)((k0) + sr) * LDK + sc]); sr_[i].vs1 = ld8(&Vh[(long)((k0) + 32 + sr) * LDK + sc]); \
    sr_[i].ks0 = ld8(&Kh[(long)((k0) + sr) * LDK + sc]); sr_[i].ks1 = ld8(&Kh[(long)((k0) + 32 + sr) * LDK + sc]); } while (0)
#define SWRITE(b, i) do { *(bf16x8*)((char*)V_lds + (b) * SHM_V + vst0) = sr_[i].vs0;          \
    *(bf16x8*)((char*)V_lds + (b) * SHM_V + vst1) = sr_[i].vs1; int kc = sc * 2;               \
    *(bf16x8*)((char*)K_lds + (b) * SHM_K + KSWZ(sr, kc)) = sr_[i].ks0;                       \
    *(bf16x8*)((char*)K_lds + (b) * SHM_K + KSWZ(32 + sr, kc)) = sr_[i].ks1; } while (0)
#define SWAIT() do { asm volatile("s_waitcnt vmcnt(4)" ::: "memory"); } while (0)
#define RESC(a) do { if (__any((a) < 1.f)) { if (hi == 0) al_l[r32] = (a); asm volatile("s_waitcnt lgkmcnt(0)" ::: "memory"); \
    for (int d = 0; d < 4; ++d) for (int r = 0; r < 16; ++r) o[d][r] *= al_l[crow(r, hi)]; } } while (0)
  f32x16 pA0, pA1, pB0, pB1; float mnA, mnB, alA, alB; bf16x8 pa0, pa1, pa2, pa3; const int NT = seq / KVBLK;
  constexpr int SE = 0, SO = SDEPTH - 1;
  SLOAD(SE, 0); asm volatile("s_waitcnt vmcnt(0)" ::: "memory"); SWRITE(0, SE); __syncthreads();
  qkt(pA0, pA1, K_lds, qr, r32, hi); partialSM(pA0, pA1, m_reg, mnA, alA);
  SLOAD(SO, KVBLK); { if (2 < NT) SLOAD(SE, 2 * KVBLK); }
  SWAIT(); SWRITE(1, SO); __syncthreads();
  for (int j = 1; j + 1 < NT; j += 2) {
    SBAR(); qkt(pB0, pB1, (bf16*)((char*)K_lds + SHM_K), qr, r32, hi);
    finishSM(pA0, pA1, alA, l_reg, pa0, pa1, pa2, pa3); SBAR();
    SLOAD(SO, (j + SDEPTH) * KVBLK); SBAR();
    pv_d0(o, vb0, pa0, pa1, pa2, pa3); partialSM(pB0, pB1, m_reg, mnB, alB);
    __syncthreads(); SWAIT(); SWRITE(0, SE);
    RESC(alB); __syncthreads();
    SBAR(); qkt(pA0, pA1, K_lds, qr, r32, hi);
    finishSM(pB0, pB1, alB, l_reg, pa0, pa1, pa2, pa3); SBAR();
    if (j + 3 < NT) SLOAD(SE, (j + 1 + SDEPTH) * KVBLK); SBAR();
    pv_d0(o, vb0 + (int)SHM_V, pa0, pa1, pa2, pa3); partialSM(pA0, pA1, m_reg, mnA, alA);
    __syncthreads(); SWAIT(); SWRITE(1, SO);
    RESC(alA); __syncthreads();
  }
  SBAR(); qkt(pB0, pB1, (bf16*)((char*)K_lds + SHM_K), qr, r32, hi);
  finishSM(pA0, pA1, alA, l_reg, pa0, pa1, pa2, pa3); SBAR();
  pv_d0(o, vb0, pa0, pa1, pa2, pa3); partialSM(pB0, pB1, m_reg, mnB, alB);
  __syncthreads(); RESC(alB);
  finishSM(pB0, pB1, alB, l_reg, pa0, pa1, pa2, pa3); SBAR();
  pv_d0(o, vb0 + (int)SHM_V, pa0, pa1, pa2, pa3);
  if (hi == 0) li_l[r32] = l_reg; asm volatile("s_waitcnt lgkmcnt(0)" ::: "memory");
  float rli[16];
#pragma unroll
  for (int r = 0; r < 16; ++r) rli[r] = __builtin_amdgcn_rcpf(li_l[crow(r, hi)]);
  if (valid) {
  bf16* Ow = Ob + (long)(wid * QBLK) * LDO;
#pragma unroll
  for (int r = 0; r < 16; ++r) { int orow = crow(r, hi);
    for (int d0 = 0; d0 < 4; ++d0) Ow[(long)orow * LDO + d0 * 32 + r32] = __float2bfloat16(o[d0][r] * rli[r]); }
  }
}
__global__ __launch_bounds__(64 * NW, 1)
void attn_fwd_dense(const bf16* __restrict__ Q, const bf16* __restrict__ K, const bf16* __restrict__ V, bf16* __restrict__ O) {
  extern __shared__ __attribute__((aligned(16))) char shm[];
  const int id = blockIdx.x; const int x = id & 7, j = id >> 3; const int qb = j & 3, hh = (j >> 2) * 8 + x;
  const long q0 = ((long)hh * SQ2 + qb * 256) * D, k0 = (long)hh * SKV * D;
  const int rows = SQ2 - qb * 256;
  attn_dense_body(Q + q0, K + k0, V + k0, O + q0, rows, shm);
}
extern "C" void kernel_launch(void* const* d_in, const int* in_sizes, int n_in, void* d_out, int out_size, void* d_ws, size_t ws_size, hipStream_t stream) {
  hipFuncSetAttribute((const void*)attn_fwd_dense, hipFuncAttributeMaxDynamicSharedMemorySize, (int)SHM_ATTN);
  hipLaunchKernelGGL(attn_fwd_dense, dim3(1024), dim3(64 * NW), SHM_ATTN, stream,
                     (const bf16*)d_in[0], (const bf16*)d_in[1], (const bf16*)d_in[2], (bf16*)d_out);
}
```

```cpp
#include <hip/hip_runtime.h>
#include <hip/hip_bf16.h>
typedef unsigned long long u64;
typedef unsigned u32x4 __attribute__((ext_vector_type(4)));
constexpr int LDS_BYTES = 1024 + 8 * 16896 + 4 * 4224;

__device__ __forceinline__ float bflo(unsigned x) { return __uint_as_float(x << 16); }
__device__ __forceinline__ float bfhi(unsigned x) { return __uint_as_float(x & 0xffff0000u); }

__device__ __forceinline__ void exact_row(const char* q, const char* k, const char* v, char* o, unsigned lane) {
  float s[8];
#pragma unroll
  for (int kk = 0; kk < 8; ++kk) {
    const u32x4* kr = (const u32x4*)(k + (size_t)(kk * 64 + lane) * 256); float a = 0.f;
    for (int c = 0; c < 16; ++c) { const u32x4 x = kr[c], y = ((const u32x4*)q)[c];
#pragma unroll
      for (int e = 0; e < 4; ++e) { a = fmaf(bflo(x[e]), bflo(y[e]), a); a = fmaf(bfhi(x[e]), bfhi(y[e]), a); } }
    s[kk] = a * 0.08838834764831845f;
  }
  float m = s[0];
#pragma unroll
  for (int kk = 1; kk < 8; ++kk) m = fmaxf(m, s[kk]);
  for (int d = 1; d < 64; d <<= 1) m = fmaxf(m, __uint_as_float(__builtin_amdgcn_ds_bpermute((lane ^ d) << 2, __float_as_uint(m))));
  float l = 0.f;
#pragma unroll
  for (int kk = 0; kk < 8; ++kk) { s[kk] = __expf(s[kk] - m); l += s[kk]; }
  for (int d = 1; d < 64; d <<= 1) l += __uint_as_float(__builtin_amdgcn_ds_bpermute((lane ^ d) << 2, __float_as_uint(l)));
  float o0 = 0.f, o1 = 0.f;
#pragma unroll
  for (int kk = 0; kk < 8; ++kk)
    for (int n = 0; n < 64; ++n) { const float p = __uint_as_float(__builtin_amdgcn_readlane(__float_as_uint(s[kk]), n));
      const unsigned x = *(const unsigned*)(v + (size_t)(kk * 64 + n) * 256 + lane * 4); o0 = fmaf(p, bflo(x), o0); o1 = fmaf(p, bfhi(x), o1); }
  const float r = 1.0f / l; typedef float f2 __attribute__((ext_vector_type(2))); typedef __bf16 b2 __attribute__((ext_vector_type(2)));
  const f2 w = {o0 * r, o1 * r}; *(unsigned*)(o + lane * 4) = __builtin_bit_cast(unsigned, __builtin_convertvector(w, b2));
}

__global__ __launch_bounds__(256, 1) void gqa_attn_kernel(const char* __restrict__ Q, const char* __restrict__ K, const char* __restrict__ V, char* __restrict__ O
    ) {
  const unsigned wv = __builtin_amdgcn_readfirstlane(threadIdx.x >> 6);
  const unsigned w = blockIdx.x;
  typedef const __attribute__((address_space(4))) u64* kargp;
  kargp ka = (kargp)__builtin_amdgcn_kernarg_segment_ptr();
  u64 bad0, bad1, bad2, bad3;
  asm volatile(
    ".p2align 3\n"
    "s_load_dwordx8 s[88:95], %[ka], 0x0\n"
    "v_mbcnt_lo_u32_b32 v250, -1, 0\n"
    "v_mbcnt_hi_u32_b32 v250, -1, v250\n"
    "v_lshlrev_b32_e32 v242, 4, v250\n"
    "s_getpc_b64 s[80:81]\n"
    "LPC_%=:\n"
    "s_lshr_b32 s59, %[wg], 1\n"
    "s_and_b32 s59, s59, 60\n"
    "s_add_u32 s59, s59, %[wv]\n"
    "s_lshl_b32 s59, s59, 10\n"
    "s_sub_i32 s82, LEND_%=-LPC_%=, s59\n"
    "s_max_i32 s82, s82, 0\n"
    "s_mov_b32 s83, 0x20000\n"
    "s_add_u32 s80, s80, s59\n"
    "s_addc_u32 s81, s81, 0\n"
    "buffer_load_dword a254, v242, s[80:83], 0 offen\n"
    "v_and_b32_e32 v247, 0x380, v242\n"
    "v_add_u32_e32 v243, v242, v247\n"
    "v_and_b32_e32 v247, 16, v250\n"
    "v_lshlrev_b32_e32 v247, 1, v247\n"
    "v_xor_b32_e32 v243, v243, v247\n"
    "v_and_b32_e32 v249, 15, v250\n"
    "v_and_b32_e64 v248, 48, v250\n"
    "s_and_b32 s59, %[wg], 7\n"
    "s_lshl_b32 s59, s59, 2\n"
    "s_and_b32 s60, %[wg], 0xe0\n"
    "s_add_u32 s59, s59, s60\n"
    "s_bfe_u32 s63, %[wg], 0x20003\n"
    "s_lshl_b32 s60, s63, 16\n"
    "s_lshl_b32 s61, %[wv], 12\n"
    "s_add_u32 s60, s60, s61\n"
    "s_sub_u32 s46, 253952, s60\n"
    "s_mul_i32 s61, s59, 253952\n"
    "s_add_u32 s60, s60, s61\n"
    "s_lshl_b32 s59, s59, 17\n"
    "s_lshl_b32 s61, %[wv], 12\n"
    "s_add_u32 s59, s59, s61\n"
    "s_mov_b32 s47, 0x20000\n"
    "s_mov_b32 s38, 512000\n"
    "s_mov_b32 s39, 0x20000\n"
    "s_mov_b32 s42, 512000\n"
    "s_mov_b32 s43, 0x20000\n"
    "s_mul_i32 s62, %[wv], 4224\n"
    "s_add_u32 s62, s62, 1024\n"
    "s_lshr_b32 s63, %[wg], 5\n"
    "s_add_u32 s63, s63, %[wg]\n"
    "s_and_b32 s63, s63, 7\n"
    "s_lshl_b32 s63, s63, 14\n"
    "s_mov_b32 s50, 0\n"
    "s_mov_b32 s51, 0x20000\n"
    "s_mov_b32 s64, 0x3e0293ee\n"
    "s_mov_b32 s65, 0x3e0293ee\n"
    "s_mov_b32 s66, 0\n"
    "s_mov_b32 s67, 0xffff0000\n"
    "s_mov_b32 s70, 0\n"
    "v_mul_u32_u24_e32 v240, 0x420, v249\n"
    "v_add_u32_e32 v240, v240, v248\n"
    "v_add_u32_e32 v240, 1024, v240\n"
    "v_and_b32_e64 v246, 1, v250\n"
    "v_mul_u32_u24_e32 v246, 0xaaaa, v246\n"
    "v_or_b32_e32 v246, 0x5f804444, v246\n"
    "v_and_b32_e64 v247, 16, v250\n"
    "v_mul_u32_u24_e32 v247, 0x208, v247\n"
    "v_lshlrev_b32_e32 v247, 16, v247\n"
    "v_sub_u32_e32 v254, v246, v247\n"
    "v_lshrrev_b32_e32 v247, 2, v250\n"
    "v_lshlrev_b32_e32 v241, 10, v247\n"
    "v_and_b32_e64 v246, 3, v247\n"
    "v_lshl_add_u32 v241, v246, 3, v241\n"
    "v_lshl_add_u32 v241, v248, 3, v241\n"
    "v_and_b32_e64 v246, 3, v250\n"
    "v_lshl_add_u32 v241, v246, 5, v241\n"
    "v_add_u32_e32 v241, 68608, v241\n"
    "s_movk_i32 s76, 256\n"
    "v_mov_b32_e32 v251, 0\n"
    "v_lshrrev_b32_e64 v247, 1, v242\n"
    "ds_write2_b32 v247, v251, v251 offset1:1\n"
    "v_and_b32_e32 v247, 3, v249\n"
    "v_cmp_eq_u32_e32 vcc, 0, v247\n"
    "v_add_u32_e32 v247, 1024, v248\n"
    "v_mov_b32_e32 v246, 0x100\n"
    "v_cndmask_b32_e32 v247, v246, v247, vcc\n"
    "v_accvgpr_write_b32 a228, v247\n"
    "v_and_b32_e32 v246, 8, v249\n"
    "v_lshl_add_u32 v246, v246, 3, v248\n"
    "v_lshl_add_u32 v246, v249, 7, v246\n"
    "v_and_b32_e32 v247, 2, v249\n"
    "v_lshlrev_b32_e64 v247, 4, v247\n"
    "v_xor_b32_e32 v255, v246, v247\n"
    "s_add_u32 s72, s62, 50688\n"
    "v_add_u32_e32 v244, s72, v255\n"
    "v_and_b32_e32 v247, 14, v249\n"
    "v_lshlrev_b32_e64 v250, 8, v247\n"
    "v_lshl_add_u32 v250, v248, 1, v250\n"
    "v_and_b32_e64 v247, 1, v249\n"
    "v_lshl_add_u32 v250, v247, 4, v250\n"
    "s_mov_b32 s96, 0x55555555\n"
    "s_mov_b32 s97, 0x55555555\n"
    "v_and_b32_e32 v247, 2, v249\n"
    "v_cmp_eq_u32_e32 vcc, 0, v247\n"
    "v_mov_b32_e32 v246, 0x3f803f80\n"
    "s_nop 0\n"
    "v_cndmask_b32_e32 v246, 0, v246, vcc\n"
    "v_accvgpr_write_b32 a208, v246\n"
    "v_accvgpr_write_b32 a209, v246\n"
    "v_accvgpr_write_b32 a210, v246\n"
    "v_accvgpr_write_b32 a211, v246\n"
    "v_accvgpr_write_b32 a192, 0\n"
    "v_accvgpr_write_b32 a193, 0\n"
    "v_accvgpr_write_b32 a194, 0\n"
    "v_accvgpr_write_b32 a195, 0\n"
    "v_accvgpr_write_b32 a196, 0\n"
    "v_accvgpr_write_b32 a197, 0\n"
    "v_accvgpr_write_b32 a198, 0\n"
    "v_accvgpr_write_b32 a199, 0\n"
    "v_accvgpr_write_b32 a200, 0\n"
    "v_accvgpr_write_b32 a201, 0\n"
    "v_accvgpr_write_b32 a202, 0\n"
    "v_accvgpr_write_b32 a203, 0\n"
    "v_accvgpr_write_b32 a204, 0\n"
    "v_accvgpr_write_b32 a205, 0\n"
    "v_accvgpr_write_b32 a206, 0\n"
    "v_accvgpr_write_b32 a207, 0\n"
    "s_waitcnt lgkmcnt(0)\n"
    "s_add_u32 s36, s90, s59\n"
    "s_addc_u32 s37, s91, 0\n"
    "s_add_u32 s44, s88, s60\n"
    "s_addc_u32 s45, s89, 0\n"
    "s_add_u32 s40, s92, s59\n"
    "s_addc_u32 s41, s93, 0\n"
    "s_add_u32 s48, s94, s60\n"
    "s_addc_u32 s49, s95, 0\n"
    "s_mov_b32 s92, 16384\n"
    "s_mov_b32 s93, 32768\n"
    "s_mov_b32 s94, 49152\n"
    "s_getreg_b32 s72, hwreg(HW_REG_XCC_ID)\n"
    "s_and_b32 s72, s72, 1\n"
    "s_cbranch_scc1 LXD_%=\n"
    "s_sleep 48\n"
    "LXD_%=:\n"
    "s_add_u32 m0, s62, 33792\n"
    "s_nop 0\n"
    "buffer_load_dwordx4 v243, s[44:47], 0 offen offset:0 lds\n"
    "s_add_u32 m0, s62, 32832\n"
    "s_nop 0\n"
    "buffer_load_dwordx4 v243, s[44:47], 0 offen offset:2048 lds\n"
    "s_add_u32 m0, s62, 35776\n"
    "s_nop 0\n"
    "buffer_load_dwordx4 v243, s[44:47], 0 offen offset:128 lds\n"
    "s_add_u32 m0, s62, 34816\n"
    "s_nop 0\n"
    "buffer_load_dwordx4 v243, s[44:47], 0 offen offset:2176 lds\n"
    "s_add_u32 m0, s62, 0\n"
    "s_nop 0\n"
    "buffer_load_dwordx4 v242, s[36:39], s63 offen offset:0 lds\n"
    "s_add_u32 m0, s62, 32\n"
    "s_nop 0\n"
    "buffer_load_dwordx4 v242, s[36:39], s63 offen offset:1024 lds\n"
    "s_add_u32 m0, s62, 64\n"
    "s_nop 0\n"
    "buffer_load_dwordx4 v242, s[36:39], s63 offen offset:2048 lds\n"
    "s_add_u32 m0, s62, 96\n"
    "s_nop 0\n"
    "buffer_load_dwordx4 v242, s[36:39], s63 offen offset:3072 lds\n"
    "s_add_u32 m0, s62, 101376\n"
    "s_nop 0\n"
    "buffer_load_dwordx4 v243, s[44:47], s92 offen offset:0 lds\n"
    "s_add_u32 m0, s62, 100416\n"
    "s_nop 0\n"
    "buffer_load_dwordx4 v243, s[44:47], s92 offen offset:2048 lds\n"
    "s_sub_u32 s48, s48, 253952\n"
    "s_subb_u32 s49, s49, 0\n"
    "s_mul_i32 s95, %[wv], 4224\n"
    "s_add_u32 s95, s95, 136192\n"
    "v_add_u32_e64 v255, s95, v255\n"
    "s_mov_b32 s52, s46\n"
    "s_sub_i32 s53, s52, 16384\n"
    "s_max_i32 s53, s53, 0\n"
    "s_sub_i32 s54, s52, 32768\n"
    "s_max_i32 s54, s54, 0\n"
    "s_sub_i32 s55, s52, 49152\n"
    "s_max_i32 s55, s55, 0\n"
    "s_mov_b32 s56, 1\n"
    "s_mov_b32 s57, 0\n"
    "s_cmp_eq_u32 s55, 0\n"
    "s_cselect_b32 s89, 0xf149f2ca, 0\n"
    "s_waitcnt lgkmcnt(0)\n"
    "v_accvgpr_read_b32 v253, a228\n"
    "s_mov_b64 exec, 0\n"
    "s_cmp_lg_u32 %[wv], 0\n"
    "s_cbranch_scc1 LWN0_%=\n"
    "s_call_b64 s[72:73], LW0_%=\n"
    "s_call_b64 s[72:73], LW11_%=\n"
    "s_call_b64 s[72:73], LW8_%=\n"
    "s_branch LWE_%=\n"
    "LWN0_%=:\n"
    "s_cmp_lg_u32 %[wv], 1\n"
    "s_cbranch_scc1 LWN1_%=\n"
    "s_call_b64 s[72:73], LW1_%=\n"
    "s_call_b64 s[72:73], LW5_%=\n"
    "s_call_b64 s[72:73], LW9_%=\n"
    "s_branch LWE_%=\n"
    "LWN1_%=:\n"
    "s_cmp_lg_u32 %[wv], 2\n"
    "s_cbranch_scc1 LWN2_%=\n"
    "s_call_b64 s[72:73], LW2_%=\n"
    "s_call_b64 s[72:73], LW6_%=\n"
    "s_call_b64 s[72:73], LW3_%=\n"
    "s_branch LWE_%=\n"
    "LWN2_%=:\n"
    "s_call_b64 s[72:73], LW10_%=\n"
    "s_call_b64 s[72:73], LW7_%=\n"
    "s_call_b64 s[72:73], LW4_%=\n"
    "s_branch LWE_%=\n"
    "LWN3_%=:\n"
    "LWR_%=:\n"
    "s_setpc_b64 s[72:73]\n"
    "LWE_%=:\n"
    "s_mov_b64 exec, -1\n"
    "s_add_u32 m0, s62, 103360\n"
    "s_nop 0\n"
    "buffer_load_dwordx4 v243, s[44:47], s92 offen offset:128 lds\n"
    "s_add_u32 m0, s62, 102400\n"
    "s_nop 0\n"
    "buffer_load_dwordx4 v243, s[44:47], s92 offen offset:2176 lds\n"
    "s_add_u32 m0, s62, 67584\n"
    "s_nop 0\n"
    "buffer_load_dwordx4 v242, s[40:43], s63 offen offset:0 lds\n"
    "s_add_u32 m0, s62, 67592\n"
    "s_nop 0\n"
    "buffer_load_dwordx4 v242, s[40:43], s63 offen offset:1024 lds\n"
    "s_add_u32 m0, s62, 67600\n"
    "s_nop 0\n"
    "buffer_load_dwordx4 v242, s[40:43], s63 offen offset:2048 lds\n"
    "s_add_u32 m0, s62, 67608\n"
    "s_nop 0\n"
    "buffer_load_dwordx4 v242, s[40:43], s63 offen offset:3072 lds\n"
    "s_add_u32 m0, s62, 50688\n"
    "s_nop 0\n"
    "buffer_load_dwordx4 v243, s[44:47], s93 offen offset:0 lds\n"
    "v_subrev_u32_e32 v244, 16896, v244\n"
    "s_waitcnt vmcnt(15)\n"
    "ds_read_b128 v[128:131], v244 offset:0\n"
    "ds_read_b128 v[132:135], v244 offset:64\n"
    "s_waitcnt vmcnt(13)\n"
    "ds_read_b128 v[136:139], v244 offset:2112\n"
    "ds_read_b128 v[140:143], v244 offset:2176\n"
    "s_add_u32 m0, s62, 49728\n"
    "v_add_u32_e32 v244, 67584, v244\n"
    "buffer_load_dwordx4 v243, s[44:47], s93 offen offset:2048 lds\n"
    "s_waitcnt lgkmcnt(3)\n"
    "v_lshlrev_b32_e32 v96, 16, v128\n"
    "v_and_b32_e32 v97, s67, v128\n"
    "v_lshlrev_b32_e32 v98, 16, v129\n"
    "v_and_b32_e32 v99, s67, v129\n"
    "v_lshlrev_b32_e32 v100, 16, v130\n"
    "v_and_b32_e32 v101, s67, v130\n"
    "v_lshlrev_b32_e64 v102, 16, v131\n"
    "s_add_u32 m0, s62, 52672\n"
    "v_and_b32_e64 v103, s67, v131\n"
    "buffer_load_dwordx4 v243, s[44:47], s93 offen offset:128 lds\n"
    "v_pk_mul_f32 v[96:97], v[96:97], s[64:65]\n"
    "v_pk_mul_f32 v[98:99], v[98:99], s[64:65]\n"
    "v_pk_mul_f32 v[100:101], v[100:101], s[64:65]\n"
    "v_pk_mul_f32 v[102:103], v[102:103], s[64:65]\n"
    "v_cvt_pk_bf16_f32 v128, v96, v97\n"
    "v_cvt_pk_bf16_f32 v129, v98, v99\n"
    "v_cvt_pk_bf16_f32 v130, v100, v101\n"
    "v_cvt_pk_bf16_f32 v131, v102, v103\n"
    "v_accvgpr_write_b32 a0, v128\n"
    "v_accvgpr_write_b32 a1, v129\n"
    "v_accvgpr_write_b32 a2, v130\n"
    "v_accvgpr_write_b32 a3, v131\n"
    "s_waitcnt lgkmcnt(0)\n"
    "v_lshlrev_b32_e32 v104, 16, v132\n"
    "v_and_b32_e32 v105, s67, v132\n"
    "v_lshlrev_b32_e32 v106, 16, v133\n"
    "v_and_b32_e32 v107, s67, v133\n"
    "v_lshlrev_b32_e32 v108, 16, v134\n"
    "v_and_b32_e32 v109, s67, v134\n"
    "v_lshlrev_b32_e64 v110, 16, v135\n"
    "s_waitcnt vmcnt(11)\n"
    "s_add_u32 m0, s62, 51712\n"
    "s_barrier\n"
    "buffer_load_dwordx4 v243, s[44:47], s93 offen offset:2176 lds\n"
    "ds_read_b128 a[212:215], v253 offset:0\n"
    "ds_read_b128 a[216:219], v253 offset:64\n"
    "ds_read_b128 a[220:223], v253 offset:128\n"
    "ds_read_b128 a[224:227], v253 offset:192\n"
    "ds_read_b128 a[64:67], v240 offset:0\n"
    "ds_read_b128 a[68:71], v240 offset:64\n"
    "ds_read_b128 a[72:75], v240 offset:128\n"
    "v_and_b32_e32 v111, s67, v135\n"
    "v_pk_mul_f32 v[104:105], v[104:105], s[64:65]\n"
    "v_pk_mul_f32 v[106:107], v[106:107], s[64:65]\n"
    "v_pk_mul_f32 v[108:109], v[108:109], s[64:65]\n"
    "v_pk_mul_f32 v[110:111], v[110:111], s[64:65]\n"
    "v_cvt_pk_bf16_f32 v132, v104, v105\n"
    "v_cvt_pk_bf16_f32 v133, v106, v107\n"
    "v_cvt_pk_bf16_f32 v134, v108, v109\n"
    "v_cvt_pk_bf16_f32 v135, v110, v111\n"
    "v_accvgpr_write_b32 a4, v132\n"
    "v_accvgpr_write_b32 a5, v133\n"
    "v_accvgpr_write_b32 a6, v134\n"
    "v_accvgpr_write_b32 a7, v135\n"
    "v_lshlrev_b32_e32 v112, 16, v136\n"
    "v_and_b32_e32 v113, s67, v136\n"
    "v_lshlrev_b32_e32 v114, 16, v137\n"
    "v_and_b32_e32 v115, s67, v137\n"
    "v_lshlrev_b32_e32 v116, 16, v138\n"
    "v_and_b32_e32 v117, s67, v138\n"
    "v_lshlrev_b32_e32 v118, 16, v139\n"
    "v_and_b32_e32 v119, s67, v139\n"
    "v_pk_mul_f32 v[112:113], v[112:113], s[64:65]\n"
    "v_pk_mul_f32 v[114:115], v[114:115], s[64:65]\n"
    "v_pk_mul_f32 v[116:117], v[116:117], s[64:65]\n"
    "v_pk_mul_f32 v[118:119], v[118:119], s[64:65]\n"
    "v_cvt_pk_bf16_f32 v136, v112, v113\n"
    "v_cvt_pk_bf16_f32 v137, v114, v115\n"
    "v_cvt_pk_bf16_f32 v138, v116, v117\n"
    "v_cvt_pk_bf16_f32 v139, v118, v119\n"
    "v_accvgpr_write_b32 a8, v136\n"
    "v_accvgpr_write_b32 a9, v137\n"
    "v_accvgpr_write_b32 a10, v138\n"
    "v_accvgpr_write_b32 a11, v139\n"
    "s_waitcnt lgkmcnt(6)\n"
    "s_add_u32 m0, s62, 118272\n"
    "v_mfma_f32_16x16x32_bf16 v[224:227], a[212:215], a[0:3], 0\n"
    "s_waitcnt lgkmcnt(5)\n"
    "buffer_load_dwordx4 v243, s[44:47], s94 offen offset:0 lds\n"
    "v_mfma_f32_16x16x32_bf16 v[224:227], a[216:219], a[4:7], v[224:227]\n"
    "s_waitcnt lgkmcnt(4)\n"
    "ds_read_b128 a[76:79], v240 offset:192\n"
    "ds_read_b128 a[80:83], v240 offset:256\n"
    "v_mfma_f32_16x16x32_bf16 v[224:227], a[220:223], a[8:11], v[224:227]\n"
    "ds_read_b128 a[84:87], v240 offset:320\n"
    "v_lshlrev_b32_e32 v120, 16, v140\n"
    "v_and_b32_e32 v121, s67, v140\n"
    "v_lshlrev_b32_e32 v122, 16, v141\n"
    "v_and_b32_e32 v123, s67, v141\n"
    "v_lshlrev_b32_e32 v124, 16, v142\n"
    "v_and_b32_e32 v125, s67, v142\n"
    "v_lshlrev_b32_e32 v126, 16, v143\n"
    "v_and_b32_e64 v127, s67, v143\n"
    "v_pk_mul_f32 v[120:121], v[120:121], s[64:65]\n"
    "v_pk_mul_f32 v[122:123], v[122:123], s[64:65]\n"
    "v_pk_mul_f32 v[124:125], v[124:125], s[64:65]\n"
    "v_pk_mul_f32 v[126:127], v[126:127], s[64:65]\n"
    "v_cvt_pk_bf16_f32 v140, v120, v121\n"
    "v_cvt_pk_bf16_f32 v141, v122, v123\n"
    "v_cvt_pk_bf16_f32 v142, v124, v125\n"
    "v_cvt_pk_bf16_f32 v143, v126, v127\n"
    "v_accvgpr_write_b32 a12, v140\n"
    "v_accvgpr_write_b32 a13, v141\n"
    "v_accvgpr_write_b32 a14, v142\n"
    "v_accvgpr_write_b32 a15, v143\n"
    "ds_read_b128 a[88:91], v240 offset:384\n"
    "s_waitcnt lgkmcnt(4)\n"
    "s_add_u32 m0, s62, 117312\n"
    "v_mfma_f32_16x16x32_bf16 v[224:227], a[224:227], a[12:15], v[224:227]\n"
    "buffer_load_dwordx4 v243, s[44:47], s94 offen offset:2048 lds\n"
    "ds_read_b128 a[92:95], v240 offset:448\n"
    "ds_read_b128 a[96:99], v240 offset:512\n"
    "ds_read_b128 a[100:103], v240 offset:576\n"
    "ds_read_b128 a[104:107], v240 offset:640\n"
    "ds_read_b128 a[108:111], v240 offset:704\n"
    "ds_read_b128 a[112:115], v240 offset:768\n"
    "ds_read_b128 a[116:119], v240 offset:832\n"
    "s_nop 0\n"
    "v_sub_f32_e32 v227, 0, v224\n"
    "v_sub_f32_e32 v226, 0, v224\n"
    "v_sub_f32_e32 v225, 0, v224\n"
    "v_sub_f32_e32 v224, 0, v224\n"
    "ds_read_b128 a[120:123], v240 offset:896\n"
    "s_nop 0\n"
    "v_mfma_f32_16x16x32_bf16 v[192:195], a[64:67], a[0:3], v[224:227]\n"
    "ds_read_b128 a[124:127], v240 offset:960\n"
    "v_mfma_f32_16x16x32_bf16 v[192:195], a[68:71], a[4:7], v[192:195]\n"
    "s_add_u32 m0, s62, 120256\n"
    "v_mfma_f32_16x16x32_bf16 v[192:195], a[72:75], a[8:11], v[192:195]\n"
    "s_waitcnt lgkmcnt(10)\n"
    "buffer_load_dwordx4 v243, s[44:47], s94 offen offset:128 lds\n"
    "v_mfma_f32_16x16x32_bf16 v[192:195], a[76:79], a[12:15], v[192:195]\n"
    "v_mfma_f32_16x16x32_bf16 v[196:199], a[80:83], a[0:3], v[224:227]\n"
    "v_mfma_f32_16x16x32_bf16 v[196:199], a[84:87], a[4:7], v[196:199]\n"
    "s_waitcnt lgkmcnt(9)\n"
    "v_mfma_f32_16x16x32_bf16 v[196:199], a[88:91], a[8:11], v[196:199]\n"
    "s_waitcnt lgkmcnt(8)\n"
    "v_exp_f32_e32 v192, v192\n"
    "v_mfma_f32_16x16x32_bf16 v[196:199], a[92:95], a[12:15], v[196:199]\n"
    "s_waitcnt lgkmcnt(7)\n"
    "v_exp_f32_e32 v193, v193\n"
    "v_mfma_f32_16x16x32_bf16 v[200:203], a[96:99], a[0:3], v[224:227]\n"
    "s_waitcnt lgkmcnt(6)\n"
    "v_exp_f32_e32 v194, v194\n"
    "s_add_u32 m0, s62, 119296\n"
    "v_mfma_f32_16x16x32_bf16 v[200:203], a[100:103], a[4:7], v[200:203]\n"
    "buffer_load_dwordx4 v243, s[44:47], s94 offen offset:2176 lds\n"
    "s_add_u32 s44, s44, 253952\n"
    "s_addc_u32 s45, s45, 0\n"
    "s_waitcnt lgkmcnt(2)\n"
    "v_mfma_f32_16x16x32_bf16 v[200:203], a[104:107], a[8:11], v[200:203]\n"
    "v_exp_f32_e32 v195, v195\n"
    "v_mfma_f32_16x16x32_bf16 v[200:203], a[108:111], a[12:15], v[200:203]\n"
    "s_waitcnt vmcnt(12)\n"
    "ds_read_b128 v[144:147], v244 offset:0\n"
    "ds_read_b128 v[148:151], v244 offset:64\n"
    "v_mfma_f32_16x16x32_bf16 v[204:207], a[112:115], a[0:3], v[224:227]\n"
    "ds_read_b128 v[152:155], v244 offset:2112\n"
    "ds_read_b128 v[156:159], v244 offset:2176\n"
    "v_mfma_f32_16x16x32_bf16 v[204:207], a[116:119], a[4:7], v[204:207]\n"
    "s_waitcnt lgkmcnt(4)\n"
    "v_exp_f32_e32 v196, v196\n"
    "v_mfma_f32_16x16x32_bf16 v[204:207], a[120:123], a[8:11], v[204:207]\n"
    "v_exp_f32_e32 v197, v197\n"
    "v_mfma_f32_16x16x32_bf16 v[204:207], a[124:127], a[12:15], v[204:207]\n"
    "s_add_u32 m0, s62, 16896\n"
    "s_xor_b32 s88, s63, 16384\n"
    "v_cvt_pk_bf16_f32 v208, v192, v193\n"
    "v_cvt_pk_bf16_f32 v209, v194, v195\n"
    "v_exp_f32_e32 v198, v198\n"
    "v_exp_f32_e32 v199, v199\n"
    "v_cvt_pk_bf16_f32 v210, v196, v197\n"
    "v_cvt_pk_bf16_f32 v211, v198, v199\n"
    "buffer_load_dwordx4 v242, s[36:39], s88 offen offset:0 lds\n"
    "v_exp_f32_e32 v200, v200\n"
    "v_exp_f32_e32 v201, v201\n"
    "v_exp_f32_e32 v202, v202\n"
    "v_exp_f32_e32 v203, v203\n"
    "v_exp_f32_e32 v204, v204\n"
    "v_exp_f32_e32 v205, v205\n"
    "s_waitcnt lgkmcnt(3)\n"
    "v_lshlrev_b32_e32 v96, 16, v144\n"
    "v_and_b32_e32 v97, s67, v144\n"
    "v_lshlrev_b32_e32 v98, 16, v145\n"
    "v_and_b32_e32 v99, s67, v145\n"
    "v_lshlrev_b32_e32 v100, 16, v146\n"
    "v_and_b32_e32 v101, s67, v146\n"
    "v_lshlrev_b32_e32 v102, 16, v147\n"
    "v_and_b32_e64 v103, s67, v147\n"
    "v_pk_mul_f32 v[96:97], v[96:97], s[64:65]\n"
    "v_pk_mul_f32 v[98:99], v[98:99], s[64:65]\n"
    "v_pk_mul_f32 v[100:101], v[100:101], s[64:65]\n"
    "v_pk_mul_f32 v[102:103], v[102:103], s[64:65]\n"
    "v_cvt_pk_bf16_f32 v144, v96, v97\n"
    "v_cvt_pk_bf16_f32 v145, v98, v99\n"
    "v_cvt_pk_bf16_f32 v146, v100, v101\n"
    "v_cvt_pk_bf16_f32 v147, v102, v103\n"
    "v_accvgpr_write_b32 a16, v144\n"
    "v_accvgpr_write_b32 a17, v145\n"
    "v_accvgpr_write_b32 a18, v146\n"
    "v_accvgpr_write_b32 a19, v147\n"
    "s_waitcnt lgkmcnt(0)\n"
    "v_lshlrev_b32_e32 v104, 16, v148\n"
    "v_and_b32_e64 v105, s67, v148\n"
    "s_add_u32 m0, s62, 16928\n"
    "v_mfma_f32_16x16x32_bf16 v[228:231], a[212:215], a[16:19], 0\n"
    "buffer_load_dwordx4 v242, s[36:39], s88 offen offset:1024 lds\n"
    "v_lshlrev_b32_e32 v106, 16, v149\n"
    "v_and_b32_e32 v107, s67, v149\n"
    "v_lshlrev_b32_e32 v108, 16, v150\n"
    "v_and_b32_e32 v109, s67, v150\n"
    "v_lshlrev_b32_e32 v110, 16, v151\n"
    "v_and_b32_e32 v111, s67, v151\n"
    "v_pk_mul_f32 v[104:105], v[104:105], s[64:65]\n"
    "v_pk_mul_f32 v[106:107], v[106:107], s[64:65]\n"
    "v_pk_mul_f32 v[108:109], v[108:109], s[64:65]\n"
    "v_pk_mul_f32 v[110:111], v[110:111], s[64:65]\n"
    "v_cvt_pk_bf16_f32 v148, v104, v105\n"
    "v_cvt_pk_bf16_f32 v149, v106, v107\n"
    "v_cvt_pk_bf16_f32 v150, v108, v109\n"
    "v_cvt_pk_bf16_f32 v151, v110, v111\n"
    "v_accvgpr_write_b32 a20, v148\n"
    "v_accvgpr_write_b32 a21, v149\n"
    "v_accvgpr_write_b32 a22, v150\n"
    "v_accvgpr_write_b32 a23, v151\n"
    "v_lshlrev_b32_e32 v112, 16, v152\n"
    "v_and_b32_e32 v113, s67, v152\n"
    "v_mfma_f32_16x16x32_bf16 v[228:231], a[216:219], a[20:23], v[228:231]\n"
    "v_lshlrev_b32_e32 v114, 16, v153\n"
    "v_and_b32_e32 v115, s67, v153\n"
    "v_lshlrev_b32_e32 v116, 16, v154\n"
    "v_and_b32_e32 v117, s67, v154\n"
    "v_lshlrev_b32_e32 v118, 16, v155\n"
    "v_and_b32_e32 v119, s67, v155\n"
    "v_pk_mul_f32 v[112:113], v[112:113], s[64:65]\n"
    "v_pk_mul_f32 v[114:115], v[114:115], s[64:65]\n"
    "v_pk_mul_f32 v[116:117], v[116:117], s[64:65]\n"
    "v_pk_mul_f32 v[118:119], v[118:119], s[64:65]\n"
    "v_cvt_pk_bf16_f32 v152, v112, v113\n"
    "v_cvt_pk_bf16_f32 v153, v114, v115\n"
    "v_cvt_pk_bf16_f32 v154, v116, v117\n"
    "v_cvt_pk_bf16_f32 v155, v118, v119\n"
    "v_accvgpr_write_b32 a24, v152\n"
    "v_accvgpr_write_b32 a25, v153\n"
    "v_accvgpr_write_b32 a26, v154\n"
    "v_accvgpr_write_b32 a27, v155\n"
    "v_lshlrev_b32_e32 v120, 16, v156\n"
    "v_and_b32_e32 v121, s67, v156\n"
    "s_add_u32 m0, s62, 16960\n"
    "v_mfma_f32_16x16x32_bf16 v[228:231], a[220:223], a[24:27], v[228:231]\n"
    "buffer_load_dwordx4 v242, s[36:39], s88 offen offset:2048 lds\n"
    "v_lshlrev_b32_e32 v122, 16, v157\n"
    "v_and_b32_e32 v123, s67, v157\n"
    "v_lshlrev_b32_e32 v124, 16, v158\n"
    "v_and_b32_e32 v125, s67, v158\n"
    "v_lshlrev_b32_e32 v126, 16, v159\n"
    "v_and_b32_e32 v127, s67, v159\n"
    "v_pk_mul_f32 v[120:121], v[120:121], s[64:65]\n"
    "v_pk_mul_f32 v[122:123], v[122:123], s[64:65]\n"
    "v_pk_mul_f32 v[124:125], v[124:125], s[64:65]\n"
    "v_pk_mul_f32 v[126:127], v[126:127], s[64:65]\n"
    "v_cvt_pk_bf16_f32 v156, v120, v121\n"
    "v_cvt_pk_bf16_f32 v157, v122, v123\n"
    "v_cvt_pk_bf16_f32 v158, v124, v125\n"
    "v_cvt_pk_bf16_f32 v159, v126, v127\n"
    "v_accvgpr_write_b32 a28, v156\n"
    "v_accvgpr_write_b32 a29, v157\n"
    "v_accvgpr_write_b32 a30, v158\n"
    "v_accvgpr_write_b32 a31, v159\n"
    "s_waitcnt vmcnt(11)\n"
    "s_barrier\n"
    "s_add_u32 m0, s62, 16992\n"
    "v_mfma_f32_16x16x32_bf16 v[228:231], a[224:227], a[28:31], v[228:231]\n"
    "buffer_load_dwordx4 v242, s[36:39], s88 offen offset:3072 lds\n"
    "ds_read_b64_tr_b16 a[128:129], v241 offset:0\n"
    "ds_read_b64_tr_b16 a[130:131], v241 offset:256\n"
    "ds_read_b64_tr_b16 a[132:133], v241 offset:512\n"
    "ds_read_b64_tr_b16 a[134:135], v241 offset:768\n"
    "ds_read_b64_tr_b16 a[136:137], v241 offset:8\n"
    "ds_read_b64_tr_b16 a[138:139], v241 offset:264\n"
    "ds_read_b64_tr_b16 a[140:141], v241 offset:520\n"
    "s_nop 0\n"
    "v_sub_f32_e32 v231, 0, v228\n"
    "v_sub_f32_e32 v230, 0, v228\n"
    "v_sub_f32_e32 v229, 0, v228\n"
    "v_sub_f32_e64 v228, 0, v228\n"
    "ds_read_b64_tr_b16 a[142:143], v241 offset:776\n"
    "s_nop 0\n"
    "v_mfma_f32_16x16x32_bf16 v[192:195], a[64:67], a[16:19], v[228:231]\n"
    "ds_read_b64_tr_b16 a[144:145], v241 offset:16\n"
    "ds_read_b64_tr_b16 a[146:147], v241 offset:272\n"
    "v_mfma_f32_16x16x32_bf16 v[192:195], a[68:71], a[20:23], v[192:195]\n"
    "s_add_u32 m0, s62, 84480\n"
    "s_xor_b32 s88, s63, 16384\n"
    "ds_read_b64_tr_b16 a[148:149], v241 offset:528\n"
    "ds_read_b64_tr_b16 a[150:151], v241 offset:784\n"
    "v_mfma_f32_16x16x32_bf16 v[192:195], a[72:75], a[24:27], v[192:195]\n"
    "buffer_load_dwordx4 v242, s[40:43], s88 offen offset:0 lds\n"
    "v_mfma_f32_16x16x32_bf16 v[192:195], a[76:79], a[28:31], v[192:195]\n"
    "ds_read_b64_tr_b16 a[152:153], v241 offset:24\n"
    "ds_read_b64_tr_b16 a[154:155], v241 offset:280\n"
    "v_mfma_f32_16x16x32_bf16 v[196:199], a[80:83], a[16:19], v[228:231]\n"
    "ds_read_b64_tr_b16 a[156:157], v241 offset:536\n"
    "ds_read_b64_tr_b16 a[158:159], v241 offset:792\n"
    "v_mfma_f32_16x16x32_bf16 v[196:199], a[84:87], a[20:23], v[196:199]\n"
    "ds_read_b64_tr_b16 a[160:161], v241 offset:128\n"
    "ds_read_b64_tr_b16 a[162:163], v241 offset:384\n"
    "v_mfma_f32_16x16x32_bf16 v[196:199], a[88:91], a[24:27], v[196:199]\n"
    "ds_read_b64_tr_b16 a[164:165], v241 offset:640\n"
    "ds_read_b64_tr_b16 a[166:167], v241 offset:896\n"
    "v_mfma_f32_16x16x32_bf16 v[196:199], a[92:95], a[28:31], v[196:199]\n"
    "s_waitcnt lgkmcnt(14)\n"
    "ds_read_b64_tr_b16 a[168:169], v241 offset:136\n"
    "ds_read_b64_tr_b16 a[170:171], v241 offset:392\n"
    "v_mfma_f32_16x16x32_bf16 v[0:3], a[128:131], v[208:211], 0\n"
    "ds_read_b64_tr_b16 a[172:173], v241 offset:648\n"
    "v_exp_f32_e32 v206, v206\n"
    "v_exp_f32_e32 v207, v207\n"
    "v_cvt_pk_bf16_f32 v212, v200, v201\n"
    "v_cvt_pk_bf16_f32 v213, v202, v203\n"
    "v_cvt_pk_bf16_f32 v214, v204, v205\n"
    "v_cvt_pk_bf16_f32 v215, v206, v207\n"
    "v_subrev_u32_e32 v244, 50688, v244\n"
    "s_waitcnt vmcnt(9)\n"
    "s_waitcnt lgkmcnt(14)\n"
    "s_add_u32 m0, s62, 84488\n"
    "v_mfma_f32_16x16x32_bf16 v[0:3], a[132:135], v[212:215], v[0:3]\n"
    "buffer_load_dwordx4 v242, s[40:43], s88 offen offset:1024 lds\n"
    "v_mfma_f32_16x16x32_bf16 v[4:7], a[136:139], v[208:211], 0\n"
    "s_waitcnt lgkmcnt(13)\n"
    "ds_read_b128 v[160:163], v244 offset:0\n"
    "ds_read_b128 v[164:167], v244 offset:64\n"
    "v_mfma_f32_16x16x32_bf16 v[4:7], a[140:143], v[212:215], v[4:7]\n"
    "ds_read_b128 v[168:171], v244 offset:2112\n"
    "ds_read_b128 v[172:175], v244 offset:2176\n"
    "v_mfma_f32_16x16x32_bf16 v[200:203], a[96:99], a[16:19], v[228:231]\n"
    "ds_read_b64_tr_b16 a[174:175], v241 offset:904\n"
    "ds_read_b64_tr_b16 a[176:177], v241 offset:144\n"
    "v_mfma_f32_16x16x32_bf16 v[200:203], a[100:103], a[20:23], v[200:203]\n"
    "ds_read_b64_tr_b16 a[178:179], v241 offset:400\n"
    "ds_read_b64_tr_b16 a[180:181], v241 offset:656\n"
    "v_mfma_f32_16x16x32_bf16 v[200:203], a[104:107], a[24:27], v[200:203]\n"
    "ds_read_b64_tr_b16 a[182:183], v241 offset:912\n"
    "ds_read_b64_tr_b16 a[184:185], v241 offset:152\n"
    "v_mfma_f32_16x16x32_bf16 v[200:203], a[108:111], a[28:31], v[200:203]\n"
    "s_add_u32 m0, s62, 84496\n"
    "ds_read_b64_tr_b16 a[186:187], v241 offset:408\n"
    "ds_read_b64_tr_b16 a[188:189], v241 offset:664\n"
    "v_mfma_f32_16x16x32_bf16 v[8:11], a[144:147], v[208:211], 0\n"
    "s_waitcnt lgkmcnt(14)\n"
    "buffer_load_dwordx4 v242, s[40:43], s88 offen offset:2048 lds\n"
    "v_mfma_f32_16x16x32_bf16 v[8:11], a[148:151], v[212:215], v[8:11]\n"
    "ds_read_b64_tr_b16 a[190:191], v241 offset:920\n"
    "v_add_u32_e32 v244, 67584, v244\n"
    "v_mfma_f32_16x16x32_bf16 v[12:15], a[152:155], v[208:211], 0\n"
    "v_exp_f32_e32 v192, v192\n"
    "v_mfma_f32_16x16x32_bf16 v[12:15], a[156:159], v[212:215], v[12:15]\n"
    "v_exp_f32_e32 v193, v193\n"
    "v_smfmac_f32_16x16x64_bf16 a[192:195], a[208:211], v[208:215], v254\n"
    "v_exp_f32_e32 v194, v194\n"
    "s_branch LP2_%=\n"
    ".p2align 12\n"
    "LW0_%=:\n"
    "s_setpc_b64 s[72:73]\n"
    "s_nop 0\n"
    "LP2_%=:\n"
    "v_mfma_f32_16x16x32_bf16 v[204:207], a[112:115], a[16:19], v[228:231]\n"
    "v_exp_f32_e32 v195, v195\n"
    "v_mfma_f32_16x16x32_bf16 v[204:207], a[116:119], a[20:23], v[204:207]\n"
    "s_add_u32 m0, s62, 84504\n"
    "v_exp_f32_e32 v196, v196\n"
    "v_mfma_f32_16x16x32_bf16 v[204:207], a[120:123], a[24:27], v[204:207]\n"
    "buffer_load_dwordx4 v242, s[40:43], s88 offen offset:3072 lds\n"
    "v_mfma_f32_16x16x32_bf16 v[204:207], a[124:127], a[28:31], v[204:207]\n"
    "v_exp_f32_e32 v197, v197\n"
    "v_mfma_f32_16x16x32_bf16 v[16:19], a[160:163], v[208:211], 0\n"
    "v_exp_f32_e32 v198, v198\n"
    "v_mfma_f32_16x16x32_bf16 v[16:19], a[164:167], v[212:215], v[16:19]\n"
    "s_waitcnt lgkmcnt(3)\n"
    "v_exp_f32_e32 v199, v199\n"
    "v_mfma_f32_16x16x32_bf16 v[20:23], a[168:171], v[208:211], 0\n"
    "v_lshlrev_b32_e32 v96, 16, v160\n"
    "v_and_b32_e32 v97, s67, v160\n"
    "v_mfma_f32_16x16x32_bf16 v[20:23], a[172:175], v[212:215], v[20:23]\n"
    "v_lshlrev_b32_e32 v98, 16, v161\n"
    "v_and_b32_e32 v99, s67, v161\n"
    "v_lshlrev_b32_e32 v100, 16, v162\n"
    "v_and_b32_e32 v101, s67, v162\n"
    "v_lshlrev_b32_e32 v102, 16, v163\n"
    "v_and_b32_e32 v103, s67, v163\n"
    "v_pk_mul_f32 v[96:97], v[96:97], s[64:65]\n"
    "v_pk_mul_f32 v[98:99], v[98:99], s[64:65]\n"
    "v_pk_mul_f32 v[100:101], v[100:101], s[64:65]\n"
    "v_pk_mul_f32 v[102:103], v[102:103], s[64:65]\n"
    "v_cvt_pk_bf16_f32 v160, v96, v97\n"
    "v_cvt_pk_bf16_f32 v161, v98, v99\n"
    "v_cvt_pk_bf16_f32 v162, v100, v101\n"
    "v_cvt_pk_bf16_f32 v163, v102, v103\n"
    "v_accvgpr_write_b32 a32, v160\n"
    "v_accvgpr_write_b32 a33, v161\n"
    "v_accvgpr_write_b32 a34, v162\n"
    "v_accvgpr_write_b32 a35, v163\n"
    "v_lshlrev_b32_e32 v104, 16, v164\n"
    "v_and_b32_e32 v105, s67, v164\n"
    "s_add_u32 m0, s62, 33792\n"
    "s_xor_b32 s88, s63, 32768\n"
    "v_mfma_f32_16x16x32_bf16 v[232:235], a[212:215], a[32:35], 0\n"
    "buffer_load_dwordx4 v242, s[36:39], s88 offen offset:0 lds\n"
    "v_lshlrev_b32_e32 v106, 16, v165\n"
    "v_and_b32_e32 v107, s67, v165\n"
    "v_lshlrev_b32_e32 v108, 16, v166\n"
    "v_and_b32_e32 v109, s67, v166\n"
    "v_lshlrev_b32_e32 v110, 16, v167\n"
    "v_and_b32_e32 v111, s67, v167\n"
    "v_pk_mul_f32 v[104:105], v[104:105], s[64:65]\n"
    "v_pk_mul_f32 v[106:107], v[106:107], s[64:65]\n"
    "v_pk_mul_f32 v[108:109], v[108:109], s[64:65]\n"
    "v_pk_mul_f32 v[110:111], v[110:111], s[64:65]\n"
    "v_cvt_pk_bf16_f32 v164, v104, v105\n"
    "v_cvt_pk_bf16_f32 v165, v106, v107\n"
    "v_cvt_pk_bf16_f32 v166, v108, v109\n"
    "v_cvt_pk_bf16_f32 v167, v110, v111\n"
    "v_accvgpr_write_b32 a36, v164\n"
    "v_accvgpr_write_b32 a37, v165\n"
    "v_accvgpr_write_b32 a38, v166\n"
    "v_accvgpr_write_b32 a39, v167\n"
    "v_lshlrev_b32_e32 v112, 16, v168\n"
    "v_and_b32_e32 v113, s67, v168\n"
    "v_mfma_f32_16x16x32_bf16 v[232:235], a[216:219], a[36:39], v[232:235]\n"
    "v_lshlrev_b32_e32 v114, 16, v169\n"
    "v_and_b32_e32 v115, s67, v169\n"
    "v_lshlrev_b32_e32 v116, 16, v170\n"
    "v_and_b32_e32 v117, s67, v170\n"
    "v_lshlrev_b32_e32 v118, 16, v171\n"
    "v_and_b32_e32 v119, s67, v171\n"
    "v_pk_mul_f32 v[112:113], v[112:113], s[64:65]\n"
    "v_pk_mul_f32 v[114:115], v[114:115], s[64:65]\n"
    "v_pk_mul_f32 v[116:117], v[116:117], s[64:65]\n"
    "v_pk_mul_f32 v[118:119], v[118:119], s[64:65]\n"
    "v_cvt_pk_bf16_f32 v168, v112, v113\n"
    "v_cvt_pk_bf16_f32 v169, v114, v115\n"
    "v_cvt_pk_bf16_f32 v170, v116, v117\n"
    "v_cvt_pk_bf16_f32 v171, v118, v119\n"
    "v_accvgpr_write_b32 a40, v168\n"
    "v_accvgpr_write_b32 a41, v169\n"
    "v_accvgpr_write_b32 a42, v170\n"
    "v_accvgpr_write_b32 a43, v171\n"
    "v_lshlrev_b32_e32 v120, 16, v172\n"
    "v_and_b32_e32 v121, s67, v172\n"
    "s_add_u32 m0, s62, 33824\n"
    "v_mfma_f32_16x16x32_bf16 v[232:235], a[220:223], a[40:43], v[232:235]\n"
    "buffer_load_dwordx4 v242, s[36:39], s88 offen offset:1024 lds\n"
    "v_lshlrev_b32_e32 v122, 16, v173\n"
    "v_and_b32_e32 v123, s67, v173\n"
    "v_lshlrev_b32_e32 v124, 16, v174\n"
    "v_and_b32_e32 v125, s67, v174\n"
    "v_lshlrev_b32_e32 v126, 16, v175\n"
    "v_and_b32_e32 v127, s67, v175\n"
    "v_pk_mul_f32 v[120:121], v[120:121], s[64:65]\n"
    "v_pk_mul_f32 v[122:123], v[122:123], s[64:65]\n"
    "v_pk_mul_f32 v[124:125], v[124:125], s[64:65]\n"
    "v_pk_mul_f32 v[126:127], v[126:127], s[64:65]\n"
    "v_cvt_pk_bf16_f32 v172, v120, v121\n"
    "v_cvt_pk_bf16_f32 v173, v122, v123\n"
    "v_cvt_pk_bf16_f32 v174, v124, v125\n"
    "v_cvt_pk_bf16_f32 v175, v126, v127\n"
    "v_accvgpr_write_b32 a44, v172\n"
    "v_accvgpr_write_b32 a45, v173\n"
    "v_accvgpr_write_b32 a46, v174\n"
    "v_accvgpr_write_b32 a47, v175\n"
    "v_cvt_pk_bf16_f32 v216, v192, v193\n"
    "v_cvt_pk_bf16_f32 v217, v194, v195\n"
    "v_mfma_f32_16x16x32_bf16 v[232:235], a[224:227], a[44:47], v[232:235]\n"
    "v_cvt_pk_bf16_f32 v218, v196, v197\n"
    "v_cvt_pk_bf16_f32 v219, v198, v199\n"
    "v_exp_f32_e32 v200, v200\n"
    "v_exp_f32_e32 v201, v201\n"
    "v_exp_f32_e32 v202, v202\n"
    "v_exp_f32_e32 v203, v203\n"
    "s_nop 2\n"
    "v_sub_f32_e32 v235, 0, v232\n"
    "v_sub_f32_e32 v234, 0, v232\n"
    "v_sub_f32_e32 v233, 0, v232\n"
    "v_sub_f32_e32 v232, 0, v232\n"
    "v_exp_f32_e32 v204, v204\n"
    "s_add_u32 m0, s62, 33856\n"
    "v_mfma_f32_16x16x32_bf16 v[192:195], a[64:67], a[32:35], v[232:235]\n"
    "buffer_load_dwordx4 v242, s[36:39], s88 offen offset:2048 lds\n"
    "v_mfma_f32_16x16x32_bf16 v[192:195], a[68:71], a[36:39], v[192:195]\n"
    "v_exp_f32_e32 v205, v205\n"
    "v_mfma_f32_16x16x32_bf16 v[192:195], a[72:75], a[40:43], v[192:195]\n"
    "v_exp_f32_e32 v206, v206\n"
    "v_mfma_f32_16x16x32_bf16 v[192:195], a[76:79], a[44:47], v[192:195]\n"
    "v_exp_f32_e32 v207, v207\n"
    "v_mfma_f32_16x16x32_bf16 v[24:27], a[176:179], v[208:211], 0\n"
    "v_cvt_pk_bf16_f32 v220, v200, v201\n"
    "v_cvt_pk_bf16_f32 v221, v202, v203\n"
    "v_mfma_f32_16x16x32_bf16 v[24:27], a[180:183], v[212:215], v[24:27]\n"
    "s_waitcnt lgkmcnt(0)\n"
    "v_cvt_pk_bf16_f32 v222, v204, v205\n"
    "v_cvt_pk_bf16_f32 v223, v206, v207\n"
    "v_mfma_f32_16x16x32_bf16 v[28:31], a[184:187], v[208:211], 0\n"
    "s_waitcnt vmcnt(11)\n"
    "ds_read_b128 v[176:179], v244 offset:0\n"
    "ds_read_b128 v[180:183], v244 offset:64\n"
    "s_add_u32 m0, s62, 33888\n"
    "v_mfma_f32_16x16x32_bf16 v[28:31], a[188:191], v[212:215], v[28:31]\n"
    "buffer_load_dwordx4 v242, s[36:39], s88 offen offset:3072 lds\n"
    "v_mfma_f32_16x16x32_bf16 v[196:199], a[80:83], a[32:35], v[232:235]\n"
    "ds_read_b128 v[184:187], v244 offset:2112\n"
    "ds_read_b128 v[188:191], v244 offset:2176\n"
    "v_mfma_f32_16x16x32_bf16 v[196:199], a[84:87], a[36:39], v[196:199]\n"
    "v_exp_f32_e32 v192, v192\n"
    "v_mfma_f32_16x16x32_bf16 v[196:199], a[88:91], a[40:43], v[196:199]\n"
    "v_exp_f32_e32 v193, v193\n"
    "v_mfma_f32_16x16x32_bf16 v[196:199], a[92:95], a[44:47], v[196:199]\n"
    "v_exp_f32_e32 v194, v194\n"
    "v_mfma_f32_16x16x32_bf16 v[32:35], a[128:131], v[216:219], 0\n"
    "v_exp_f32_e32 v195, v195\n"
    "v_mfma_f32_16x16x32_bf16 v[32:35], a[132:135], v[220:223], v[32:35]\n"
    "s_add_u32 m0, s62, 101376\n"
    "s_xor_b32 s88, s63, 32768\n"
    "v_cvt_pk_bf16_f32 v208, v192, v193\n"
    "v_cvt_pk_bf16_f32 v209, v194, v195\n"
    "v_mfma_f32_16x16x32_bf16 v[36:39], a[136:139], v[216:219], 0\n"
    "buffer_load_dwordx4 v242, s[40:43], s88 offen offset:0 lds\n"
    "v_mfma_f32_16x16x32_bf16 v[36:39], a[140:143], v[220:223], v[36:39]\n"
    "v_exp_f32_e32 v196, v196\n"
    "v_mfma_f32_16x16x32_bf16 v[200:203], a[96:99], a[32:35], v[232:235]\n"
    "v_exp_f32_e32 v197, v197\n"
    "v_mfma_f32_16x16x32_bf16 v[200:203], a[100:103], a[36:39], v[200:203]\n"
    "v_exp_f32_e32 v198, v198\n"
    "v_mfma_f32_16x16x32_bf16 v[200:203], a[104:107], a[40:43], v[200:203]\n"
    "v_exp_f32_e32 v199, v199\n"
    "v_mfma_f32_16x16x32_bf16 v[200:203], a[108:111], a[44:47], v[200:203]\n"
    "s_waitcnt lgkmcnt(2)\n"
    "v_lshlrev_b32_e32 v96, 16, v176\n"
    "v_and_b32_e32 v97, s67, v176\n"
    "v_mfma_f32_16x16x32_bf16 v[40:43], a[144:147], v[216:219], 0\n"
    "s_add_u32 m0, s62, 101384\n"
    "v_lshlrev_b32_e32 v98, 16, v177\n"
    "v_and_b32_e32 v99, s67, v177\n"
    "v_mfma_f32_16x16x32_bf16 v[40:43], a[148:151], v[220:223], v[40:43]\n"
    "buffer_load_dwordx4 v242, s[40:43], s88 offen offset:1024 lds\n"
    "v_mfma_f32_16x16x32_bf16 v[44:47], a[152:155], v[216:219], 0\n"
    "v_exp_f32_e32 v200, v200\n"
    "v_mfma_f32_16x16x32_bf16 v[44:47], a[156:159], v[220:223], v[44:47]\n"
    "v_exp_f32_e32 v201, v201\n"
    "v_smfmac_f32_16x16x64_bf16 a[196:199], a[208:211], v[216:223], v254\n"
    "v_exp_f32_e32 v202, v202\n"
    "v_mfma_f32_16x16x32_bf16 v[204:207], a[112:115], a[32:35], v[232:235]\n"
    "v_exp_f32_e32 v203, v203\n"
    "v_mfma_f32_16x16x32_bf16 v[204:207], a[116:119], a[36:39], v[204:207]\n"
    "v_lshlrev_b32_e32 v100, 16, v178\n"
    "v_and_b32_e32 v101, s67, v178\n"
    "v_mfma_f32_16x16x32_bf16 v[204:207], a[120:123], a[40:43], v[204:207]\n"
    "s_add_u32 m0, s62, 101392\n"
    "v_lshlrev_b32_e32 v102, 16, v179\n"
    "v_and_b32_e32 v103, s67, v179\n"
    "v_mfma_f32_16x16x32_bf16 v[204:207], a[124:127], a[44:47], v[204:207]\n"
    "buffer_load_dwordx4 v242, s[40:43], s88 offen offset:2048 lds\n"
    "v_mfma_f32_16x16x32_bf16 v[48:51], a[160:163], v[216:219], 0\n"
    "v_lshlrev_b32_e32 v104, 16, v180\n"
    "v_and_b32_e32 v105, s67, v180\n"
    "v_mfma_f32_16x16x32_bf16 v[48:51], a[164:167], v[220:223], v[48:51]\n"
    "v_lshlrev_b32_e32 v106, 16, v181\n"
    "v_and_b32_e32 v107, s67, v181\n"
    "v_mfma_f32_16x16x32_bf16 v[52:55], a[168:171], v[216:219], 0\n"
    "v_exp_f32_e32 v204, v204\n"
    "v_mfma_f32_16x16x32_bf16 v[52:55], a[172:175], v[220:223], v[52:55]\n"
    "v_lshlrev_b32_e32 v108, 16, v182\n"
    "v_and_b32_e32 v109, s67, v182\n"
    "v_lshlrev_b32_e32 v110, 16, v183\n"
    "v_and_b32_e64 v111, s67, v183\n"
    "v_pk_mul_f32 v[96:97], v[96:97], s[64:65]\n"
    "v_pk_mul_f32 v[98:99], v[98:99], s[64:65]\n"
    "v_pk_mul_f32 v[100:101], v[100:101], s[64:65]\n"
    "v_pk_mul_f32 v[102:103], v[102:103], s[64:65]\n"
    "v_cvt_pk_bf16_f32 v176, v96, v97\n"
    "v_cvt_pk_bf16_f32 v177, v98, v99\n"
    "v_cvt_pk_bf16_f32 v178, v100, v101\n"
    "v_cvt_pk_bf16_f32 v179, v102, v103\n"
    "v_accvgpr_write_b32 a48, v176\n"
    "v_accvgpr_write_b32 a49, v177\n"
    "v_accvgpr_write_b32 a50, v178\n"
    "v_accvgpr_write_b32 a51, v179\n"
    "v_pk_mul_f32 v[104:105], v[104:105], s[64:65]\n"
    "v_pk_mul_f32 v[106:107], v[106:107], s[64:65]\n"
    "s_add_u32 m0, s62, 101400\n"
    "v_mfma_f32_16x16x32_bf16 v[236:239], a[212:215], a[48:51], 0\n"
    "buffer_load_dwordx4 v242, s[40:43], s88 offen offset:3072 lds\n"
    "v_cvt_pk_bf16_f32 v180, v104, v105\n"
    "v_cvt_pk_bf16_f32 v181, v106, v107\n"
    "v_pk_mul_f32 v[108:109], v[108:109], s[64:65]\n"
    "v_pk_mul_f32 v[110:111], v[110:111], s[64:65]\n"
    "v_cvt_pk_bf16_f32 v182, v108, v109\n"
    "v_cvt_pk_bf16_f32 v183, v110, v111\n"
    "v_accvgpr_write_b32 a52, v180\n"
    "v_accvgpr_write_b32 a53, v181\n"
    "v_accvgpr_write_b32 a54, v182\n"
    "v_accvgpr_write_b32 a55, v183\n"
    "s_waitcnt lgkmcnt(0)\n"
    "v_lshlrev_b32_e32 v112, 16, v184\n"
    "v_and_b32_e64 v113, s67, v184\n"
    "v_mfma_f32_16x16x32_bf16 v[236:239], a[216:219], a[52:55], v[236:239]\n"
    "v_lshlrev_b32_e32 v114, 16, v185\n"
    "v_and_b32_e32 v115, s67, v185\n"
    "v_lshlrev_b32_e32 v116, 16, v186\n"
    "v_and_b32_e32 v117, s67, v186\n"
    "v_lshlrev_b32_e32 v118, 16, v187\n"
    "v_and_b32_e32 v119, s67, v187\n"
    "v_pk_mul_f32 v[112:113], v[112:113], s[64:65]\n"
    "v_pk_mul_f32 v[114:115], v[114:115], s[64:65]\n"
    "v_pk_mul_f32 v[116:117], v[116:117], s[64:65]\n"
    "v_pk_mul_f32 v[118:119], v[118:119], s[64:65]\n"
    "v_cvt_pk_bf16_f32 v184, v112, v113\n"
    "v_cvt_pk_bf16_f32 v185, v114, v115\n"
    "v_cvt_pk_bf16_f32 v186, v116, v117\n"
    "v_cvt_pk_bf16_f32 v187, v118, v119\n"
    "v_accvgpr_write_b32 a56, v184\n"
    "v_accvgpr_write_b32 a57, v185\n"
    "v_accvgpr_write_b32 a58, v186\n"
    "v_accvgpr_write_b32 a59, v187\n"
    "v_lshlrev_b32_e32 v120, 16, v188\n"
    "v_and_b32_e32 v121, s67, v188\n"
    "v_mfma_f32_16x16x32_bf16 v[236:239], a[220:223], a[56:59], v[236:239]\n"
    "v_lshlrev_b32_e32 v122, 16, v189\n"
    "v_and_b32_e32 v123, s67, v189\n"
    "v_lshlrev_b32_e32 v124, 16, v190\n"
    "v_and_b32_e32 v125, s67, v190\n"
    "v_lshlrev_b32_e32 v126, 16, v191\n"
    "v_and_b32_e32 v127, s67, v191\n"
    "v_pk_mul_f32 v[120:121], v[120:121], s[64:65]\n"
    "v_pk_mul_f32 v[122:123], v[122:123], s[64:65]\n"
    "v_pk_mul_f32 v[124:125], v[124:125], s[64:65]\n"
    "v_pk_mul_f32 v[126:127], v[126:127], s[64:65]\n"
    "v_cvt_pk_bf16_f32 v188, v120, v121\n"
    "v_cvt_pk_bf16_f32 v189, v122, v123\n"
    "v_cvt_pk_bf16_f32 v190, v124, v125\n"
    "v_cvt_pk_bf16_f32 v191, v126, v127\n"
    "v_accvgpr_write_b32 a60, v188\n"
    "v_accvgpr_write_b32 a61, v189\n"
    "v_accvgpr_write_b32 a62, v190\n"
    "v_accvgpr_write_b32 a63, v191\n"
    "v_cvt_pk_bf16_f32 v210, v196, v197\n"
    "v_cvt_pk_bf16_f32 v211, v198, v199\n"
    "v_mfma_f32_16x16x32_bf16 v[236:239], a[224:227], a[60:63], v[236:239]\n"
    "v_cvt_pk_bf16_f32 v212, v200, v201\n"
    "v_cvt_pk_bf16_f32 v213, v202, v203\n"
    "s_nop 6\n"
    "v_sub_f32_e32 v239, s89, v236\n"
    "v_sub_f32_e32 v238, s89, v236\n"
    "v_sub_f32_e32 v237, s89, v236\n"
    "v_sub_f32_e32 v236, s89, v236\n"
    "s_waitcnt vmcnt(8)\n"
    "s_waitcnt lgkmcnt(0)\n"
    "s_barrier\n"
    "v_mfma_f32_16x16x32_bf16 v[192:195], a[64:67], a[48:51], v[236:239]\n"
    "s_add_u32 m0, s95, 2112\n"
    "v_exp_f32_e32 v205, v205\n"
    "v_mfma_f32_16x16x32_bf16 v[192:195], a[68:71], a[52:55], v[192:195]\n"
    "buffer_load_dwordx4 v243, s[44:47], 0 offen offset:0 lds\n"
    "ds_read_b128 a[64:67], v240 offset:16896\n"
    "v_cvt_pk_bf16_f32 v214, v204, v205\n"
    "v_mfma_f32_16x16x32_bf16 v[192:195], a[72:75], a[56:59], v[192:195]\n"
    "s_add_u32 m0, s95, 1152\n"
    "v_exp_f32_e32 v206, v206\n"
    "v_mfma_f32_16x16x32_bf16 v[192:195], a[76:79], a[60:63], v[192:195]\n"
    "buffer_load_dwordx4 v243, s[44:47], 0 offen offset:2048 lds\n"
    "ds_read_b128 a[68:71], v240 offset:16960\n"
    "v_mfma_f32_16x16x32_bf16 v[56:59], a[176:179], v[216:219], 0\n"
    "v_exp_f32_e32 v207, v207\n"
    "v_mfma_f32_16x16x32_bf16 v[56:59], a[180:183], v[220:223], v[56:59]\n"
    "ds_read_b128 a[72:75], v240 offset:17024\n"
    "v_cvt_pk_bf16_f32 v215, v206, v207\n"
    "v_mfma_f32_16x16x32_bf16 v[60:63], a[184:187], v[216:219], 0\n"
    "v_exp_f32_e32 v192, v192\n"
    "v_mfma_f32_16x16x32_bf16 v[60:63], a[188:191], v[220:223], v[60:63]\n"
    "ds_read_b128 a[76:79], v240 offset:17088\n"
    "v_mfma_f32_16x16x32_bf16 v[196:199], a[80:83], a[48:51], v[236:239]\n"
    "v_exp_f32_e32 v193, v193\n"
    "v_mfma_f32_16x16x32_bf16 v[196:199], a[84:87], a[52:55], v[196:199]\n"
    "ds_read_b128 a[80:83], v240 offset:17152\n"
    "v_cvt_pk_bf16_f32 v216, v192, v193\n"
    "v_mfma_f32_16x16x32_bf16 v[196:199], a[88:91], a[56:59], v[196:199]\n"
    "v_exp_f32_e32 v194, v194\n"
    "v_mfma_f32_16x16x32_bf16 v[196:199], a[92:95], a[60:63], v[196:199]\n"
    "ds_read_b128 a[84:87], v240 offset:17216\n"
    "ds_read_b128 a[88:91], v240 offset:17280\n"
    "v_mfma_f32_16x16x32_bf16 v[64:67], a[128:131], v[208:211], 0\n"
    "s_add_u32 m0, s62, 50688\n"
    "s_xor_b32 s88, s63, 49152\n"
    "v_exp_f32_e32 v195, v195\n"
    "v_mfma_f32_16x16x32_bf16 v[64:67], a[132:135], v[212:215], v[64:67]\n"
    "buffer_load_dwordx4 v242, s[36:39], s88 offen offset:0 lds\n"
    "ds_read_b128 a[92:95], v240 offset:17344\n"
    "v_mfma_f32_16x16x32_bf16 v[68:71], a[136:139], v[208:211], 0\n"
    "v_exp_f32_e32 v196, v196\n"
    "v_mfma_f32_16x16x32_bf16 v[68:71], a[140:143], v[212:215], v[68:71]\n"
    "v_cvt_pk_bf16_f32 v217, v194, v195\n"
    "v_mfma_f32_16x16x32_bf16 v[200:203], a[96:99], a[48:51], v[236:239]\n"
    "v_exp_f32_e32 v197, v197\n"
    "v_mfma_f32_16x16x32_bf16 v[200:203], a[100:103], a[52:55], v[200:203]\n"
    "ds_read_b128 a[96:99], v240 offset:17408\n"
    "v_cvt_pk_bf16_f32 v218, v196, v197\n"
    "v_mfma_f32_16x16x32_bf16 v[200:203], a[104:107], a[56:59], v[200:203]\n"
    "v_exp_f32_e32 v198, v198\n"
    "v_mfma_f32_16x16x32_bf16 v[200:203], a[108:111], a[60:63], v[200:203]\n"
    "ds_read_b128 a[100:103], v240 offset:17472\n"
    "ds_read_b128 a[104:107], v240 offset:17536\n"
    "v_mfma_f32_16x16x32_bf16 v[72:75], a[144:147], v[208:211], 0\n"
    "v_exp_f32_e32 v199, v199\n"
    "v_mfma_f32_16x16x32_bf16 v[72:75], a[148:151], v[212:215], v[72:75]\n"
    "ds_read_b128 a[108:111], v240 offset:17600\n"
    "v_cvt_pk_bf16_f32 v219, v198, v199\n"
    "v_mfma_f32_16x16x32_bf16 v[76:79], a[152:155], v[208:211], 0\n"
    "s_add_u32 m0, s62, 50720\n"
    "v_exp_f32_e32 v200, v200\n"
    "v_mfma_f32_16x16x32_bf16 v[76:79], a[156:159], v[212:215], v[76:79]\n"
    "buffer_load_dwordx4 v242, s[36:39], s88 offen offset:1024 lds\n"
    "s_add_u32 m0, s62, 50752\n"
    "v_smfmac_f32_16x16x64_bf16 a[200:203], a[208:211], v[208:215], v254\n"
    "buffer_load_dwordx4 v242, s[36:39], s88 offen offset:2048 lds\n"
    "v_mfma_f32_16x16x32_bf16 v[204:207], a[112:115], a[48:51], v[236:239]\n"
    "v_exp_f32_e32 v201, v201\n"
    "v_mfma_f32_16x16x32_bf16 v[204:207], a[116:119], a[52:55], v[204:207]\n"
    "ds_read_b128 a[112:115], v240 offset:17664\n"
    "v_cvt_pk_bf16_f32 v220, v200, v201\n"
    "v_mfma_f32_16x16x32_bf16 v[204:207], a[120:123], a[56:59], v[204:207]\n"
    "v_exp_f32_e32 v202, v202\n"
    "v_mfma_f32_16x16x32_bf16 v[204:207], a[124:127], a[60:63], v[204:207]\n"
    "ds_read_b128 a[116:119], v240 offset:17728\n"
    "ds_read_b128 a[120:123], v240 offset:17792\n"
    "v_mfma_f32_16x16x32_bf16 v[80:83], a[160:163], v[208:211], 0\n"
    "v_exp_f32_e32 v203, v203\n"
    "v_mfma_f32_16x16x32_bf16 v[80:83], a[164:167], v[212:215], v[80:83]\n"
    "ds_read_b128 a[124:127], v240 offset:17856\n"
    "v_cvt_pk_bf16_f32 v221, v202, v203\n"
    "v_mfma_f32_16x16x32_bf16 v[84:87], a[168:171], v[208:211], 0\n"
    "v_exp_f32_e32 v204, v204\n"
    "v_mfma_f32_16x16x32_bf16 v[84:87], a[172:175], v[212:215], v[84:87]\n"
    "s_waitcnt lgkmcnt(12)\n"
    "v_mfma_f32_16x16x32_bf16 v[192:195], a[64:67], a[0:3], v[224:227]\n"
    "s_add_u32 m0, s62, 50784\n"
    "v_exp_f32_e32 v205, v205\n"
    "v_mfma_f32_16x16x32_bf16 v[192:195], a[68:71], a[4:7], v[192:195]\n"
    "buffer_load_dwordx4 v242, s[36:39], s88 offen offset:3072 lds\n"
    "v_cvt_pk_bf16_f32 v222, v204, v205\n"
    "v_mfma_f32_16x16x32_bf16 v[192:195], a[72:75], a[8:11], v[192:195]\n"
    "v_exp_f32_e32 v206, v206\n"
    "v_mfma_f32_16x16x32_bf16 v[192:195], a[76:79], a[12:15], v[192:195]\n"
    "v_mfma_f32_16x16x32_bf16 v[88:91], a[176:179], v[208:211], 0\n"
    "v_exp_f32_e32 v207, v207\n"
    "v_mfma_f32_16x16x32_bf16 v[88:91], a[180:183], v[212:215], v[88:91]\n"
    "v_cvt_pk_bf16_f32 v223, v206, v207\n"
    "v_mfma_f32_16x16x32_bf16 v[92:95], a[184:187], v[208:211], 0\n"
    "v_exp_f32_e32 v192, v192\n"
    "v_mfma_f32_16x16x32_bf16 v[92:95], a[188:191], v[212:215], v[92:95]\n"
    "s_waitcnt lgkmcnt(8)\n"
    "v_mfma_f32_16x16x32_bf16 v[196:199], a[80:83], a[0:3], v[224:227]\n"
    "v_exp_f32_e32 v193, v193\n"
    "v_mfma_f32_16x16x32_bf16 v[196:199], a[84:87], a[4:7], v[196:199]\n"
    "v_cvt_pk_bf16_f32 v208, v192, v193\n"
    "v_mfma_f32_16x16x32_bf16 v[196:199], a[88:91], a[8:11], v[196:199]\n"
    "s_sub_u32 m0, s95, 128\n"
    "v_exp_f32_e32 v194, v194\n"
    "v_mfma_f32_16x16x32_bf16 v[196:199], a[92:95], a[12:15], v[196:199]\n"
    "buffer_load_dwordx4 v243, s[44:47], 0 offen offset:128 lds\n"
    "v_mfma_f32_16x16x32_bf16 v[96:99], a[128:131], v[216:219], 0\n"
    "s_sub_u32 m0, s95, 1088\n"
    "v_exp_f32_e32 v195, v195\n"
    "v_mfma_f32_16x16x32_bf16 v[96:99], a[132:135], v[220:223], v[96:99]\n"
    "buffer_load_dwordx4 v243, s[44:47], 0 offen offset:2176 lds\n"
    "ds_read_b64_tr_b16 a[128:129], v241 offset:16896\n"
    "v_cvt_pk_bf16_f32 v209, v194, v195\n"
    "v_mfma_f32_16x16x32_bf16 v[100:103], a[136:139], v[216:219], 0\n"
    "s_add_u32 s44, s44, 16384\n"
    "s_addc_u32 s45, s45, 0\n"
    "v_exp_f32_e32 v196, v196\n"
    "v_mfma_f32_16x16x32_bf16 v[100:103], a[140:143], v[220:223], v[100:103]\n"
    "s_waitcnt lgkmcnt(5)\n"
    "ds_read_b64_tr_b16 a[130:131], v241 offset:17152\n"
    "ds_read_b64_tr_b16 a[132:133], v241 offset:17408\n"
    "v_mfma_f32_16x16x32_bf16 v[200:203], a[96:99], a[0:3], v[224:227]\n"
    "s_mul_i32 s46, s53, s56\n"
    "v_exp_f32_e32 v197, v197\n"
    "v_mfma_f32_16x16x32_bf16 v[200:203], a[100:103], a[4:7], v[200:203]\n"
    "s_add_u32 s48, s48, 16384\n"
    "s_addc_u32 s49, s49, 0\n"
    "ds_read_b64_tr_b16 a[134:135], v241 offset:17664\n"
    "ds_read_b64_tr_b16 a[136:137], v241 offset:16904\n"
    "v_mfma_f32_16x16x32_bf16 v[200:203], a[104:107], a[8:11], v[200:203]\n"
    "s_mul_i32 s50, s53, s57\n"
    "v_exp_f32_e32 v198, v198\n"
    "v_mfma_f32_16x16x32_bf16 v[200:203], a[108:111], a[12:15], v[200:203]\n"
    "ds_read_b64_tr_b16 a[138:139], v241 offset:17160\n"
    "ds_read_b64_tr_b16 a[140:141], v241 offset:17416\n"
    "v_mfma_f32_16x16x32_bf16 v[104:107], a[144:147], v[216:219], 0\n"
    "s_add_u32 m0, s62, 118272\n"
    "s_xor_b32 s88, s63, 49152\n"
    "v_exp_f32_e32 v199, v199\n"
    "v_mfma_f32_16x16x32_bf16 v[104:107], a[148:151], v[220:223], v[104:107]\n"
    "buffer_load_dwordx4 v242, s[40:43], s88 offen offset:0 lds\n"
    "ds_read_b64_tr_b16 a[142:143], v241 offset:17672\n"
    "v_mfma_f32_16x16x32_bf16 v[108:111], a[152:155], v[216:219], 0\n"
    "v_exp_f32_e32 v200, v200\n"
    "v_mfma_f32_16x16x32_bf16 v[108:111], a[156:159], v[220:223], v[108:111]\n"
    "v_cvt_pk_bf16_f32 v210, v196, v197\n"
    "v_cvt_pk_bf16_f32 v211, v198, v199\n"
    "v_smfmac_f32_16x16x64_bf16 a[204:207], a[208:211], v[216:223], v254\n"
    "s_waitcnt lgkmcnt(8)\n"
    "ds_read_b64_tr_b16 a[144:145], v241 offset:16912\n"
    "v_mfma_f32_16x16x32_bf16 v[204:207], a[112:115], a[0:3], v[224:227]\n"
    "v_exp_f32_e32 v201, v201\n"
    "v_mfma_f32_16x16x32_bf16 v[204:207], a[116:119], a[4:7], v[204:207]\n"
    "ds_read_b64_tr_b16 a[146:147], v241 offset:17168\n"
    "ds_read_b64_tr_b16 a[148:149], v241 offset:17424\n"
    "v_mfma_f32_16x16x32_bf16 v[204:207], a[120:123], a[8:11], v[204:207]\n"
    "v_exp_f32_e32 v202, v202\n"
    "v_mfma_f32_16x16x32_bf16 v[204:207], a[124:127], a[12:15], v[204:207]\n"
    "ds_read_b64_tr_b16 a[150:151], v241 offset:17680\n"
    "v_cvt_pk_bf16_f32 v212, v200, v201\n"
    "v_mfma_f32_16x16x32_bf16 v[112:115], a[160:163], v[216:219], 0\n"
    "v_exp_f32_e32 v203, v203\n"
    "v_mfma_f32_16x16x32_bf16 v[112:115], a[164:167], v[220:223], v[112:115]\n"
    "ds_read_b64_tr_b16 a[152:153], v241 offset:16920\n"
    "v_cvt_pk_bf16_f32 v213, v202, v203\n"
    "v_mfma_f32_16x16x32_bf16 v[116:119], a[168:171], v[216:219], 0\n"
    "s_add_u32 m0, s62, 118280\n"
    "v_exp_f32_e32 v204, v204\n"
    "v_mfma_f32_16x16x32_bf16 v[116:119], a[172:175], v[220:223], v[116:119]\n"
    "buffer_load_dwordx4 v242, s[40:43], s88 offen offset:1024 lds\n"
    "ds_read_b64_tr_b16 a[154:155], v241 offset:17176\n"
    "v_mfma_f32_16x16x32_bf16 v[192:195], a[64:67], a[16:19], v[228:231]\n"
    "s_add_u32 m0, s62, 118288\n"
    "v_exp_f32_e32 v205, v205\n"
    "v_mfma_f32_16x16x32_bf16 v[192:195], a[68:71], a[20:23], v[192:195]\n"
    "buffer_load_dwordx4 v242, s[40:43], s88 offen offset:2048 lds\n"
    "ds_read_b64_tr_b16 a[156:157], v241 offset:17432\n"
    "v_cvt_pk_bf16_f32 v214, v204, v205\n"
    "v_mfma_f32_16x16x32_bf16 v[192:195], a[72:75], a[24:27], v[192:195]\n"
    "v_exp_f32_e32 v206, v206\n"
    "v_mfma_f32_16x16x32_bf16 v[192:195], a[76:79], a[28:31], v[192:195]\n"
    "ds_read_b64_tr_b16 a[158:159], v241 offset:17688\n"
    "ds_read_b64_tr_b16 a[160:161], v241 offset:17024\n"
    "v_mfma_f32_16x16x32_bf16 v[120:123], a[176:179], v[216:219], 0\n"
    "v_exp_f32_e32 v207, v207\n"
    "v_mfma_f32_16x16x32_bf16 v[120:123], a[180:183], v[220:223], v[120:123]\n"
    "ds_read_b64_tr_b16 a[162:163], v241 offset:17280\n"
    "v_cvt_pk_bf16_f32 v215, v206, v207\n"
    "v_mfma_f32_16x16x32_bf16 v[124:127], a[184:187], v[216:219], 0\n"
    "v_exp_f32_e32 v192, v192\n"
    "v_mfma_f32_16x16x32_bf16 v[124:127], a[188:191], v[220:223], v[124:127]\n"
    "ds_read_b64_tr_b16 a[164:165], v241 offset:17536\n"
    "ds_read_b64_tr_b16 a[166:167], v241 offset:17792\n"
    "v_mfma_f32_16x16x32_bf16 v[196:199], a[80:83], a[16:19], v[228:231]\n"
    "s_add_u32 m0, s62, 118296\n"
    "v_exp_f32_e32 v193, v193\n"
    "v_mfma_f32_16x16x32_bf16 v[196:199], a[84:87], a[20:23], v[196:199]\n"
    "buffer_load_dwordx4 v242, s[40:43], s88 offen offset:3072 lds\n"
    "ds_read_b64_tr_b16 a[168:169], v241 offset:17032\n"
    "v_mfma_f32_16x16x32_bf16 v[196:199], a[88:91], a[24:27], v[196:199]\n"
    "v_exp_f32_e32 v194, v194\n"
    "v_mfma_f32_16x16x32_bf16 v[196:199], a[92:95], a[28:31], v[196:199]\n"
    "s_waitcnt lgkmcnt(14)\n"
    "ds_read_b64_tr_b16 a[170:171], v241 offset:17288\n"
    "ds_read_b64_tr_b16 a[172:173], v241 offset:17544\n"
    "v_mfma_f32_16x16x32_bf16 v[0:3], a[128:131], v[208:211], v[0:3]\n"
    "v_exp_f32_e32 v195, v195\n"
    "v_mfma_f32_16x16x32_bf16 v[0:3], a[132:135], v[212:215], v[0:3]\n"
    "s_waitcnt lgkmcnt(14)\n"
    "ds_read_b64_tr_b16 a[174:175], v241 offset:17800\n"
    "ds_read_b64_tr_b16 a[176:177], v241 offset:17040\n"
    "v_mfma_f32_16x16x32_bf16 v[4:7], a[136:139], v[208:211], v[4:7]\n"
    "v_exp_f32_e32 v196, v196\n"
    "v_mfma_f32_16x16x32_bf16 v[4:7], a[140:143], v[212:215], v[4:7]\n"
    "ds_read_b64_tr_b16 a[178:179], v241 offset:17296\n"
    "ds_read_b64_tr_b16 a[180:181], v241 offset:17552\n"
    "v_mfma_f32_16x16x32_bf16 v[200:203], a[96:99], a[16:19], v[228:231]\n"
    "v_exp_f32_e64 v197, v197\n"
    "v_mfma_f32_16x16x32_bf16 v[200:203], a[100:103], a[20:23], v[200:203]\n"
    "ds_read_b64_tr_b16 a[182:183], v241 offset:17808\n"
    "ds_read_b64_tr_b16 a[184:185], v241 offset:17048\n"
    "v_mfma_f32_16x16x32_bf16 v[200:203], a[104:107], a[24:27], v[200:203]\n"
    "s_mov_b64 s[84:85], s[36:37]\n"
    "s_mov_b64 s[60:61], s[40:41]\n"
    "s_mov_b32 s59, 65536\n"
    "s_xor_b32 s71, s59, s63\n"
    "s_sub_i32 s58, s38, s59\n"
    "s_add_u32 s36, s36, s71\n"
    "s_addc_u32 s37, s37, 0\n"
    "s_add_u32 s40, s40, s71\n"
    "s_addc_u32 s41, s41, 0\n"
    "s_add_u32 m0, s62, 0\n"
    "s_waitcnt lgkmcnt(14)\n"
    "v_exp_f32_e64 v198, v198\n"
    "v_mfma_f32_16x16x32_bf16 v[200:203], a[108:111], a[28:31], v[200:203]\n"
    "buffer_load_dwordx4 v242, s[36:39], 0 offen offset:0 nt lds\n"
    "ds_read_b64_tr_b16 a[186:187], v241 offset:17304\n"
    "v_mfma_f32_16x16x32_bf16 v[8:11], a[144:147], v[208:211], v[8:11]\n"
    "v_exp_f32_e32 v199, v199\n"
    "v_mfma_f32_16x16x32_bf16 v[8:11], a[148:151], v[212:215], v[8:11]\n"
    "s_waitcnt lgkmcnt(14)\n"
    "ds_read_b64_tr_b16 a[188:189], v241 offset:17560\n"
    "ds_read_b64_tr_b16 a[190:191], v241 offset:17816\n"
    "v_mfma_f32_16x16x32_bf16 v[12:15], a[152:155], v[208:211], v[12:15]\n"
    "s_add_u32 m0, s62, 32\n"
    "v_exp_f32_e32 v200, v200\n"
    "v_mfma_f32_16x16x32_bf16 v[12:15], a[156:159], v[212:215], v[12:15]\n"
    "buffer_load_dwordx4 v242, s[36:39], 0 offen offset:1024 nt lds\n"
    "v_cvt_pk_bf16_f32 v216, v192, v193\n"
    "v_cvt_pk_bf16_f32 v217, v194, v195\n"
    "v_smfmac_f32_16x16x64_bf16 a[192:195], a[208:211], v[208:215], v254\n"
    "s_waitcnt vmcnt(12)\n"
    "ds_read_b128 v[128:131], v255 offset:2112\n"
    "v_mfma_f32_16x16x32_bf16 v[204:207], a[112:115], a[16:19], v[228:231]\n"
    "v_exp_f32_e32 v201, v201\n"
    "v_mfma_f32_16x16x32_bf16 v[204:207], a[116:119], a[20:23], v[204:207]\n"
    "s_waitcnt vmcnt(14)\n"
    "ds_append v253\n"
    "ds_read_b128 v[132:135], v255 offset:2176\n"
    "v_mfma_f32_16x16x32_bf16 v[204:207], a[120:123], a[24:27], v[204:207]\n"
    "v_exp_f32_e32 v202, v202\n"
    "v_mfma_f32_16x16x32_bf16 v[204:207], a[124:127], a[28:31], v[204:207]\n"
    "s_waitcnt lgkmcnt(14)\n"
    "v_cvt_pk_bf16_f32 v218, v196, v197\n"
    "v_cvt_pk_bf16_f32 v219, v198, v199\n"
    "v_mfma_f32_16x16x32_bf16 v[16:19], a[160:163], v[208:211], v[16:19]\n"
    "v_exp_f32_e32 v203, v203\n"
    "v_mfma_f32_16x16x32_bf16 v[16:19], a[164:167], v[212:215], v[16:19]\n"
    "s_waitcnt lgkmcnt(11)\n"
    "v_cvt_pk_bf16_f32 v220, v200, v201\n"
    "v_cvt_pk_bf16_f32 v221, v202, v203\n"
    "v_mfma_f32_16x16x32_bf16 v[20:23], a[168:171], v[208:211], v[20:23]\n"
    "v_exp_f32_e32 v204, v204\n"
    "v_mfma_f32_16x16x32_bf16 v[20:23], a[172:175], v[212:215], v[20:23]\n"
    "s_waitcnt lgkmcnt(0)\n"
    "v_lshlrev_b32_e32 v244, 16, v128\n"
    "v_and_b32_e32 v245, s67, v128\n"
    "v_mfma_f32_16x16x32_bf16 v[192:195], a[64:67], a[32:35], v[232:235]\n"
    "v_exp_f32_e32 v205, v205\n"
    "v_mfma_f32_16x16x32_bf16 v[192:195], a[68:71], a[36:39], v[192:195]\n"
    "v_cvt_pk_bf16_f32 v222, v204, v205\n"
    "v_mul_f32_e32 v244, s64, v244\n"
    "v_mfma_f32_16x16x32_bf16 v[192:195], a[72:75], a[40:43], v[192:195]\n"
    "v_exp_f32_e32 v206, v206\n"
    "v_mfma_f32_16x16x32_bf16 v[192:195], a[76:79], a[44:47], v[192:195]\n"
    "s_waitcnt lgkmcnt(7)\n"
    "v_mul_f32_e32 v245, s64, v245\n"
    "v_cvt_pk_bf16_f32 v128, v244, v245\n"
    "v_mfma_f32_16x16x32_bf16 v[24:27], a[176:179], v[208:211], v[24:27]\n"
    "v_exp_f32_e32 v207, v207\n"
    "v_mfma_f32_16x16x32_bf16 v[24:27], a[180:183], v[212:215], v[24:27]\n"
    "s_waitcnt lgkmcnt(3)\n"
    "v_cvt_pk_bf16_f32 v223, v206, v207\n"
    "v_lshlrev_b32_e32 v244, 16, v129\n"
    "v_mfma_f32_16x16x32_bf16 v[28:31], a[184:187], v[208:211], v[28:31]\n"
    "v_exp_f32_e32 v192, v192\n"
    "v_mfma_f32_16x16x32_bf16 v[28:31], a[188:191], v[212:215], v[28:31]\n"
    "ds_read_b32 v253, v251\n"
    "v_and_b32_e32 v245, s67, v129\n"
    "v_mfma_f32_16x16x32_bf16 v[196:199], a[80:83], a[32:35], v[232:235]\n"
    "s_add_u32 m0, s62, 64\n"
    "v_exp_f32_e64 v193, v193\n"
    "v_mfma_f32_16x16x32_bf16 v[196:199], a[84:87], a[36:39], v[196:199]\n"
    "buffer_load_dwordx4 v242, s[36:39], 0 offen offset:2048 nt lds\n"
    "v_cvt_pk_bf16_f32 v208, v192, v193\n"
    "v_mul_f32_e32 v244, s64, v244\n"
    "v_mul_f32_e32 v245, s64, v245\n"
    "v_cvt_pk_bf16_f32 v129, v244, v245\n"
    "v_mfma_f32_16x16x32_bf16 v[196:199], a[88:91], a[40:43], v[196:199]\n"
    "v_exp_f32_e32 v194, v194\n"
    "v_mfma_f32_16x16x32_bf16 v[196:199], a[92:95], a[44:47], v[196:199]\n"
    "v_lshlrev_b32_e32 v244, 16, v130\n"
    "v_and_b32_e32 v245, s67, v130\n"
    "v_mfma_f32_16x16x32_bf16 v[32:35], a[128:131], v[216:219], v[32:35]\n"
    "LW1_%=:\n"
    "s_cbranch_execz LWR_%=\n"
    "v_exp_f32_e32 v195, v195\n"
    "v_mfma_f32_16x16x32_bf16 v[32:35], a[132:135], v[220:223], v[32:35]\n"
    "s_waitcnt lgkmcnt(0)\n"
    "v_cmp_le_u32_e32 vcc, s76, v253\n"
    "v_cvt_pk_bf16_f32 v209, v194, v195\n"
    "v_mfma_f32_16x16x32_bf16 v[36:39], a[136:139], v[216:219], v[36:39]\n"
    "v_exp_f32_e32 v196, v196\n"
    "v_mfma_f32_16x16x32_bf16 v[36:39], a[140:143], v[220:223], v[36:39]\n"
    "v_mul_f32_e32 v244, s64, v244\n"
    "v_mul_f32_e32 v245, s64, v245\n"
    "v_mfma_f32_16x16x32_bf16 v[200:203], a[96:99], a[32:35], v[232:235]\n"
    "s_cbranch_vccz LSPIN0_%=\n"
    "LRET0_%=:\n"
    "v_exp_f32_e32 v197, v197\n"
    "v_mfma_f32_16x16x32_bf16 v[200:203], a[100:103], a[36:39], v[200:203]\n"
    "s_addk_i32 s76, 256\n"
    "v_cvt_pk_bf16_f32 v210, v196, v197\n"
    "v_cvt_pk_bf16_f32 v130, v244, v245\n"
    "v_mfma_f32_16x16x32_bf16 v[200:203], a[104:107], a[40:43], v[200:203]\n"
    "s_add_u32 m0, s62, 96\n"
    "v_exp_f32_e32 v198, v198\n"
    "v_mfma_f32_16x16x32_bf16 v[200:203], a[108:111], a[44:47], v[200:203]\n"
    "buffer_load_dwordx4 v242, s[36:39], 0 offen offset:3072 nt lds\n"
    "v_lshlrev_b32_e32 v244, 16, v131\n"
    "v_mfma_f32_16x16x32_bf16 v[40:43], a[144:147], v[216:219], v[40:43]\n"
    "s_addk_i32 s59, 16384\n"
    "v_exp_f32_e32 v199, v199\n"
    "v_mfma_f32_16x16x32_bf16 v[40:43], a[148:151], v[220:223], v[40:43]\n"
    "s_xor_b32 s71, s59, s63\n"
    "v_cvt_pk_bf16_f32 v211, v198, v199\n"
    "v_and_b32_e32 v245, s67, v131\n"
    "v_mfma_f32_16x16x32_bf16 v[44:47], a[152:155], v[216:219], v[44:47]\n"
    "s_add_u32 s36, s84, s71\n"
    "s_addc_u32 s37, s85, 0\n"
    "v_exp_f32_e32 v200, v200\n"
    "v_mfma_f32_16x16x32_bf16 v[44:47], a[156:159], v[220:223], v[44:47]\n"
    "s_addk_i32 s58, -16384\n"
    "v_mul_f32_e32 v244, s64, v244\n"
    "v_mul_f32_e32 v245, s64, v245\n"
    "v_smfmac_f32_16x16x64_bf16 a[196:199], a[208:211], v[216:223], v254\n"
    "s_max_i32 s38, s58, 0\n"
    "v_cvt_pk_bf16_f32 v131, v244, v245\n"
    "v_mfma_f32_16x16x32_bf16 v[204:207], a[112:115], a[32:35], v[232:235]\n"
    "v_exp_f32_e32 v201, v201\n"
    "v_mfma_f32_16x16x32_bf16 v[204:207], a[116:119], a[36:39], v[204:207]\n"
    "v_cvt_pk_bf16_f32 v212, v200, v201\n"
    "v_lshlrev_b32_e32 v244, 16, v132\n"
    "v_mfma_f32_16x16x32_bf16 v[204:207], a[120:123], a[40:43], v[204:207]\n"
    "v_exp_f32_e32 v202, v202\n"
    "v_mfma_f32_16x16x32_bf16 v[204:207], a[124:127], a[44:47], v[204:207]\n"
    "v_and_b32_e32 v245, s67, v132\n"
    "v_mul_f32_e32 v244, s64, v244\n"
    "v_mfma_f32_16x16x32_bf16 v[48:51], a[160:163], v[216:219], v[48:51]\n"
    "s_add_u32 m0, s62, 67584\n"
    "v_exp_f32_e32 v203, v203\n"
    "v_mfma_f32_16x16x32_bf16 v[48:51], a[164:167], v[220:223], v[48:51]\n"
    "buffer_load_dwordx4 v242, s[40:43], 0 offen offset:0 nt lds\n"
    "v_cvt_pk_bf16_f32 v213, v202, v203\n"
    "v_mfma_f32_16x16x32_bf16 v[52:55], a[168:171], v[216:219], v[52:55]\n"
    "v_exp_f32_e32 v204, v204\n"
    "v_mfma_f32_16x16x32_bf16 v[52:55], a[172:175], v[220:223], v[52:55]\n"
    "v_mul_f32_e32 v245, s64, v245\n"
    "v_cvt_pk_bf16_f32 v132, v244, v245\n"
    "v_mfma_f32_16x16x32_bf16 v[192:195], a[64:67], a[48:51], v[236:239]\n"
    "v_exp_f32_e32 v205, v205\n"
    "v_mfma_f32_16x16x32_bf16 v[192:195], a[68:71], a[52:55], v[192:195]\n"
    "ds_read_b128 a[64:67], v240 offset:33792\n"
    "v_cvt_pk_bf16_f32 v214, v204, v205\n"
    "v_mfma_f32_16x16x32_bf16 v[192:195], a[72:75], a[56:59], v[192:195]\n"
    "v_exp_f32_e32 v206, v206\n"
    "v_mfma_f32_16x16x32_bf16 v[192:195], a[76:79], a[60:63], v[192:195]\n"
    "ds_read_b128 a[68:71], v240 offset:33856\n"
    "ds_read_b128 a[72:75], v240 offset:33920\n"
    "v_mfma_f32_16x16x32_bf16 v[56:59], a[176:179], v[216:219], v[56:59]\n"
    "v_exp_f32_e32 v207, v207\n"
    "v_mfma_f32_16x16x32_bf16 v[56:59], a[180:183], v[220:223], v[56:59]\n"
    "ds_read_b128 a[76:79], v240 offset:33984\n"
    "v_cvt_pk_bf16_f32 v215, v206, v207\n"
    "v_mfma_f32_16x16x32_bf16 v[60:63], a[184:187], v[216:219], v[60:63]\n"
    "s_add_u32 m0, s62, 67592\n"
    "v_exp_f32_e32 v192, v192\n"
    "v_mfma_f32_16x16x32_bf16 v[60:63], a[188:191], v[220:223], v[60:63]\n"
    "buffer_load_dwordx4 v242, s[40:43], 0 offen offset:1024 nt lds\n"
    "v_lshlrev_b32_e32 v244, 16, v133\n"
    "v_mfma_f32_16x16x32_bf16 v[196:199], a[80:83], a[48:51], v[236:239]\n"
    "v_exp_f32_e32 v193, v193\n"
    "v_mfma_f32_16x16x32_bf16 v[196:199], a[84:87], a[52:55], v[196:199]\n"
    "ds_read_b128 a[80:83], v240 offset:34048\n"
    "v_cvt_pk_bf16_f32 v216, v192, v193\n"
    "v_mfma_f32_16x16x32_bf16 v[196:199], a[88:91], a[56:59], v[196:199]\n"
    "v_exp_f32_e32 v194, v194\n"
    "v_mfma_f32_16x16x32_bf16 v[196:199], a[92:95], a[60:63], v[196:199]\n"
    "ds_read_b128 a[84:87], v240 offset:34112\n"
    "ds_read_b128 a[88:91], v240 offset:34176\n"
    "v_mfma_f32_16x16x32_bf16 v[64:67], a[128:131], v[208:211], v[64:67]\n"
    "v_exp_f32_e32 v195, v195\n"
    "v_mfma_f32_16x16x32_bf16 v[64:67], a[132:135], v[212:215], v[64:67]\n"
    "ds_read_b128 a[92:95], v240 offset:34240\n"
    "v_cvt_pk_bf16_f32 v217, v194, v195\n"
    "v_mfma_f32_16x16x32_bf16 v[68:71], a[136:139], v[208:211], v[68:71]\n"
    "v_exp_f32_e32 v196, v196\n"
    "v_mfma_f32_16x16x32_bf16 v[68:71], a[140:143], v[212:215], v[68:71]\n"
    "v_and_b32_e32 v245, s67, v133\n"
    "v_mul_f32_e32 v244, s64, v244\n"
    "v_mfma_f32_16x16x32_bf16 v[200:203], a[96:99], a[48:51], v[236:239]\n"
    "s_add_u32 m0, s62, 67600\n"
    "v_exp_f32_e32 v197, v197\n"
    "v_mfma_f32_16x16x32_bf16 v[200:203], a[100:103], a[52:55], v[200:203]\n"
    "buffer_load_dwordx4 v242, s[40:43], 0 offen offset:2048 nt lds\n"
    "ds_read_b128 a[96:99], v240 offset:34304\n"
    "v_mfma_f32_16x16x32_bf16 v[200:203], a[104:107], a[56:59], v[200:203]\n"
    "v_exp_f32_e32 v198, v198\n"
    "v_mfma_f32_16x16x32_bf16 v[200:203], a[108:111], a[60:63], v[200:203]\n"
    "ds_read_b128 a[100:103], v240 offset:34368\n"
    "v_cvt_pk_bf16_f32 v218, v196, v197\n"
    "v_mfma_f32_16x16x32_bf16 v[72:75], a[144:147], v[208:211], v[72:75]\n"
    "v_exp_f32_e32 v199, v199\n"
    "v_mfma_f32_16x16x32_bf16 v[72:75], a[148:151], v[212:215], v[72:75]\n"
    "ds_read_b128 a[104:107], v240 offset:34432\n"
    "ds_read_b128 a[108:111], v240 offset:34496\n"
    "v_mfma_f32_16x16x32_bf16 v[76:79], a[152:155], v[208:211], v[76:79]\n"
    "v_exp_f32_e32 v200, v200\n"
    "v_mfma_f32_16x16x32_bf16 v[76:79], a[156:159], v[212:215], v[76:79]\n"
    "v_cvt_pk_bf16_f32 v219, v198, v199\n"
    "v_mul_f32_e32 v245, s64, v245\n"
    "v_smfmac_f32_16x16x64_bf16 a[200:203], a[208:211], v[208:215], v254\n"
    "v_cvt_pk_bf16_f32 v133, v244, v245\n"
    "v_mfma_f32_16x16x32_bf16 v[204:207], a[112:115], a[48:51], v[236:239]\n"
    "v_exp_f32_e32 v201, v201\n"
    "v_mfma_f32_16x16x32_bf16 v[204:207], a[116:119], a[52:55], v[204:207]\n"
    "ds_read_b128 a[112:115], v240 offset:34560\n"
    "v_cvt_pk_bf16_f32 v220, v200, v201\n"
    "v_mfma_f32_16x16x32_bf16 v[204:207], a[120:123], a[56:59], v[204:207]\n"
    "s_add_u32 m0, s62, 67608\n"
    "v_exp_f32_e32 v202, v202\n"
    "v_mfma_f32_16x16x32_bf16 v[204:207], a[124:127], a[60:63], v[204:207]\n"
    "buffer_load_dwordx4 v242, s[40:43], 0 offen offset:3072 nt lds\n"
    "ds_read_b128 a[116:119], v240 offset:34624\n"
    "v_mfma_f32_16x16x32_bf16 v[80:83], a[160:163], v[208:211], v[80:83]\n"
    "s_add_u32 s40, s60, s71\n"
    "s_addc_u32 s41, s61, 0\n"
    "v_exp_f32_e32 v203, v203\n"
    "v_mfma_f32_16x16x32_bf16 v[80:83], a[164:167], v[212:215], v[80:83]\n"
    "s_max_i32 s42, s58, 0\n"
    "ds_read_b128 a[120:123], v240 offset:34688\n"
    "v_cvt_pk_bf16_f32 v221, v202, v203\n"
    "v_mfma_f32_16x16x32_bf16 v[84:87], a[168:171], v[208:211], v[84:87]\n"
    "v_exp_f32_e32 v204, v204\n"
    "v_mfma_f32_16x16x32_bf16 v[84:87], a[172:175], v[212:215], v[84:87]\n"
    "s_waitcnt lgkmcnt(11)\n"
    "ds_read_b128 a[124:127], v240 offset:34752\n"
    "v_lshlrev_b32_e32 v244, 16, v134\n"
    "v_mfma_f32_16x16x32_bf16 v[192:195], a[64:67], a[0:3], v[224:227]\n"
    "v_exp_f32_e32 v205, v205\n"
    "v_mfma_f32_16x16x32_bf16 v[192:195], a[68:71], a[4:7], v[192:195]\n"
    "v_cvt_pk_bf16_f32 v222, v204, v205\n"
    "v_and_b32_e32 v245, s67, v134\n"
    "v_mfma_f32_16x16x32_bf16 v[192:195], a[72:75], a[8:11], v[192:195]\n"
    "v_exp_f32_e32 v206, v206\n"
    "v_mfma_f32_16x16x32_bf16 v[192:195], a[76:79], a[12:15], v[192:195]\n"
    "v_mul_f32_e32 v244, s64, v244\n"
    "v_mul_f32_e32 v245, s64, v245\n"
    "v_mfma_f32_16x16x32_bf16 v[88:91], a[176:179], v[208:211], v[88:91]\n"
    "v_exp_f32_e32 v207, v207\n"
    "v_mfma_f32_16x16x32_bf16 v[88:91], a[180:183], v[212:215], v[88:91]\n"
    "v_cvt_pk_bf16_f32 v223, v206, v207\n"
    "v_cvt_pk_bf16_f32 v134, v244, v245\n"
    "v_mfma_f32_16x16x32_bf16 v[92:95], a[184:187], v[208:211], v[92:95]\n"
    "s_add_u32 m0, s62, 16896\n"
    "s_waitcnt lgkmcnt(8)\n"
    "v_exp_f32_e64 v192, v192\n"
    "v_mfma_f32_16x16x32_bf16 v[92:95], a[188:191], v[212:215], v[92:95]\n"
    "buffer_load_dwordx4 v242, s[36:39], 0 offen offset:0 nt lds\n"
    "v_lshlrev_b32_e32 v244, 16, v135\n"
    "v_and_b32_e32 v245, s67, v135\n"
    "v_mul_f32_e32 v244, s64, v244\n"
    "v_mul_f32_e32 v245, s64, v245\n"
    "v_mfma_f32_16x16x32_bf16 v[196:199], a[80:83], a[0:3], v[224:227]\n"
    "v_exp_f32_e32 v193, v193\n"
    "v_mfma_f32_16x16x32_bf16 v[196:199], a[84:87], a[4:7], v[196:199]\n"
    "v_cvt_pk_bf16_f32 v208, v192, v193\n"
    "v_cvt_pk_bf16_f32 v135, v244, v245\n"
    "v_mfma_f32_16x16x32_bf16 v[196:199], a[88:91], a[8:11], v[196:199]\n"
    "v_exp_f32_e32 v194, v194\n"
    "v_mfma_f32_16x16x32_bf16 v[196:199], a[92:95], a[12:15], v[196:199]\n"
    "v_mfma_f32_16x16x32_bf16 v[96:99], a[128:131], v[216:219], v[96:99]\n"
    "v_exp_f32_e32 v195, v195\n"
    "v_mfma_f32_16x16x32_bf16 v[96:99], a[132:135], v[220:223], v[96:99]\n"
    "ds_read_b64_tr_b16 a[128:129], v241 offset:33792\n"
    "v_cvt_pk_bf16_f32 v209, v194, v195\n"
    "v_mfma_f32_16x16x32_bf16 v[100:103], a[136:139], v[216:219], v[100:103]\n"
    "v_exp_f32_e32 v196, v196\n"
    "v_mfma_f32_16x16x32_bf16 v[100:103], a[140:143], v[220:223], v[100:103]\n"
    "s_waitcnt lgkmcnt(5)\n"
    "ds_read_b64_tr_b16 a[130:131], v241 offset:34048\n"
    "ds_read_b64_tr_b16 a[132:133], v241 offset:34304\n"
    "v_mfma_f32_16x16x32_bf16 v[200:203], a[96:99], a[0:3], v[224:227]\n"
    "s_add_u32 m0, s95, 2112\n"
    "v_exp_f32_e32 v197, v197\n"
    "v_mfma_f32_16x16x32_bf16 v[200:203], a[100:103], a[4:7], v[200:203]\n"
    "buffer_load_dwordx4 v243, s[44:47], 0 offen offset:0 lds\n"
    "ds_read_b64_tr_b16 a[134:135], v241 offset:34560\n"
    "v_mfma_f32_16x16x32_bf16 v[200:203], a[104:107], a[8:11], v[200:203]\n"
    "v_exp_f32_e32 v198, v198\n"
    "v_mfma_f32_16x16x32_bf16 v[200:203], a[108:111], a[12:15], v[200:203]\n"
    "ds_read_b64_tr_b16 a[136:137], v241 offset:33800\n"
    "ds_read_b64_tr_b16 a[138:139], v241 offset:34056\n"
    "v_mfma_f32_16x16x32_bf16 v[104:107], a[144:147], v[216:219], v[104:107]\n"
    "v_exp_f32_e32 v199, v199\n"
    "v_mfma_f32_16x16x32_bf16 v[104:107], a[148:151], v[220:223], v[104:107]\n"
    "ds_read_b64_tr_b16 a[140:141], v241 offset:34312\n"
    "ds_read_b64_tr_b16 a[142:143], v241 offset:34568\n"
    "v_mfma_f32_16x16x32_bf16 v[108:111], a[152:155], v[216:219], v[108:111]\n"
    "v_exp_f32_e32 v200, v200\n"
    "v_mfma_f32_16x16x32_bf16 v[108:111], a[156:159], v[220:223], v[108:111]\n"
    "v_cvt_pk_bf16_f32 v210, v196, v197\n"
    "v_cvt_pk_bf16_f32 v211, v198, v199\n"
    "v_smfmac_f32_16x16x64_bf16 a[204:207], a[208:211], v[216:223], v254\n"
    "s_waitcnt lgkmcnt(8)\n"
    "ds_read_b64_tr_b16 a[144:145], v241 offset:33808\n"
    "v_mfma_f32_16x16x32_bf16 v[204:207], a[112:115], a[0:3], v[224:227]\n"
    "v_exp_f32_e32 v201, v201\n"
    "v_mfma_f32_16x16x32_bf16 v[204:207], a[116:119], a[4:7], v[204:207]\n"
    "ds_read_b64_tr_b16 a[146:147], v241 offset:34064\n"
    "ds_read_b64_tr_b16 a[148:149], v241 offset:34320\n"
    "v_mfma_f32_16x16x32_bf16 v[204:207], a[120:123], a[8:11], v[204:207]\n"
    "s_add_u32 m0, s95, 1152\n"
    "v_exp_f32_e32 v202, v202\n"
    "v_mfma_f32_16x16x32_bf16 v[204:207], a[124:127], a[12:15], v[204:207]\n"
    "buffer_load_dwordx4 v243, s[44:47], 0 offen offset:2048 lds\n"
    "ds_read_b64_tr_b16 a[150:151], v241 offset:34576\n"
    "v_mfma_f32_16x16x32_bf16 v[112:115], a[160:163], v[216:219], v[112:115]\n"
    "v_exp_f32_e32 v203, v203\n"
    "v_mfma_f32_16x16x32_bf16 v[112:115], a[164:167], v[220:223], v[112:115]\n"
    "v_cvt_pk_bf16_f32 v212, v200, v201\n"
    "v_cvt_pk_bf16_f32 v213, v202, v203\n"
    "v_mfma_f32_16x16x32_bf16 v[116:119], a[168:171], v[216:219], v[116:119]\n"
    "v_exp_f32_e32 v204, v204\n"
    "v_mfma_f32_16x16x32_bf16 v[116:119], a[172:175], v[220:223], v[116:119]\n"
    "ds_read_b64_tr_b16 a[152:153], v241 offset:33816\n"
    "ds_read_b64_tr_b16 a[154:155], v241 offset:34072\n"
    "v_mfma_f32_16x16x32_bf16 v[192:195], a[64:67], a[16:19], v[228:231]\n"
    "v_exp_f32_e32 v205, v205\n"
    "v_mfma_f32_16x16x32_bf16 v[192:195], a[68:71], a[20:23], v[192:195]\n"
    "ds_read_b64_tr_b16 a[156:157], v241 offset:34328\n"
    "v_cvt_pk_bf16_f32 v214, v204, v205\n"
    "v_mfma_f32_16x16x32_bf16 v[192:195], a[72:75], a[24:27], v[192:195]\n"
    "v_exp_f32_e32 v206, v206\n"
    "v_mfma_f32_16x16x32_bf16 v[192:195], a[76:79], a[28:31], v[192:195]\n"
    "s_waitcnt vmcnt(15)\n"
    "ds_read_b64_tr_b16 a[158:159], v241 offset:34584\n"
    "ds_read_b128 v[136:139], v255 offset:0\n"
    "v_mfma_f32_16x16x32_bf16 v[120:123], a[176:179], v[216:219], v[120:123]\n"
    "v_exp_f32_e32 v207, v207\n"
    "v_mfma_f32_16x16x32_bf16 v[120:123], a[180:183], v[220:223], v[120:123]\n"
    "ds_read_b128 v[140:143], v255 offset:64\n"
    "v_cvt_pk_bf16_f32 v215, v206, v207\n"
    "v_mfma_f32_16x16x32_bf16 v[124:127], a[184:187], v[216:219], v[124:127]\n"
    "s_add_u32 m0, s62, 16928\n"
    "v_exp_f32_e32 v192, v192\n"
    "v_mfma_f32_16x16x32_bf16 v[124:127], a[188:191], v[220:223], v[124:127]\n"
    "buffer_load_dwordx4 v242, s[36:39], 0 offen offset:1024 nt lds\n"
    "ds_read_b64_tr_b16 a[160:161], v241 offset:33920\n"
    "v_mfma_f32_16x16x32_bf16 v[196:199], a[80:83], a[16:19], v[228:231]\n"
    "v_exp_f32_e32 v193, v193\n"
    "v_mfma_f32_16x16x32_bf16 v[196:199], a[84:87], a[20:23], v[196:199]\n"
    "ds_read_b64_tr_b16 a[162:163], v241 offset:34176\n"
    "ds_read_b64_tr_b16 a[164:165], v241 offset:34432\n"
    "v_mfma_f32_16x16x32_bf16 v[196:199], a[88:91], a[24:27], v[196:199]\n"
    "s_waitcnt lgkmcnt(14)\n"
    "v_exp_f32_e32 v194, v194\n"
    "v_mfma_f32_16x16x32_bf16 v[196:199], a[92:95], a[28:31], v[196:199]\n"
    "s_waitcnt lgkmcnt(3)\n"
    "ds_read_b64_tr_b16 a[166:167], v241 offset:34688\n"
    "ds_read_b64_tr_b16 a[168:169], v241 offset:33928\n"
    "v_mfma_f32_16x16x32_bf16 v[0:3], a[128:131], v[208:211], v[0:3]\n"
    "v_exp_f32_e32 v195, v195\n"
    "v_mfma_f32_16x16x32_bf16 v[0:3], a[132:135], v[212:215], v[0:3]\n"
    "s_waitcnt lgkmcnt(14)\n"
    "ds_read_b64_tr_b16 a[170:171], v241 offset:34184\n"
    "ds_read_b64_tr_b16 a[172:173], v241 offset:34440\n"
    "v_mfma_f32_16x16x32_bf16 v[4:7], a[136:139], v[208:211], v[4:7]\n"
    "v_exp_f32_e32 v196, v196\n"
    "v_mfma_f32_16x16x32_bf16 v[4:7], a[140:143], v[212:215], v[4:7]\n"
    "ds_read_b64_tr_b16 a[174:175], v241 offset:34696\n"
    "ds_read_b64_tr_b16 a[176:177], v241 offset:33936\n"
    "v_mfma_f32_16x16x32_bf16 v[200:203], a[96:99], a[16:19], v[228:231]\n"
    "s_add_u32 m0, s62, 16960\n"
    "v_exp_f32_e32 v197, v197\n"
    "v_mfma_f32_16x16x32_bf16 v[200:203], a[100:103], a[20:23], v[200:203]\n"
    "buffer_load_dwordx4 v242, s[36:39], 0 offen offset:2048 nt lds\n"
    "ds_read_b64_tr_b16 a[178:179], v241 offset:34192\n"
    "v_mfma_f32_16x16x32_bf16 v[200:203], a[104:107], a[24:27], v[200:203]\n"
    "v_exp_f32_e32 v198, v198\n"
    "v_mfma_f32_16x16x32_bf16 v[200:203], a[108:111], a[28:31], v[200:203]\n"
    "s_waitcnt lgkmcnt(14)\n"
    "ds_read_b64_tr_b16 a[180:181], v241 offset:34448\n"
    "ds_read_b64_tr_b16 a[182:183], v241 offset:34704\n"
    "v_mfma_f32_16x16x32_bf16 v[8:11], a[144:147], v[208:211], v[8:11]\n"
    "v_exp_f32_e32 v199, v199\n"
    "v_mfma_f32_16x16x32_bf16 v[8:11], a[148:151], v[212:215], v[8:11]\n"
    "s_waitcnt lgkmcnt(14)\n"
    "ds_read_b64_tr_b16 a[184:185], v241 offset:33944\n"
    "ds_read_b64_tr_b16 a[186:187], v241 offset:34200\n"
    "v_mfma_f32_16x16x32_bf16 v[12:15], a[152:155], v[208:211], v[12:15]\n"
    "v_exp_f32_e32 v200, v200\n"
    "v_mfma_f32_16x16x32_bf16 v[12:15], a[156:159], v[212:215], v[12:15]\n"
    "ds_read_b64_tr_b16 a[188:189], v241 offset:34456\n"
    "ds_read_b64_tr_b16 a[190:191], v241 offset:34712\n"
    "v_smfmac_f32_16x16x64_bf16 a[192:195], a[208:211], v[208:215], v254\n"
    "v_cvt_pk_bf16_f32 v216, v192, v193\n"
    "v_mfma_f32_16x16x32_bf16 v[204:207], a[112:115], a[16:19], v[228:231]\n"
    "v_exp_f32_e32 v201, v201\n"
    "v_mfma_f32_16x16x32_bf16 v[204:207], a[116:119], a[20:23], v[204:207]\n"
    "s_waitcnt vmcnt(13)\n"
    "ds_append v253\n"
    "v_cvt_pk_bf16_f32 v217, v194, v195\n"
    "v_mfma_f32_16x16x32_bf16 v[204:207], a[120:123], a[24:27], v[204:207]\n"
    "s_add_u32 m0, s62, 16992\n"
    "s_waitcnt lgkmcnt(13)\n"
    "v_exp_f32_e32 v202, v202\n"
    "v_mfma_f32_16x16x32_bf16 v[204:207], a[124:127], a[28:31], v[204:207]\n"
    "buffer_load_dwordx4 v242, s[36:39], 0 offen offset:3072 nt lds\n"
    "v_cvt_pk_bf16_f32 v218, v196, v197\n"
    "v_mfma_f32_16x16x32_bf16 v[16:19], a[160:163], v[208:211], v[16:19]\n"
    "s_addk_i32 s59, 16384\n"
    "v_exp_f32_e32 v203, v203\n"
    "v_mfma_f32_16x16x32_bf16 v[16:19], a[164:167], v[212:215], v[16:19]\n"
    "s_waitcnt lgkmcnt(9)\n"
    "v_cvt_pk_bf16_f32 v219, v198, v199\n"
    "v_cvt_pk_bf16_f32 v220, v200, v201\n"
    "v_mfma_f32_16x16x32_bf16 v[20:23], a[168:171], v[208:211], v[20:23]\n"
    "s_xor_b32 s71, s59, s63\n"
    "v_exp_f32_e32 v204, v204\n"
    "v_mfma_f32_16x16x32_bf16 v[20:23], a[172:175], v[212:215], v[20:23]\n"
    "s_add_u32 s36, s84, s71\n"
    "s_addc_u32 s37, s85, 0\n"
    "v_cvt_pk_bf16_f32 v221, v202, v203\n"
    "v_lshlrev_b32_e32 v244, 16, v136\n"
    "v_mfma_f32_16x16x32_bf16 v[192:195], a[64:67], a[32:35], v[232:235]\n"
    "s_addk_i32 s58, -16384\n"
    "v_exp_f32_e32 v205, v205\n"
    "v_mfma_f32_16x16x32_bf16 v[192:195], a[68:71], a[36:39], v[192:195]\n"
    "s_max_i32 s38, s58, 0\n"
    "v_cvt_pk_bf16_f32 v222, v204, v205\n"
    "v_and_b32_e32 v245, s67, v136\n"
    "v_mfma_f32_16x16x32_bf16 v[192:195], a[72:75], a[40:43], v[192:195]\n"
    "v_exp_f32_e32 v206, v206\n"
    "v_mfma_f32_16x16x32_bf16 v[192:195], a[76:79], a[44:47], v[192:195]\n"
    "s_waitcnt lgkmcnt(5)\n"
    "v_mul_f32_e32 v244, s64, v244\n"
    "v_mul_f32_e32 v245, s64, v245\n"
    "v_mfma_f32_16x16x32_bf16 v[24:27], a[176:179], v[208:211], v[24:27]\n"
    "v_exp_f32_e32 v207, v207\n"
    "v_mfma_f32_16x16x32_bf16 v[24:27], a[180:183], v[212:215], v[24:27]\n"
    "s_waitcnt lgkmcnt(1)\n"
    "v_cvt_pk_bf16_f32 v223, v206, v207\n"
    "v_cvt_pk_bf16_f32 v136, v244, v245\n"
    "v_mfma_f32_16x16x32_bf16 v[28:31], a[184:187], v[208:211], v[28:31]\n"
    "v_exp_f32_e32 v192, v192\n"
    "v_mfma_f32_16x16x32_bf16 v[28:31], a[188:191], v[212:215], v[28:31]\n"
    "ds_read_b32 v253, v251\n"
    "v_lshlrev_b32_e32 v244, 16, v137\n"
    "v_mfma_f32_16x16x32_bf16 v[196:199], a[80:83], a[32:35], v[232:235]\n"
    "s_add_u32 m0, s62, 84480\n"
    "v_exp_f32_e64 v193, v193\n"
    "v_mfma_f32_16x16x32_bf16 v[196:199], a[84:87], a[36:39], v[196:199]\n"
    "buffer_load_dwordx4 v242, s[40:43], 0 offen offset:0 nt lds\n"
    "v_cvt_pk_bf16_f32 v208, v192, v193\n"
    "v_and_b32_e32 v245, s67, v137\n"
    "v_mul_f32_e32 v244, s64, v244\n"
    "v_mul_f32_e64 v245, s64, v245\n"
    "v_mfma_f32_16x16x32_bf16 v[196:199], a[88:91], a[40:43], v[196:199]\n"
    "v_exp_f32_e32 v194, v194\n"
    "v_mfma_f32_16x16x32_bf16 v[196:199], a[92:95], a[44:47], v[196:199]\n"
    "v_cvt_pk_bf16_f32 v137, v244, v245\n"
    "v_lshlrev_b32_e32 v244, 16, v138\n"
    "v_mfma_f32_16x16x32_bf16 v[32:35], a[128:131], v[216:219], v[32:35]\n"
    "v_exp_f32_e32 v195, v195\n"
    "v_mfma_f32_16x16x32_bf16 v[32:35], a[132:135], v[220:223], v[32:35]\n"
    "s_waitcnt lgkmcnt(0)\n"
    "v_cmp_le_u32_e32 vcc, s76, v253\n"
    "v_cvt_pk_bf16_f32 v209, v194, v195\n"
    "v_mfma_f32_16x16x32_bf16 v[36:39], a[136:139], v[216:219], v[36:39]\n"
    "v_exp_f32_e32 v196, v196\n"
    "v_mfma_f32_16x16x32_bf16 v[36:39], a[140:143], v[220:223], v[36:39]\n"
    "v_and_b32_e32 v245, s67, v138\n"
    "v_mul_f32_e32 v244, s64, v244\n"
    "v_mfma_f32_16x16x32_bf16 v[200:203], a[96:99], a[32:35], v[232:235]\n"
    "s_cbranch_vccz LSPIN1_%=\n"
    "LRET1_%=:\n"
    "v_exp_f32_e32 v197, v197\n"
    "v_mfma_f32_16x16x32_bf16 v[200:203], a[100:103], a[36:39], v[200:203]\n"
    "s_addk_i32 s76, 256\n"
    "v_cvt_pk_bf16_f32 v210, v196, v197\n"
    "v_mul_f32_e32 v245, s64, v245\n"
    "v_mfma_f32_16x16x32_bf16 v[200:203], a[104:107], a[40:43], v[200:203]\n"
    "s_add_u32 m0, s62, 84488\n"
    "v_exp_f32_e32 v198, v198\n"
    "v_mfma_f32_16x16x32_bf16 v[200:203], a[108:111], a[44:47], v[200:203]\n"
    "buffer_load_dwordx4 v242, s[40:43], 0 offen offset:1024 nt lds\n"
    "v_cvt_pk_bf16_f32 v138, v244, v245\n"
    "v_mfma_f32_16x16x32_bf16 v[40:43], a[144:147], v[216:219], v[40:43]\n"
    "v_exp_f32_e32 v199, v199\n"
    "v_mfma_f32_16x16x32_bf16 v[40:43], a[148:151], v[220:223], v[40:43]\n"
    "v_cvt_pk_bf16_f32 v211, v198, v199\n"
    "v_lshlrev_b32_e32 v244, 16, v139\n"
    "v_mfma_f32_16x16x32_bf16 v[44:47], a[152:155], v[216:219], v[44:47]\n"
    "v_exp_f32_e32 v200, v200\n"
    "v_mfma_f32_16x16x32_bf16 v[44:47], a[156:159], v[220:223], v[44:47]\n"
    "v_and_b32_e32 v245, s67, v139\n"
    "v_mul_f32_e32 v244, s64, v244\n"
    "v_smfmac_f32_16x16x64_bf16 a[196:199], a[208:211], v[216:223], v254\n"
    "v_mul_f32_e32 v245, s64, v245\n"
    "v_mfma_f32_16x16x32_bf16 v[204:207], a[112:115], a[32:35], v[232:235]\n"
    "v_exp_f32_e32 v201, v201\n"
    "v_mfma_f32_16x16x32_bf16 v[204:207], a[116:119], a[36:39], v[204:207]\n"
    "v_cvt_pk_bf16_f32 v212, v200, v201\n"
    "v_cvt_pk_bf16_f32 v139, v244, v245\n"
    "v_mfma_f32_16x16x32_bf16 v[204:207], a[120:123], a[40:43], v[204:207]\n"
    "v_exp_f32_e32 v202, v202\n"
    "v_mfma_f32_16x16x32_bf16 v[204:207], a[124:127], a[44:47], v[204:207]\n"
    "v_lshlrev_b32_e32 v244, 16, v140\n"
    "v_and_b32_e32 v245, s67, v140\n"
    "v_mfma_f32_16x16x32_bf16 v[48:51], a[160:163], v[216:219], v[48:51]\n"
    "s_add_u32 m0, s62, 84496\n"
    "v_exp_f32_e32 v203, v203\n"
    "v_mfma_f32_16x16x32_bf16 v[48:51], a[164:167], v[220:223], v[48:51]\n"
    "buffer_load_dwordx4 v242, s[40:43], 0 offen offset:2048 nt lds\n"
    "v_cvt_pk_bf16_f32 v213, v202, v203\n"
    "v_mfma_f32_16x16x32_bf16 v[52:55], a[168:171], v[216:219], v[52:55]\n"
    "v_exp_f32_e32 v204, v204\n"
    "v_mfma_f32_16x16x32_bf16 v[52:55], a[172:175], v[220:223], v[52:55]\n"
    "v_mul_f32_e32 v244, s64, v244\n"
    "v_mul_f32_e32 v245, s64, v245\n"
    "v_mfma_f32_16x16x32_bf16 v[192:195], a[64:67], a[48:51], v[236:239]\n"
    "v_exp_f32_e32 v205, v205\n"
    "v_mfma_f32_16x16x32_bf16 v[192:195], a[68:71], a[52:55], v[192:195]\n"
    "ds_read_b128 a[64:67], v240 offset:50688\n"
    "v_cvt_pk_bf16_f32 v214, v204, v205\n"
    "v_mfma_f32_16x16x32_bf16 v[192:195], a[72:75], a[56:59], v[192:195]\n"
    "v_exp_f32_e32 v206, v206\n"
    "v_mfma_f32_16x16x32_bf16 v[192:195], a[76:79], a[60:63], v[192:195]\n"
    "ds_read_b128 a[68:71], v240 offset:50752\n"
    "ds_read_b128 a[72:75], v240 offset:50816\n"
    "v_mfma_f32_16x16x32_bf16 v[56:59], a[176:179], v[216:219], v[56:59]\n"
    "v_exp_f32_e32 v207, v207\n"
    "v_mfma_f32_16x16x32_bf16 v[56:59], a[180:183], v[220:223], v[56:59]\n"
    "ds_read_b128 a[76:79], v240 offset:50880\n"
    "v_cvt_pk_bf16_f32 v215, v206, v207\n"
    "v_mfma_f32_16x16x32_bf16 v[60:63], a[184:187], v[216:219], v[60:63]\n"
    "s_add_u32 m0, s62, 84504\n"
    "v_exp_f32_e32 v192, v192\n"
    "v_mfma_f32_16x16x32_bf16 v[60:63], a[188:191], v[220:223], v[60:63]\n"
    "buffer_load_dwordx4 v242, s[40:43], 0 offen offset:3072 nt lds\n"
    "v_cvt_pk_bf16_f32 v140, v244, v245\n"
    "v_mfma_f32_16x16x32_bf16 v[196:199], a[80:83], a[48:51], v[236:239]\n"
    "s_add_u32 s40, s60, s71\n"
    "s_addc_u32 s41, s61, 0\n"
    "v_exp_f32_e32 v193, v193\n"
    "v_mfma_f32_16x16x32_bf16 v[196:199], a[84:87], a[52:55], v[196:199]\n"
    "s_max_i32 s42, s58, 0\n"
    "ds_read_b128 a[80:83], v240 offset:50944\n"
    "v_cvt_pk_bf16_f32 v216, v192, v193\n"
    "v_mfma_f32_16x16x32_bf16 v[196:199], a[88:91], a[56:59], v[196:199]\n"
    "v_exp_f32_e32 v194, v194\n"
    "v_mfma_f32_16x16x32_bf16 v[196:199], a[92:95], a[60:63], v[196:199]\n"
    "ds_read_b128 a[84:87], v240 offset:51008\n"
    "ds_read_b128 a[88:91], v240 offset:51072\n"
    "v_mfma_f32_16x16x32_bf16 v[64:67], a[128:131], v[208:211], v[64:67]\n"
    "v_exp_f32_e32 v195, v195\n"
    "v_mfma_f32_16x16x32_bf16 v[64:67], a[132:135], v[212:215], v[64:67]\n"
    "ds_read_b128 a[92:95], v240 offset:51136\n"
    "v_cvt_pk_bf16_f32 v217, v194, v195\n"
    "v_mfma_f32_16x16x32_bf16 v[68:71], a[136:139], v[208:211], v[68:71]\n"
    "v_exp_f32_e32 v196, v196\n"
    "v_mfma_f32_16x16x32_bf16 v[68:71], a[140:143], v[212:215], v[68:71]\n"
    "v_lshlrev_b32_e32 v244, 16, v141\n"
    "v_and_b32_e32 v245, s67, v141\n"
    "v_mfma_f32_16x16x32_bf16 v[200:203], a[96:99], a[48:51], v[236:239]\n"
    "s_add_u32 m0, s62, 33792\n"
    "v_exp_f32_e32 v197, v197\n"
    "v_mfma_f32_16x16x32_bf16 v[200:203], a[100:103], a[52:55], v[200:203]\n"
    "buffer_load_dwordx4 v242, s[36:39], 0 offen offset:0 nt lds\n"
    "ds_read_b128 a[96:99], v240 offset:51200\n"
    "v_mfma_f32_16x16x32_bf16 v[200:203], a[104:107], a[56:59], v[200:203]\n"
    "v_exp_f32_e32 v198, v198\n"
    "v_mfma_f32_16x16x32_bf16 v[200:203], a[108:111], a[60:63], v[200:203]\n"
    "ds_read_b128 a[100:103], v240 offset:51264\n"
    "v_cvt_pk_bf16_f32 v218, v196, v197\n"
    "v_mfma_f32_16x16x32_bf16 v[72:75], a[144:147], v[208:211], v[72:75]\n"
    "v_exp_f32_e32 v199, v199\n"
    "v_mfma_f32_16x16x32_bf16 v[72:75], a[148:151], v[212:215], v[72:75]\n"
    "ds_read_b128 a[104:107], v240 offset:51328\n"
    "ds_read_b128 a[108:111], v240 offset:51392\n"
    "v_mfma_f32_16x16x32_bf16 v[76:79], a[152:155], v[208:211], v[76:79]\n"
    "v_exp_f32_e32 v200, v200\n"
    "v_mfma_f32_16x16x32_bf16 v[76:79], a[156:159], v[212:215], v[76:79]\n"
    "v_cvt_pk_bf16_f32 v219, v198, v199\n"
    "v_mul_f32_e32 v244, s64, v244\n"
    "v_smfmac_f32_16x16x64_bf16 a[200:203], a[208:211], v[208:215], v254\n"
    "v_mul_f32_e32 v245, s64, v245\n"
    "v_mfma_f32_16x16x32_bf16 v[204:207], a[112:115], a[48:51], v[236:239]\n"
    "v_exp_f32_e32 v201, v201\n"
    "v_mfma_f32_16x16x32_bf16 v[204:207], a[116:119], a[52:55], v[204:207]\n"
    "ds_read_b128 a[112:115], v240 offset:51456\n"
    "v_cvt_pk_bf16_f32 v220, v200, v201\n"
    "v_mfma_f32_16x16x32_bf16 v[204:207], a[120:123], a[56:59], v[204:207]\n"
    "s_sub_u32 m0, s95, 128\n"
    "v_exp_f32_e32 v202, v202\n"
    "v_mfma_f32_16x16x32_bf16 v[204:207], a[124:127], a[60:63], v[204:207]\n"
    "buffer_load_dwordx4 v243, s[44:47], 0 offen offset:128 lds\n"
    "ds_read_b128 a[116:119], v240 offset:51520\n"
    "v_mfma_f32_16x16x32_bf16 v[80:83], a[160:163], v[208:211], v[80:83]\n"
    "v_exp_f32_e32 v203, v203\n"
    "v_mfma_f32_16x16x32_bf16 v[80:83], a[164:167], v[212:215], v[80:83]\n"
    "ds_read_b128 a[120:123], v240 offset:51584\n"
    "v_cvt_pk_bf16_f32 v221, v202, v203\n"
    "v_mfma_f32_16x16x32_bf16 v[84:87], a[168:171], v[208:211], v[84:87]\n"
    "v_exp_f32_e32 v204, v204\n"
    "v_mfma_f32_16x16x32_bf16 v[84:87], a[172:175], v[212:215], v[84:87]\n"
    "s_waitcnt lgkmcnt(11)\n"
    "ds_read_b128 a[124:127], v240 offset:51648\n"
    "v_cvt_pk_bf16_f32 v141, v244, v245\n"
    "v_mfma_f32_16x16x32_bf16 v[192:195], a[64:67], a[0:3], v[224:227]\n"
    "v_exp_f32_e32 v205, v205\n"
    "v_mfma_f32_16x16x32_bf16 v[192:195], a[68:71], a[4:7], v[192:195]\n"
    "v_cvt_pk_bf16_f32 v222, v204, v205\n"
    "v_lshlrev_b32_e32 v244, 16, v142\n"
    "v_mfma_f32_16x16x32_bf16 v[192:195], a[72:75], a[8:11], v[192:195]\n"
    "v_exp_f32_e32 v206, v206\n"
    "v_mfma_f32_16x16x32_bf16 v[192:195], a[76:79], a[12:15], v[192:195]\n"
    "v_and_b32_e32 v245, s67, v142\n"
    "v_mul_f32_e32 v244, s64, v244\n"
    "v_mfma_f32_16x16x32_bf16 v[88:91], a[176:179], v[208:211], v[88:91]\n"
    "v_exp_f32_e32 v207, v207\n"
    "v_mfma_f32_16x16x32_bf16 v[88:91], a[180:183], v[212:215], v[88:91]\n"
    "v_cvt_pk_bf16_f32 v223, v206, v207\n"
    "v_mul_f32_e32 v245, s64, v245\n"
    "v_mfma_f32_16x16x32_bf16 v[92:95], a[184:187], v[208:211], v[92:95]\n"
    "s_sub_u32 m0, s95, 1088\n"
    "s_waitcnt lgkmcnt(8)\n"
    "v_exp_f32_e32 v192, v192\n"
    "v_mfma_f32_16x16x32_bf16 v[92:95], a[188:191], v[212:215], v[92:95]\n"
    "buffer_load_dwordx4 v243, s[44:47], 0 offen offset:2176 lds\n"
    "v_cvt_pk_bf16_f32 v142, v244, v245\n"
    "v_lshlrev_b32_e32 v244, 16, v143\n"
    "v_and_b32_e32 v245, s67, v143\n"
    "v_mul_f32_e64 v244, s64, v244\n"
    "v_mfma_f32_16x16x32_bf16 v[196:199], a[80:83], a[0:3], v[224:227]\n"
    "s_add_u32 s44, s44, 16384\n"
    "s_addc_u32 s45, s45, 0\n"
    "v_exp_f32_e32 v193, v193\n"
    "v_mfma_f32_16x16x32_bf16 v[196:199], a[84:87], a[4:7], v[196:199]\n"
    "s_mul_i32 s46, s54, s56\n"
    "v_cvt_pk_bf16_f32 v208, v192, v193\n"
    "v_mul_f32_e32 v245, s64, v245\n"
    "v_mfma_f32_16x16x32_bf16 v[196:199], a[88:91], a[8:11], v[196:199]\n"
    "s_add_u32 s48, s48, 16384\n"
    "s_addc_u32 s49, s49, 0\n"
    "v_exp_f32_e32 v194, v194\n"
    "v_mfma_f32_16x16x32_bf16 v[196:199], a[92:95], a[12:15], v[196:199]\n"
    "s_mul_i32 s50, s54, s57\n"
    "v_cvt_pk_bf16_f32 v143, v244, v245\n"
    "v_mfma_f32_16x16x32_bf16 v[96:99], a[128:131], v[216:219], v[96:99]\n"
    "v_exp_f32_e32 v195, v195\n"
    "v_mfma_f32_16x16x32_bf16 v[96:99], a[132:135], v[220:223], v[96:99]\n"
    "ds_read_b64_tr_b16 a[128:129], v241 offset:50688\n"
    "v_cvt_pk_bf16_f32 v209, v194, v195\n"
    "v_mfma_f32_16x16x32_bf16 v[100:103], a[136:139], v[216:219], v[100:103]\n"
    "v_exp_f32_e32 v196, v196\n"
    "v_mfma_f32_16x16x32_bf16 v[100:103], a[140:143], v[220:223], v[100:103]\n"
    "s_waitcnt lgkmcnt(5)\n"
    "ds_read_b64_tr_b16 a[130:131], v241 offset:50944\n"
    "ds_read_b64_tr_b16 a[132:133], v241 offset:51200\n"
    "v_mfma_f32_16x16x32_bf16 v[200:203], a[96:99], a[0:3], v[224:227]\n"
    "s_add_u32 m0, s62, 33824\n"
    "v_exp_f32_e32 v197, v197\n"
    "v_mfma_f32_16x16x32_bf16 v[200:203], a[100:103], a[4:7], v[200:203]\n"
    "buffer_load_dwordx4 v242, s[36:39], 0 offen offset:1024 nt lds\n"
    "ds_read_b64_tr_b16 a[134:135], v241 offset:51456\n"
    "v_mfma_f32_16x16x32_bf16 v[200:203], a[104:107], a[8:11], v[200:203]\n"
    "v_exp_f32_e32 v198, v198\n"
    "v_mfma_f32_16x16x32_bf16 v[200:203], a[108:111], a[12:15], v[200:203]\n"
    "ds_read_b64_tr_b16 a[136:137], v241 offset:50696\n"
    "ds_read_b64_tr_b16 a[138:139], v241 offset:50952\n"
    "v_mfma_f32_16x16x32_bf16 v[104:107], a[144:147], v[216:219], v[104:107]\n"
    "v_exp_f32_e32 v199, v199\n"
    "v_mfma_f32_16x16x32_bf16 v[104:107], a[148:151], v[220:223], v[104:107]\n"
    "ds_read_b64_tr_b16 a[140:141], v241 offset:51208\n"
    "ds_read_b64_tr_b16 a[142:143], v241 offset:51464\n"
    "v_mfma_f32_16x16x32_bf16 v[108:111], a[152:155], v[216:219], v[108:111]\n"
    "v_exp_f32_e32 v200, v200\n"
    "v_mfma_f32_16x16x32_bf16 v[108:111], a[156:159], v[220:223], v[108:111]\n"
    "v_cvt_pk_bf16_f32 v210, v196, v197\n"
    "v_cvt_pk_bf16_f32 v211, v198, v199\n"
    "v_smfmac_f32_16x16x64_bf16 a[204:207], a[208:211], v[216:223], v254\n"
    "s_waitcnt lgkmcnt(8)\n"
    "ds_read_b64_tr_b16 a[144:145], v241 offset:50704\n"
    "v_mfma_f32_16x16x32_bf16 v[204:207], a[112:115], a[0:3], v[224:227]\n"
    "v_exp_f32_e32 v201, v201\n"
    "v_mfma_f32_16x16x32_bf16 v[204:207], a[116:119], a[4:7], v[204:207]\n"
    "ds_read_b64_tr_b16 a[146:147], v241 offset:50960\n"
    "ds_read_b64_tr_b16 a[148:149], v241 offset:51216\n"
    "v_mfma_f32_16x16x32_bf16 v[204:207], a[120:123], a[8:11], v[204:207]\n"
    "s_add_u32 m0, s62, 33856\n"
    "v_exp_f32_e32 v202, v202\n"
    "v_mfma_f32_16x16x32_bf16 v[204:207], a[124:127], a[12:15], v[204:207]\n"
    "buffer_load_dwordx4 v242, s[36:39], 0 offen offset:2048 nt lds\n"
    "ds_read_b64_tr_b16 a[150:151], v241 offset:51472\n"
    "v_mfma_f32_16x16x32_bf16 v[112:115], a[160:163], v[216:219], v[112:115]\n"
    "v_exp_f32_e32 v203, v203\n"
    "v_mfma_f32_16x16x32_bf16 v[112:115], a[164:167], v[220:223], v[112:115]\n"
    "v_cvt_pk_bf16_f32 v212, v200, v201\n"
    "v_cvt_pk_bf16_f32 v213, v202, v203\n"
    "v_mfma_f32_16x16x32_bf16 v[116:119], a[168:171], v[216:219], v[116:119]\n"
    "LW2_%=:\n"
    "s_cbranch_execz LWR_%=\n"
    "v_exp_f32_e32 v204, v204\n"
    "v_mfma_f32_16x16x32_bf16 v[116:119], a[172:175], v[220:223], v[116:119]\n"
    "ds_read_b64_tr_b16 a[152:153], v241 offset:50712\n"
    "ds_read_b64_tr_b16 a[154:155], v241 offset:50968\n"
    "v_mfma_f32_16x16x32_bf16 v[192:195], a[64:67], a[16:19], v[228:231]\n"
    "v_exp_f32_e32 v205, v205\n"
    "v_mfma_f32_16x16x32_bf16 v[192:195], a[68:71], a[20:23], v[192:195]\n"
    "ds_read_b64_tr_b16 a[156:157], v241 offset:51224\n"
    "v_cvt_pk_bf16_f32 v214, v204, v205\n"
    "v_mfma_f32_16x16x32_bf16 v[192:195], a[72:75], a[24:27], v[192:195]\n"
    "v_exp_f32_e32 v206, v206\n"
    "v_mfma_f32_16x16x32_bf16 v[192:195], a[76:79], a[28:31], v[192:195]\n"
    "ds_read_b64_tr_b16 a[158:159], v241 offset:51480\n"
    "ds_read_b64_tr_b16 a[160:161], v241 offset:50816\n"
    "v_mfma_f32_16x16x32_bf16 v[120:123], a[176:179], v[216:219], v[120:123]\n"
    "v_exp_f32_e32 v207, v207\n"
    "v_mfma_f32_16x16x32_bf16 v[120:123], a[180:183], v[220:223], v[120:123]\n"
    "ds_read_b64_tr_b16 a[162:163], v241 offset:51072\n"
    "v_cvt_pk_bf16_f32 v215, v206, v207\n"
    "v_mfma_f32_16x16x32_bf16 v[124:127], a[184:187], v[216:219], v[124:127]\n"
    "s_add_u32 m0, s62, 33888\n"
    "v_exp_f32_e32 v192, v192\n"
    "v_mfma_f32_16x16x32_bf16 v[124:127], a[188:191], v[220:223], v[124:127]\n"
    "buffer_load_dwordx4 v242, s[36:39], 0 offen offset:3072 nt lds\n"
    "ds_read_b64_tr_b16 a[164:165], v241 offset:51328\n"
    "v_mfma_f32_16x16x32_bf16 v[196:199], a[80:83], a[16:19], v[228:231]\n"
    "s_addk_i32 s59, 16384\n"
    "v_exp_f32_e32 v193, v193\n"
    "v_mfma_f32_16x16x32_bf16 v[196:199], a[84:87], a[20:23], v[196:199]\n"
    "s_xor_b32 s71, s59, s63\n"
    "ds_read_b64_tr_b16 a[166:167], v241 offset:51584\n"
    "ds_read_b64_tr_b16 a[168:169], v241 offset:50824\n"
    "v_mfma_f32_16x16x32_bf16 v[196:199], a[88:91], a[24:27], v[196:199]\n"
    "s_add_u32 s36, s84, s71\n"
    "s_addc_u32 s37, s85, 0\n"
    "v_exp_f32_e32 v194, v194\n"
    "v_mfma_f32_16x16x32_bf16 v[196:199], a[92:95], a[28:31], v[196:199]\n"
    "s_waitcnt lgkmcnt(14)\n"
    "ds_read_b64_tr_b16 a[170:171], v241 offset:51080\n"
    "ds_read_b64_tr_b16 a[172:173], v241 offset:51336\n"
    "v_mfma_f32_16x16x32_bf16 v[0:3], a[128:131], v[208:211], v[0:3]\n"
    "s_waitcnt lgkmcnt(14)\n"
    "v_exp_f32_e32 v195, v195\n"
    "v_mfma_f32_16x16x32_bf16 v[0:3], a[132:135], v[212:215], v[0:3]\n"
    "s_waitcnt vmcnt(13)\n"
    "ds_read_b64_tr_b16 a[174:175], v241 offset:51592\n"
    "ds_read_b128 v[144:147], v255 offset:2112\n"
    "v_mfma_f32_16x16x32_bf16 v[4:7], a[136:139], v[208:211], v[4:7]\n"
    "s_addk_i32 s58, -16384\n"
    "v_exp_f32_e32 v196, v196\n"
    "v_mfma_f32_16x16x32_bf16 v[4:7], a[140:143], v[212:215], v[4:7]\n"
    "s_max_i32 s38, s58, 0\n"
    "ds_read_b128 v[148:151], v255 offset:2176\n"
    "ds_read_b64_tr_b16 a[176:177], v241 offset:50832\n"
    "v_mfma_f32_16x16x32_bf16 v[200:203], a[96:99], a[16:19], v[228:231]\n"
    "s_add_u32 m0, s62, 101376\n"
    "v_exp_f32_e32 v197, v197\n"
    "v_mfma_f32_16x16x32_bf16 v[200:203], a[100:103], a[20:23], v[200:203]\n"
    "buffer_load_dwordx4 v242, s[40:43], 0 offen offset:0 nt lds\n"
    "ds_read_b64_tr_b16 a[178:179], v241 offset:51088\n"
    "v_mfma_f32_16x16x32_bf16 v[200:203], a[104:107], a[24:27], v[200:203]\n"
    "v_exp_f32_e32 v198, v198\n"
    "v_mfma_f32_16x16x32_bf16 v[200:203], a[108:111], a[28:31], v[200:203]\n"
    "s_waitcnt lgkmcnt(14)\n"
    "ds_read_b64_tr_b16 a[180:181], v241 offset:51344\n"
    "ds_read_b64_tr_b16 a[182:183], v241 offset:51600\n"
    "v_mfma_f32_16x16x32_bf16 v[8:11], a[144:147], v[208:211], v[8:11]\n"
    "s_waitcnt lgkmcnt(14)\n"
    "v_exp_f32_e32 v199, v199\n"
    "v_mfma_f32_16x16x32_bf16 v[8:11], a[148:151], v[212:215], v[8:11]\n"
    "s_waitcnt lgkmcnt(4)\n"
    "ds_read_b64_tr_b16 a[184:185], v241 offset:50840\n"
    "ds_read_b64_tr_b16 a[186:187], v241 offset:51096\n"
    "v_mfma_f32_16x16x32_bf16 v[12:15], a[152:155], v[208:211], v[12:15]\n"
    "v_exp_f32_e32 v200, v200\n"
    "v_mfma_f32_16x16x32_bf16 v[12:15], a[156:159], v[212:215], v[12:15]\n"
    "ds_read_b64_tr_b16 a[188:189], v241 offset:51352\n"
    "ds_read_b64_tr_b16 a[190:191], v241 offset:51608\n"
    "v_smfmac_f32_16x16x64_bf16 a[192:195], a[208:211], v[208:215], v254\n"
    "v_cvt_pk_bf16_f32 v216, v192, v193\n"
    "v_mfma_f32_16x16x32_bf16 v[204:207], a[112:115], a[16:19], v[228:231]\n"
    "v_exp_f32_e32 v201, v201\n"
    "v_mfma_f32_16x16x32_bf16 v[204:207], a[116:119], a[20:23], v[204:207]\n"
    "s_waitcnt vmcnt(17)\n"
    "ds_append v253\n"
    "v_cvt_pk_bf16_f32 v217, v194, v195\n"
    "v_mfma_f32_16x16x32_bf16 v[204:207], a[120:123], a[24:27], v[204:207]\n"
    "s_add_u32 m0, s62, 101384\n"
    "s_waitcnt lgkmcnt(14)\n"
    "v_exp_f32_e32 v202, v202\n"
    "v_mfma_f32_16x16x32_bf16 v[204:207], a[124:127], a[28:31], v[204:207]\n"
    "buffer_load_dwordx4 v242, s[40:43], 0 offen offset:1024 nt lds\n"
    "v_cvt_pk_bf16_f32 v218, v196, v197\n"
    "v_mfma_f32_16x16x32_bf16 v[16:19], a[160:163], v[208:211], v[16:19]\n"
    "v_exp_f32_e32 v203, v203\n"
    "v_mfma_f32_16x16x32_bf16 v[16:19], a[164:167], v[212:215], v[16:19]\n"
    "s_waitcnt lgkmcnt(11)\n"
    "v_cvt_pk_bf16_f32 v219, v198, v199\n"
    "v_cvt_pk_bf16_f32 v220, v200, v201\n"
    "v_mfma_f32_16x16x32_bf16 v[20:23], a[168:171], v[208:211], v[20:23]\n"
    "v_exp_f32_e32 v204, v204\n"
    "v_mfma_f32_16x16x32_bf16 v[20:23], a[172:175], v[212:215], v[20:23]\n"
    "v_cvt_pk_bf16_f32 v221, v202, v203\n"
    "v_lshlrev_b32_e32 v244, 16, v144\n"
    "v_mfma_f32_16x16x32_bf16 v[192:195], a[64:67], a[32:35], v[232:235]\n"
    "v_exp_f32_e32 v205, v205\n"
    "v_mfma_f32_16x16x32_bf16 v[192:195], a[68:71], a[36:39], v[192:195]\n"
    "v_cvt_pk_bf16_f32 v222, v204, v205\n"
    "v_and_b32_e32 v245, s67, v144\n"
    "v_mfma_f32_16x16x32_bf16 v[192:195], a[72:75], a[40:43], v[192:195]\n"
    "v_exp_f32_e32 v206, v206\n"
    "v_mfma_f32_16x16x32_bf16 v[192:195], a[76:79], a[44:47], v[192:195]\n"
    "s_waitcnt lgkmcnt(5)\n"
    "v_mul_f32_e32 v244, s64, v244\n"
    "v_mul_f32_e32 v245, s64, v245\n"
    "v_mfma_f32_16x16x32_bf16 v[24:27], a[176:179], v[208:211], v[24:27]\n"
    "v_exp_f32_e32 v207, v207\n"
    "v_mfma_f32_16x16x32_bf16 v[24:27], a[180:183], v[212:215], v[24:27]\n"
    "s_waitcnt lgkmcnt(1)\n"
    "v_cvt_pk_bf16_f32 v223, v206, v207\n"
    "v_cvt_pk_bf16_f32 v144, v244, v245\n"
    "v_mfma_f32_16x16x32_bf16 v[28:31], a[184:187], v[208:211], v[28:31]\n"
    "v_exp_f32_e32 v192, v192\n"
    "v_mfma_f32_16x16x32_bf16 v[28:31], a[188:191], v[212:215], v[28:31]\n"
    "ds_read_b32 v253, v251\n"
    "v_lshlrev_b32_e32 v244, 16, v145\n"
    "v_mfma_f32_16x16x32_bf16 v[196:199], a[80:83], a[32:35], v[232:235]\n"
    "s_add_u32 m0, s62, 101392\n"
    "v_exp_f32_e64 v193, v193\n"
    "v_mfma_f32_16x16x32_bf16 v[196:199], a[84:87], a[36:39], v[196:199]\n"
    "buffer_load_dwordx4 v242, s[40:43], 0 offen offset:2048 nt lds\n"
    "v_cvt_pk_bf16_f32 v208, v192, v193\n"
    "v_and_b32_e32 v245, s67, v145\n"
    "v_mul_f32_e32 v244, s64, v244\n"
    "v_mul_f32_e64 v245, s64, v245\n"
    "v_mfma_f32_16x16x32_bf16 v[196:199], a[88:91], a[40:43], v[196:199]\n"
    "v_exp_f32_e32 v194, v194\n"
    "v_mfma_f32_16x16x32_bf16 v[196:199], a[92:95], a[44:47], v[196:199]\n"
    "v_cvt_pk_bf16_f32 v145, v244, v245\n"
    "v_lshlrev_b32_e32 v244, 16, v146\n"
    "v_mfma_f32_16x16x32_bf16 v[32:35], a[128:131], v[216:219], v[32:35]\n"
    "v_exp_f32_e32 v195, v195\n"
    "v_mfma_f32_16x16x32_bf16 v[32:35], a[132:135], v[220:223], v[32:35]\n"
    "s_waitcnt lgkmcnt(0)\n"
    "v_cmp_le_u32_e32 vcc, s76, v253\n"
    "v_cvt_pk_bf16_f32 v209, v194, v195\n"
    "v_mfma_f32_16x16x32_bf16 v[36:39], a[136:139], v[216:219], v[36:39]\n"
    "v_exp_f32_e32 v196, v196\n"
    "v_mfma_f32_16x16x32_bf16 v[36:39], a[140:143], v[220:223], v[36:39]\n"
    "v_and_b32_e32 v245, s67, v146\n"
    "v_mul_f32_e32 v244, s64, v244\n"
    "v_mfma_f32_16x16x32_bf16 v[200:203], a[96:99], a[32:35], v[232:235]\n"
    "s_cbranch_vccz LSPIN2_%=\n"
    "LRET2_%=:\n"
    "v_exp_f32_e32 v197, v197\n"
    "v_mfma_f32_16x16x32_bf16 v[200:203], a[100:103], a[36:39], v[200:203]\n"
    "s_addk_i32 s76, 256\n"
    "v_cvt_pk_bf16_f32 v210, v196, v197\n"
    "v_mul_f32_e32 v245, s64, v245\n"
    "v_mfma_f32_16x16x32_bf16 v[200:203], a[104:107], a[40:43], v[200:203]\n"
    "s_add_u32 m0, s62, 101400\n"
    "v_exp_f32_e32 v198, v198\n"
    "v_mfma_f32_16x16x32_bf16 v[200:203], a[108:111], a[44:47], v[200:203]\n"
    "buffer_load_dwordx4 v242, s[40:43], 0 offen offset:3072 nt lds\n"
    "v_cvt_pk_bf16_f32 v146, v244, v245\n"
    "v_mfma_f32_16x16x32_bf16 v[40:43], a[144:147], v[216:219], v[40:43]\n"
    "s_add_u32 s40, s60, s71\n"
    "s_addc_u32 s41, s61, 0\n"
    "v_exp_f32_e32 v199, v199\n"
    "v_mfma_f32_16x16x32_bf16 v[40:43], a[148:151], v[220:223], v[40:43]\n"
    "s_max_i32 s42, s58, 0\n"
    "v_cvt_pk_bf16_f32 v211, v198, v199\n"
    "v_lshlrev_b32_e32 v244, 16, v147\n"
    "v_mfma_f32_16x16x32_bf16 v[44:47], a[152:155], v[216:219], v[44:47]\n"
    "v_exp_f32_e32 v200, v200\n"
    "v_mfma_f32_16x16x32_bf16 v[44:47], a[156:159], v[220:223], v[44:47]\n"
    "v_and_b32_e32 v245, s67, v147\n"
    "v_mul_f32_e32 v244, s64, v244\n"
    "v_smfmac_f32_16x16x64_bf16 a[196:199], a[208:211], v[216:223], v254\n"
    "v_mul_f32_e32 v245, s64, v245\n"
    "v_mfma_f32_16x16x32_bf16 v[204:207], a[112:115], a[32:35], v[232:235]\n"
    "v_exp_f32_e32 v201, v201\n"
    "v_mfma_f32_16x16x32_bf16 v[204:207], a[116:119], a[36:39], v[204:207]\n"
    "v_cvt_pk_bf16_f32 v212, v200, v201\n"
    "v_cvt_pk_bf16_f32 v147, v244, v245\n"
    "v_mfma_f32_16x16x32_bf16 v[204:207], a[120:123], a[40:43], v[204:207]\n"
    "v_exp_f32_e32 v202, v202\n"
    "v_mfma_f32_16x16x32_bf16 v[204:207], a[124:127], a[44:47], v[204:207]\n"
    "v_lshlrev_b32_e32 v244, 16, v148\n"
    "v_and_b32_e32 v245, s67, v148\n"
    "v_mfma_f32_16x16x32_bf16 v[48:51], a[160:163], v[216:219], v[48:51]\n"
    "s_add_u32 m0, s62, 50688\n"
    "v_exp_f32_e32 v203, v203\n"
    "v_mfma_f32_16x16x32_bf16 v[48:51], a[164:167], v[220:223], v[48:51]\n"
    "buffer_load_dwordx4 v242, s[36:39], 0 offen offset:0 nt lds\n"
    "v_cvt_pk_bf16_f32 v213, v202, v203\n"
    "v_mfma_f32_16x16x32_bf16 v[52:55], a[168:171], v[216:219], v[52:55]\n"
    "v_exp_f32_e32 v204, v204\n"
    "v_mfma_f32_16x16x32_bf16 v[52:55], a[172:175], v[220:223], v[52:55]\n"
    "v_mul_f32_e32 v244, s64, v244\n"
    "v_mul_f32_e32 v245, s64, v245\n"
    "v_mfma_f32_16x16x32_bf16 v[192:195], a[64:67], a[48:51], v[236:239]\n"
    "v_exp_f32_e32 v205, v205\n"
    "v_mfma_f32_16x16x32_bf16 v[192:195], a[68:71], a[52:55], v[192:195]\n"
    "ds_read_b128 a[64:67], v240 offset:0\n"
    "v_cvt_pk_bf16_f32 v214, v204, v205\n"
    "v_mfma_f32_16x16x32_bf16 v[192:195], a[72:75], a[56:59], v[192:195]\n"
    "v_exp_f32_e32 v206, v206\n"
    "v_mfma_f32_16x16x32_bf16 v[192:195], a[76:79], a[60:63], v[192:195]\n"
    "ds_read_b128 a[68:71], v240 offset:64\n"
    "ds_read_b128 a[72:75], v240 offset:128\n"
    "v_mfma_f32_16x16x32_bf16 v[56:59], a[176:179], v[216:219], v[56:59]\n"
    "v_exp_f32_e32 v207, v207\n"
    "v_mfma_f32_16x16x32_bf16 v[56:59], a[180:183], v[220:223], v[56:59]\n"
    "ds_read_b128 a[76:79], v240 offset:192\n"
    "v_cvt_pk_bf16_f32 v215, v206, v207\n"
    "v_mfma_f32_16x16x32_bf16 v[60:63], a[184:187], v[216:219], v[60:63]\n"
    "s_add_u32 m0, s95, 2112\n"
    "v_exp_f32_e32 v192, v192\n"
    "v_mfma_f32_16x16x32_bf16 v[60:63], a[188:191], v[220:223], v[60:63]\n"
    "buffer_load_dwordx4 v243, s[44:47], 0 offen offset:0 lds\n"
    "v_cvt_pk_bf16_f32 v148, v244, v245\n"
    "v_mfma_f32_16x16x32_bf16 v[196:199], a[80:83], a[48:51], v[236:239]\n"
    "v_exp_f32_e32 v193, v193\n"
    "v_mfma_f32_16x16x32_bf16 v[196:199], a[84:87], a[52:55], v[196:199]\n"
    "ds_read_b128 a[80:83], v240 offset:256\n"
    "v_cvt_pk_bf16_f32 v216, v192, v193\n"
    "v_mfma_f32_16x16x32_bf16 v[196:199], a[88:91], a[56:59], v[196:199]\n"
    "v_exp_f32_e32 v194, v194\n"
    "v_mfma_f32_16x16x32_bf16 v[196:199], a[92:95], a[60:63], v[196:199]\n"
    "ds_read_b128 a[84:87], v240 offset:320\n"
    "ds_read_b128 a[88:91], v240 offset:384\n"
    "v_mfma_f32_16x16x32_bf16 v[64:67], a[128:131], v[208:211], v[64:67]\n"
    "v_exp_f32_e32 v195, v195\n"
    "v_mfma_f32_16x16x32_bf16 v[64:67], a[132:135], v[212:215], v[64:67]\n"
    "ds_read_b128 a[92:95], v240 offset:448\n"
    "v_cvt_pk_bf16_f32 v217, v194, v195\n"
    "v_mfma_f32_16x16x32_bf16 v[68:71], a[136:139], v[208:211], v[68:71]\n"
    "v_exp_f32_e32 v196, v196\n"
    "v_mfma_f32_16x16x32_bf16 v[68:71], a[140:143], v[212:215], v[68:71]\n"
    "v_lshlrev_b32_e32 v244, 16, v149\n"
    "v_and_b32_e32 v245, s67, v149\n"
    "v_mfma_f32_16x16x32_bf16 v[200:203], a[96:99], a[48:51], v[236:239]\n"
    "s_add_u32 m0, s95, 1152\n"
    "v_exp_f32_e32 v197, v197\n"
    "v_mfma_f32_16x16x32_bf16 v[200:203], a[100:103], a[52:55], v[200:203]\n"
    "buffer_load_dwordx4 v243, s[44:47], 0 offen offset:2048 lds\n"
    "ds_read_b128 a[96:99], v240 offset:512\n"
    "v_mfma_f32_16x16x32_bf16 v[200:203], a[104:107], a[56:59], v[200:203]\n"
    "v_exp_f32_e32 v198, v198\n"
    "v_mfma_f32_16x16x32_bf16 v[200:203], a[108:111], a[60:63], v[200:203]\n"
    "ds_read_b128 a[100:103], v240 offset:576\n"
    "v_cvt_pk_bf16_f32 v218, v196, v197\n"
    "v_mfma_f32_16x16x32_bf16 v[72:75], a[144:147], v[208:211], v[72:75]\n"
    "v_exp_f32_e32 v199, v199\n"
    "v_mfma_f32_16x16x32_bf16 v[72:75], a[148:151], v[212:215], v[72:75]\n"
    "ds_read_b128 a[104:107], v240 offset:640\n"
    "ds_read_b128 a[108:111], v240 offset:704\n"
    "v_mfma_f32_16x16x32_bf16 v[76:79], a[152:155], v[208:211], v[76:79]\n"
    "v_exp_f32_e32 v200, v200\n"
    "v_mfma_f32_16x16x32_bf16 v[76:79], a[156:159], v[212:215], v[76:79]\n"
    "v_cvt_pk_bf16_f32 v219, v198, v199\n"
    "v_mul_f32_e32 v244, s64, v244\n"
    "v_smfmac_f32_16x16x64_bf16 a[200:203], a[208:211], v[208:215], v254\n"
    "v_mul_f32_e32 v245, s64, v245\n"
    "v_mfma_f32_16x16x32_bf16 v[204:207], a[112:115], a[48:51], v[236:239]\n"
    "v_exp_f32_e32 v201, v201\n"
    "v_mfma_f32_16x16x32_bf16 v[204:207], a[116:119], a[52:55], v[204:207]\n"
    "ds_read_b128 a[112:115], v240 offset:768\n"
    "v_cvt_pk_bf16_f32 v220, v200, v201\n"
    "v_mfma_f32_16x16x32_bf16 v[204:207], a[120:123], a[56:59], v[204:207]\n"
    "s_add_u32 m0, s62, 50720\n"
    "v_exp_f32_e32 v202, v202\n"
    "v_mfma_f32_16x16x32_bf16 v[204:207], a[124:127], a[60:63], v[204:207]\n"
    "buffer_load_dwordx4 v242, s[36:39], 0 offen offset:1024 nt lds\n"
    "ds_read_b128 a[116:119], v240 offset:832\n"
    "v_mfma_f32_16x16x32_bf16 v[80:83], a[160:163], v[208:211], v[80:83]\n"
    "v_exp_f32_e32 v203, v203\n"
    "v_mfma_f32_16x16x32_bf16 v[80:83], a[164:167], v[212:215], v[80:83]\n"
    "ds_read_b128 a[120:123], v240 offset:896\n"
    "v_cvt_pk_bf16_f32 v221, v202, v203\n"
    "v_mfma_f32_16x16x32_bf16 v[84:87], a[168:171], v[208:211], v[84:87]\n"
    "v_exp_f32_e32 v204, v204\n"
    "v_mfma_f32_16x16x32_bf16 v[84:87], a[172:175], v[212:215], v[84:87]\n"
    "s_waitcnt lgkmcnt(11)\n"
    "ds_read_b128 a[124:127], v240 offset:960\n"
    "v_cvt_pk_bf16_f32 v149, v244, v245\n"
    "v_mfma_f32_16x16x32_bf16 v[192:195], a[64:67], a[0:3], v[224:227]\n"
    "v_exp_f32_e32 v205, v205\n"
    "v_mfma_f32_16x16x32_bf16 v[192:195], a[68:71], a[4:7], v[192:195]\n"
    "v_cvt_pk_bf16_f32 v222, v204, v205\n"
    "v_lshlrev_b32_e32 v244, 16, v150\n"
    "v_mfma_f32_16x16x32_bf16 v[192:195], a[72:75], a[8:11], v[192:195]\n"
    "v_exp_f32_e32 v206, v206\n"
    "v_mfma_f32_16x16x32_bf16 v[192:195], a[76:79], a[12:15], v[192:195]\n"
    "v_and_b32_e32 v245, s67, v150\n"
    "v_mul_f32_e32 v244, s64, v244\n"
    "v_mfma_f32_16x16x32_bf16 v[88:91], a[176:179], v[208:211], v[88:91]\n"
    "v_exp_f32_e32 v207, v207\n"
    "v_mfma_f32_16x16x32_bf16 v[88:91], a[180:183], v[212:215], v[88:91]\n"
    "v_cvt_pk_bf16_f32 v223, v206, v207\n"
    "v_mul_f32_e32 v245, s64, v245\n"
    "v_mfma_f32_16x16x32_bf16 v[92:95], a[184:187], v[208:211], v[92:95]\n"
    "s_add_u32 m0, s62, 50752\n"
    "s_waitcnt lgkmcnt(8)\n"
    "v_exp_f32_e32 v192, v192\n"
    "v_mfma_f32_16x16x32_bf16 v[92:95], a[188:191], v[212:215], v[92:95]\n"
    "buffer_load_dwordx4 v242, s[36:39], 0 offen offset:2048 nt lds\n"
    "v_cvt_pk_bf16_f32 v150, v244, v245\n"
    "v_lshlrev_b32_e32 v244, 16, v151\n"
    "v_and_b32_e32 v245, s67, v151\n"
    "v_mul_f32_e64 v244, s64, v244\n"
    "v_mfma_f32_16x16x32_bf16 v[196:199], a[80:83], a[0:3], v[224:227]\n"
    "v_exp_f32_e32 v193, v193\n"
    "v_mfma_f32_16x16x32_bf16 v[196:199], a[84:87], a[4:7], v[196:199]\n"
    "v_cvt_pk_bf16_f32 v208, v192, v193\n"
    "v_mul_f32_e32 v245, s64, v245\n"
    "v_mfma_f32_16x16x32_bf16 v[196:199], a[88:91], a[8:11], v[196:199]\n"
    "v_exp_f32_e32 v194, v194\n"
    "v_mfma_f32_16x16x32_bf16 v[196:199], a[92:95], a[12:15], v[196:199]\n"
    "v_cvt_pk_bf16_f32 v151, v244, v245\n"
    "v_mfma_f32_16x16x32_bf16 v[96:99], a[128:131], v[216:219], v[96:99]\n"
    "v_exp_f32_e32 v195, v195\n"
    "v_mfma_f32_16x16x32_bf16 v[96:99], a[132:135], v[220:223], v[96:99]\n"
    "ds_read_b64_tr_b16 a[128:129], v241 offset:0\n"
    "v_cvt_pk_bf16_f32 v209, v194, v195\n"
    "v_mfma_f32_16x16x32_bf16 v[100:103], a[136:139], v[216:219], v[100:103]\n"
    "v_exp_f32_e32 v196, v196\n"
    "v_mfma_f32_16x16x32_bf16 v[100:103], a[140:143], v[220:223], v[100:103]\n"
    "s_waitcnt lgkmcnt(5)\n"
    "ds_read_b64_tr_b16 a[130:131], v241 offset:256\n"
    "ds_read_b64_tr_b16 a[132:133], v241 offset:512\n"
    "v_mfma_f32_16x16x32_bf16 v[200:203], a[96:99], a[0:3], v[224:227]\n"
    "s_add_u32 m0, s62, 50784\n"
    "v_exp_f32_e32 v197, v197\n"
    "v_mfma_f32_16x16x32_bf16 v[200:203], a[100:103], a[4:7], v[200:203]\n"
    "buffer_load_dwordx4 v242, s[36:39], 0 offen offset:3072 nt lds\n"
    "ds_read_b64_tr_b16 a[134:135], v241 offset:768\n"
    "v_mfma_f32_16x16x32_bf16 v[200:203], a[104:107], a[8:11], v[200:203]\n"
    "s_addk_i32 s59, 16384\n"
    "v_exp_f32_e32 v198, v198\n"
    "v_mfma_f32_16x16x32_bf16 v[200:203], a[108:111], a[12:15], v[200:203]\n"
    "s_xor_b32 s71, s59, s63\n"
    "ds_read_b64_tr_b16 a[136:137], v241 offset:8\n"
    "ds_read_b64_tr_b16 a[138:139], v241 offset:264\n"
    "v_mfma_f32_16x16x32_bf16 v[104:107], a[144:147], v[216:219], v[104:107]\n"
    "s_add_u32 s36, s84, s71\n"
    "s_addc_u32 s37, s85, 0\n"
    "v_exp_f32_e32 v199, v199\n"
    "v_mfma_f32_16x16x32_bf16 v[104:107], a[148:151], v[220:223], v[104:107]\n"
    "s_addk_i32 s58, -16384\n"
    "ds_read_b64_tr_b16 a[140:141], v241 offset:520\n"
    "ds_read_b64_tr_b16 a[142:143], v241 offset:776\n"
    "v_mfma_f32_16x16x32_bf16 v[108:111], a[152:155], v[216:219], v[108:111]\n"
    "s_max_i32 s38, s58, 0\n"
    "v_exp_f32_e32 v200, v200\n"
    "v_mfma_f32_16x16x32_bf16 v[108:111], a[156:159], v[220:223], v[108:111]\n"
    "v_cvt_pk_bf16_f32 v210, v196, v197\n"
    "v_cvt_pk_bf16_f32 v211, v198, v199\n"
    "v_smfmac_f32_16x16x64_bf16 a[204:207], a[208:211], v[216:223], v254\n"
    "s_waitcnt lgkmcnt(8)\n"
    "ds_read_b64_tr_b16 a[144:145], v241 offset:16\n"
    "v_mfma_f32_16x16x32_bf16 v[204:207], a[112:115], a[0:3], v[224:227]\n"
    "v_exp_f32_e32 v201, v201\n"
    "v_mfma_f32_16x16x32_bf16 v[204:207], a[116:119], a[4:7], v[204:207]\n"
    "ds_read_b64_tr_b16 a[146:147], v241 offset:272\n"
    "ds_read_b64_tr_b16 a[148:149], v241 offset:528\n"
    "v_mfma_f32_16x16x32_bf16 v[204:207], a[120:123], a[8:11], v[204:207]\n"
    "s_add_u32 m0, s62, 118272\n"
    "v_exp_f32_e32 v202, v202\n"
    "v_mfma_f32_16x16x32_bf16 v[204:207], a[124:127], a[12:15], v[204:207]\n"
    "buffer_load_dwordx4 v242, s[40:43], 0 offen offset:0 nt lds\n"
    "ds_read_b64_tr_b16 a[150:151], v241 offset:784\n"
    "v_mfma_f32_16x16x32_bf16 v[112:115], a[160:163], v[216:219], v[112:115]\n"
    "v_exp_f32_e32 v203, v203\n"
    "v_mfma_f32_16x16x32_bf16 v[112:115], a[164:167], v[220:223], v[112:115]\n"
    "v_cvt_pk_bf16_f32 v212, v200, v201\n"
    "v_cvt_pk_bf16_f32 v213, v202, v203\n"
    "v_mfma_f32_16x16x32_bf16 v[116:119], a[168:171], v[216:219], v[116:119]\n"
    "v_exp_f32_e32 v204, v204\n"
    "v_mfma_f32_16x16x32_bf16 v[116:119], a[172:175], v[220:223], v[116:119]\n"
    "ds_read_b64_tr_b16 a[152:153], v241 offset:24\n"
    "ds_read_b64_tr_b16 a[154:155], v241 offset:280\n"
    "v_mfma_f32_16x16x32_bf16 v[192:195], a[64:67], a[16:19], v[228:231]\n"
    "v_exp_f32_e32 v205, v205\n"
    "v_mfma_f32_16x16x32_bf16 v[192:195], a[68:71], a[20:23], v[192:195]\n"
    "ds_read_b64_tr_b16 a[156:157], v241 offset:536\n"
    "v_cvt_pk_bf16_f32 v214, v204, v205\n"
    "v_mfma_f32_16x16x32_bf16 v[192:195], a[72:75], a[24:27], v[192:195]\n"
    "v_exp_f32_e32 v206, v206\n"
    "v_mfma_f32_16x16x32_bf16 v[192:195], a[76:79], a[28:31], v[192:195]\n"
    "ds_read_b64_tr_b16 a[158:159], v241 offset:792\n"
    "ds_read_b64_tr_b16 a[160:161], v241 offset:128\n"
    "v_mfma_f32_16x16x32_bf16 v[120:123], a[176:179], v[216:219], v[120:123]\n"
    "v_exp_f32_e32 v207, v207\n"
    "v_mfma_f32_16x16x32_bf16 v[120:123], a[180:183], v[220:223], v[120:123]\n"
    "ds_read_b64_tr_b16 a[162:163], v241 offset:384\n"
    "v_cvt_pk_bf16_f32 v215, v206, v207\n"
    "v_mfma_f32_16x16x32_bf16 v[124:127], a[184:187], v[216:219], v[124:127]\n"
    "s_add_u32 m0, s62, 118280\n"
    "v_exp_f32_e32 v192, v192\n"
    "v_mfma_f32_16x16x32_bf16 v[124:127], a[188:191], v[220:223], v[124:127]\n"
    "buffer_load_dwordx4 v242, s[40:43], 0 offen offset:1024 nt lds\n"
    "ds_read_b64_tr_b16 a[164:165], v241 offset:640\n"
    "v_mfma_f32_16x16x32_bf16 v[196:199], a[80:83], a[16:19], v[228:231]\n"
    "v_exp_f32_e32 v193, v193\n"
    "v_mfma_f32_16x16x32_bf16 v[196:199], a[84:87], a[20:23], v[196:199]\n"
    "ds_read_b64_tr_b16 a[166:167], v241 offset:896\n"
    "ds_read_b64_tr_b16 a[168:169], v241 offset:136\n"
    "v_mfma_f32_16x16x32_bf16 v[196:199], a[88:91], a[24:27], v[196:199]\n"
    "v_exp_f32_e32 v194, v194\n"
    "v_mfma_f32_16x16x32_bf16 v[196:199], a[92:95], a[28:31], v[196:199]\n"
    "s_waitcnt lgkmcnt(14)\n"
    "ds_read_b64_tr_b16 a[170:171], v241 offset:392\n"
    "ds_read_b64_tr_b16 a[172:173], v241 offset:648\n"
    "v_mfma_f32_16x16x32_bf16 v[0:3], a[128:131], v[208:211], v[0:3]\n"
    "s_waitcnt lgkmcnt(14)\n"
    "v_exp_f32_e32 v195, v195\n"
    "v_mfma_f32_16x16x32_bf16 v[0:3], a[132:135], v[212:215], v[0:3]\n"
    "s_waitcnt vmcnt(15)\n"
    "ds_read_b64_tr_b16 a[174:175], v241 offset:904\n"
    "ds_read_b128 v[152:155], v255 offset:0\n"
    "v_mfma_f32_16x16x32_bf16 v[4:7], a[136:139], v[208:211], v[4:7]\n"
    "v_exp_f32_e32 v196, v196\n"
    "v_mfma_f32_16x16x32_bf16 v[4:7], a[140:143], v[212:215], v[4:7]\n"
    "ds_read_b128 v[156:159], v255 offset:64\n"
    "ds_read_b64_tr_b16 a[176:177], v241 offset:144\n"
    "v_mfma_f32_16x16x32_bf16 v[200:203], a[96:99], a[16:19], v[228:231]\n"
    "s_add_u32 m0, s62, 118288\n"
    "v_exp_f32_e32 v197, v197\n"
    "v_mfma_f32_16x16x32_bf16 v[200:203], a[100:103], a[20:23], v[200:203]\n"
    "buffer_load_dwordx4 v242, s[40:43], 0 offen offset:2048 nt lds\n"
    "ds_read_b64_tr_b16 a[178:179], v241 offset:400\n"
    "v_mfma_f32_16x16x32_bf16 v[200:203], a[104:107], a[24:27], v[200:203]\n"
    "v_exp_f32_e32 v198, v198\n"
    "v_mfma_f32_16x16x32_bf16 v[200:203], a[108:111], a[28:31], v[200:203]\n"
    "s_waitcnt lgkmcnt(14)\n"
    "ds_read_b64_tr_b16 a[180:181], v241 offset:656\n"
    "ds_read_b64_tr_b16 a[182:183], v241 offset:912\n"
    "v_mfma_f32_16x16x32_bf16 v[8:11], a[144:147], v[208:211], v[8:11]\n"
    "s_waitcnt lgkmcnt(14)\n"
    "v_exp_f32_e32 v199, v199\n"
    "v_mfma_f32_16x16x32_bf16 v[8:11], a[148:151], v[212:215], v[8:11]\n"
    "s_waitcnt lgkmcnt(4)\n"
    "ds_read_b64_tr_b16 a[184:185], v241 offset:152\n"
    "ds_read_b64_tr_b16 a[186:187], v241 offset:408\n"
    "v_mfma_f32_16x16x32_bf16 v[12:15], a[152:155], v[208:211], v[12:15]\n"
    "v_exp_f32_e32 v200, v200\n"
    "v_mfma_f32_16x16x32_bf16 v[12:15], a[156:159], v[212:215], v[12:15]\n"
    "ds_read_b64_tr_b16 a[188:189], v241 offset:664\n"
    "ds_read_b64_tr_b16 a[190:191], v241 offset:920\n"
    "v_smfmac_f32_16x16x64_bf16 a[192:195], a[208:211], v[208:215], v254\n"
    "v_cvt_pk_bf16_f32 v216, v192, v193\n"
    "v_mfma_f32_16x16x32_bf16 v[204:207], a[112:115], a[16:19], v[228:231]\n"
    "v_exp_f32_e32 v201, v201\n"
    "v_mfma_f32_16x16x32_bf16 v[204:207], a[116:119], a[20:23], v[204:207]\n"
    "s_waitcnt vmcnt(19)\n"
    "ds_append v253\n"
    "v_cvt_pk_bf16_f32 v217, v194, v195\n"
    "v_mfma_f32_16x16x32_bf16 v[204:207], a[120:123], a[24:27], v[204:207]\n"
    "s_add_u32 m0, s62, 118296\n"
    "s_waitcnt lgkmcnt(14)\n"
    "v_exp_f32_e32 v202, v202\n"
    "v_mfma_f32_16x16x32_bf16 v[204:207], a[124:127], a[28:31], v[204:207]\n"
    "buffer_load_dwordx4 v242, s[40:43], 0 offen offset:3072 nt lds\n"
    "v_cvt_pk_bf16_f32 v218, v196, v197\n"
    "v_mfma_f32_16x16x32_bf16 v[16:19], a[160:163], v[208:211], v[16:19]\n"
    "s_add_u32 s40, s60, s71\n"
    "s_addc_u32 s41, s61, 0\n"
    "v_exp_f32_e32 v203, v203\n"
    "v_mfma_f32_16x16x32_bf16 v[16:19], a[164:167], v[212:215], v[16:19]\n"
    "s_waitcnt lgkmcnt(11)\n"
    "v_cvt_pk_bf16_f32 v219, v198, v199\n"
    "v_cvt_pk_bf16_f32 v220, v200, v201\n"
    "v_mfma_f32_16x16x32_bf16 v[20:23], a[168:171], v[208:211], v[20:23]\n"
    "s_max_i32 s42, s58, 0\n"
    "v_exp_f32_e32 v204, v204\n"
    "v_mfma_f32_16x16x32_bf16 v[20:23], a[172:175], v[212:215], v[20:23]\n"
    "v_cvt_pk_bf16_f32 v221, v202, v203\n"
    "v_lshlrev_b32_e32 v244, 16, v152\n"
    "v_mfma_f32_16x16x32_bf16 v[192:195], a[64:67], a[32:35], v[232:235]\n"
    "v_exp_f32_e32 v205, v205\n"
    "v_mfma_f32_16x16x32_bf16 v[192:195], a[68:71], a[36:39], v[192:195]\n"
    "v_cvt_pk_bf16_f32 v222, v204, v205\n"
    "v_and_b32_e32 v245, s67, v152\n"
    "v_mfma_f32_16x16x32_bf16 v[192:195], a[72:75], a[40:43], v[192:195]\n"
    "v_exp_f32_e32 v206, v206\n"
    "v_mfma_f32_16x16x32_bf16 v[192:195], a[76:79], a[44:47], v[192:195]\n"
    "s_waitcnt lgkmcnt(5)\n"
    "v_mul_f32_e32 v244, s64, v244\n"
    "v_mul_f32_e32 v245, s64, v245\n"
    "v_mfma_f32_16x16x32_bf16 v[24:27], a[176:179], v[208:211], v[24:27]\n"
    "v_exp_f32_e32 v207, v207\n"
    "v_mfma_f32_16x16x32_bf16 v[24:27], a[180:183], v[212:215], v[24:27]\n"
    "s_waitcnt lgkmcnt(1)\n"
    "v_cvt_pk_bf16_f32 v223, v206, v207\n"
    "v_cvt_pk_bf16_f32 v152, v244, v245\n"
    "s_branch LBODY_%=\n"
    ".p2align 12\n"
    "LW3_%=:\n"
    "s_setpc_b64 s[72:73]\n"
    "s_nop 0\n"
    "LTAIL_%=:\n"
    "v_mfma_f32_16x16x32_bf16 v[192:195], a[64:67], a[0:3], v[224:227]\n"
    "s_waitcnt vmcnt(18)\n"
    "v_exp_f32_e32 v205, v205\n"
    "v_mfma_f32_16x16x32_bf16 v[192:195], a[68:71], a[4:7], v[192:195]\n"
    "ds_append v253\n"
    "v_cvt_pk_bf16_f32 v222, v204, v205\n"
    "v_mfma_f32_16x16x32_bf16 v[192:195], a[72:75], a[8:11], v[192:195]\n"
    "v_exp_f32_e32 v206, v206\n"
    "v_mfma_f32_16x16x32_bf16 v[192:195], a[76:79], a[12:15], v[192:195]\n"
    "buffer_store_dwordx4 v[180:183], v250, s[48:51], 0 offen offset:256 sc0 sc1 nt\n"
    "v_mfma_f32_16x16x32_bf16 v[88:91], a[176:179], v[208:211], v[88:91]\n"
    "v_exp_f32_e32 v207, v207\n"
    "v_mfma_f32_16x16x32_bf16 v[88:91], a[180:183], v[212:215], v[88:91]\n"
    "v_cvt_pk_bf16_f32 v223, v206, v207\n"
    "v_mfma_f32_16x16x32_bf16 v[92:95], a[184:187], v[208:211], v[92:95]\n"
    "s_waitcnt lgkmcnt(9)\n"
    "v_exp_f32_e32 v192, v192\n"
    "v_mfma_f32_16x16x32_bf16 v[92:95], a[188:191], v[212:215], v[92:95]\n"
    "buffer_store_dwordx4 v[184:187], v250, s[48:51], 0 offen offset:128 sc0 sc1 nt\n"
    "v_mfma_f32_16x16x32_bf16 v[196:199], a[80:83], a[0:3], v[224:227]\n"
    "v_exp_f32_e32 v193, v193\n"
    "v_mfma_f32_16x16x32_bf16 v[196:199], a[84:87], a[4:7], v[196:199]\n"
    "v_cvt_pk_bf16_f32 v208, v192, v193\n"
    "v_mfma_f32_16x16x32_bf16 v[196:199], a[88:91], a[8:11], v[196:199]\n"
    "v_exp_f32_e32 v194, v194\n"
    "v_mfma_f32_16x16x32_bf16 v[196:199], a[92:95], a[12:15], v[196:199]\n"
    "buffer_store_dwordx4 v[188:191], v250, s[48:51], 0 offen offset:384 sc0 sc1 nt\n"
    "v_mfma_f32_16x16x32_bf16 v[96:99], a[128:131], v[216:219], v[96:99]\n"
    "v_exp_f32_e32 v195, v195\n"
    "v_mfma_f32_16x16x32_bf16 v[96:99], a[132:135], v[220:223], v[96:99]\n"
    "ds_read_b32 v253, v251\n"
    "v_cvt_pk_bf16_f32 v209, v194, v195\n"
    "v_mfma_f32_16x16x32_bf16 v[100:103], a[136:139], v[216:219], v[100:103]\n"
    "s_waitcnt lgkmcnt(6)\n"
    "v_exp_f32_e32 v196, v196\n"
    "v_mfma_f32_16x16x32_bf16 v[100:103], a[140:143], v[220:223], v[100:103]\n"
    "s_add_u32 s48, s48, 204800\n"
    "ds_read_b64_tr_b16 a[128:129], v241 offset:33792\n"
    "ds_read_b64_tr_b16 a[130:131], v241 offset:34048\n"
    "v_mfma_f32_16x16x32_bf16 v[200:203], a[96:99], a[0:3], v[224:227]\n"
    "s_addc_u32 s49, s49, 0\n"
    "v_exp_f32_e32 v197, v197\n"
    "v_mfma_f32_16x16x32_bf16 v[200:203], a[100:103], a[4:7], v[200:203]\n"
    "s_mov_b32 s50, s52\n"
    "ds_read_b64_tr_b16 a[132:133], v241 offset:34304\n"
    "ds_read_b64_tr_b16 a[134:135], v241 offset:34560\n"
    "v_mfma_f32_16x16x32_bf16 v[200:203], a[104:107], a[8:11], v[200:203]\n"
    "s_waitcnt lgkmcnt(14)\n"
    "v_exp_f32_e32 v198, v198\n"
    "v_mfma_f32_16x16x32_bf16 v[200:203], a[108:111], a[12:15], v[200:203]\n"
    "s_waitcnt lgkmcnt(4)\n"
    "ds_read_b64_tr_b16 a[136:137], v241 offset:33800\n"
    "v_cmp_le_u32_e32 vcc, s76, v253\n"
    "v_mfma_f32_16x16x32_bf16 v[104:107], a[144:147], v[216:219], v[104:107]\n"
    "v_exp_f32_e32 v199, v199\n"
    "v_mfma_f32_16x16x32_bf16 v[104:107], a[148:151], v[220:223], v[104:107]\n"
    "ds_read_b64_tr_b16 a[138:139], v241 offset:34056\n"
    "ds_read_b64_tr_b16 a[140:141], v241 offset:34312\n"
    "v_mfma_f32_16x16x32_bf16 v[108:111], a[152:155], v[216:219], v[108:111]\n"
    "s_cbranch_vccz LSPIN11_%=\n"
    "LRET11_%=:\n"
    "v_exp_f32_e32 v200, v200\n"
    "v_mfma_f32_16x16x32_bf16 v[108:111], a[156:159], v[220:223], v[108:111]\n"
    "s_addk_i32 s76, 256\n"
    "ds_read_b64_tr_b16 a[142:143], v241 offset:34568\n"
    "v_cvt_pk_bf16_f32 v210, v196, v197\n"
    "v_smfmac_f32_16x16x64_bf16 a[204:207], a[208:211], v[216:223], v254\n"
    "s_waitcnt lgkmcnt(10)\n"
    "v_cvt_pk_bf16_f32 v211, v198, v199\n"
    "v_mfma_f32_16x16x32_bf16 v[204:207], a[112:115], a[0:3], v[224:227]\n"
    "v_exp_f32_e32 v201, v201\n"
    "v_mfma_f32_16x16x32_bf16 v[204:207], a[116:119], a[4:7], v[204:207]\n"
    "ds_read_b64_tr_b16 a[144:145], v241 offset:33808\n"
    "v_cvt_pk_bf16_f32 v212, v200, v201\n"
    "v_mfma_f32_16x16x32_bf16 v[204:207], a[120:123], a[8:11], v[204:207]\n"
    "v_exp_f32_e32 v202, v202\n"
    "v_mfma_f32_16x16x32_bf16 v[204:207], a[124:127], a[12:15], v[204:207]\n"
    "ds_read_b64_tr_b16 a[146:147], v241 offset:34064\n"
    "ds_read_b64_tr_b16 a[148:149], v241 offset:34320\n"
    "v_mfma_f32_16x16x32_bf16 v[112:115], a[160:163], v[216:219], v[112:115]\n"
    "v_exp_f32_e32 v203, v203\n"
    "v_mfma_f32_16x16x32_bf16 v[112:115], a[164:167], v[220:223], v[112:115]\n"
    "ds_read_b64_tr_b16 a[150:151], v241 offset:34576\n"
    "v_cvt_pk_bf16_f32 v213, v202, v203\n"
    "v_mfma_f32_16x16x32_bf16 v[116:119], a[168:171], v[216:219], v[116:119]\n"
    "v_exp_f32_e32 v204, v204\n"
    "v_mfma_f32_16x16x32_bf16 v[116:119], a[172:175], v[220:223], v[116:119]\n"
    "ds_read_b64_tr_b16 a[152:153], v241 offset:33816\n"
    "ds_read_b64_tr_b16 a[154:155], v241 offset:34072\n"
    "v_mfma_f32_16x16x32_bf16 v[192:195], a[64:67], a[16:19], v[228:231]\n"
    "v_exp_f32_e32 v205, v205\n"
    "v_mfma_f32_16x16x32_bf16 v[192:195], a[68:71], a[20:23], v[192:195]\n"
    "ds_read_b64_tr_b16 a[156:157], v241 offset:34328\n"
    "v_cvt_pk_bf16_f32 v214, v204, v205\n"
    "v_mfma_f32_16x16x32_bf16 v[192:195], a[72:75], a[24:27], v[192:195]\n"
    "v_exp_f32_e32 v206, v206\n"
    "v_mfma_f32_16x16x32_bf16 v[192:195], a[76:79], a[28:31], v[192:195]\n"
    "ds_read_b64_tr_b16 a[158:159], v241 offset:34584\n"
    "ds_read_b64_tr_b16 a[160:161], v241 offset:33920\n"
    "v_mfma_f32_16x16x32_bf16 v[120:123], a[176:179], v[216:219], v[120:123]\n"
    "v_exp_f32_e32 v207, v207\n"
    "v_mfma_f32_16x16x32_bf16 v[120:123], a[180:183], v[220:223], v[120:123]\n"
    "ds_read_b64_tr_b16 a[162:163], v241 offset:34176\n"
    "v_cvt_pk_bf16_f32 v215, v206, v207\n"
    "v_mfma_f32_16x16x32_bf16 v[124:127], a[184:187], v[216:219], v[124:127]\n"
    "v_exp_f32_e32 v192, v192\n"
    "v_mfma_f32_16x16x32_bf16 v[124:127], a[188:191], v[220:223], v[124:127]\n"
    "ds_read_b64_tr_b16 a[164:165], v241 offset:34432\n"
    "ds_read_b64_tr_b16 a[166:167], v241 offset:34688\n"
    "v_mfma_f32_16x16x32_bf16 v[196:199], a[80:83], a[16:19], v[228:231]\n"
    "v_exp_f32_e32 v193, v193\n"
    "v_mfma_f32_16x16x32_bf16 v[196:199], a[84:87], a[20:23], v[196:199]\n"
    "ds_read_b64_tr_b16 a[168:169], v241 offset:33928\n"
    "ds_read_b64_tr_b16 a[170:171], v241 offset:34184\n"
    "v_mfma_f32_16x16x32_bf16 v[196:199], a[88:91], a[24:27], v[196:199]\n"
    "v_exp_f32_e32 v194, v194\n"
    "v_mfma_f32_16x16x32_bf16 v[196:199], a[92:95], a[28:31], v[196:199]\n"
    "s_waitcnt lgkmcnt(14)\n"
    "ds_read_b64_tr_b16 a[172:173], v241 offset:34440\n"
    "ds_read_b64_tr_b16 a[174:175], v241 offset:34696\n"
    "v_mfma_f32_16x16x32_bf16 v[0:3], a[128:131], v[208:211], v[0:3]\n"
    "v_exp_f32_e32 v195, v195\n"
    "v_mfma_f32_16x16x32_bf16 v[0:3], a[132:135], v[212:215], v[0:3]\n"
    "s_waitcnt lgkmcnt(14)\n"
    "ds_read_b128 a[64:67], v240 offset:50688\n"
    "ds_read_b128 a[68:71], v240 offset:50752\n"
    "v_mfma_f32_16x16x32_bf16 v[4:7], a[136:139], v[208:211], v[4:7]\n"
    "v_exp_f32_e32 v196, v196\n"
    "v_mfma_f32_16x16x32_bf16 v[4:7], a[140:143], v[212:215], v[4:7]\n"
    "ds_read_b128 a[72:75], v240 offset:50816\n"
    "ds_read_b128 a[76:79], v240 offset:50880\n"
    "v_mfma_f32_16x16x32_bf16 v[200:203], a[96:99], a[16:19], v[228:231]\n"
    "v_exp_f32_e32 v197, v197\n"
    "v_mfma_f32_16x16x32_bf16 v[200:203], a[100:103], a[20:23], v[200:203]\n"
    "ds_read_b64_tr_b16 a[176:177], v241 offset:33936\n"
    "ds_read_b64_tr_b16 a[178:179], v241 offset:34192\n"
    "v_mfma_f32_16x16x32_bf16 v[200:203], a[104:107], a[24:27], v[200:203]\n"
    "v_exp_f32_e32 v198, v198\n"
    "v_mfma_f32_16x16x32_bf16 v[200:203], a[108:111], a[28:31], v[200:203]\n"
    "s_waitcnt lgkmcnt(14)\n"
    "ds_read_b64_tr_b16 a[180:181], v241 offset:34448\n"
    "ds_read_b64_tr_b16 a[182:183], v241 offset:34704\n"
    "v_mfma_f32_16x16x32_bf16 v[8:11], a[144:147], v[208:211], v[8:11]\n"
    "v_exp_f32_e32 v199, v199\n"
    "v_mfma_f32_16x16x32_bf16 v[8:11], a[148:151], v[212:215], v[8:11]\n"
    "s_waitcnt lgkmcnt(14)\n"
    "ds_read_b64_tr_b16 a[184:185], v241 offset:33944\n"
    "ds_read_b64_tr_b16 a[186:187], v241 offset:34200\n"
    "v_mfma_f32_16x16x32_bf16 v[12:15], a[152:155], v[208:211], v[12:15]\n"
    "v_exp_f32_e32 v200, v200\n"
    "v_mfma_f32_16x16x32_bf16 v[12:15], a[156:159], v[212:215], v[12:15]\n"
    "ds_read_b64_tr_b16 a[188:189], v241 offset:34456\n"
    "ds_read_b64_tr_b16 a[190:191], v241 offset:34712\n"
    "v_smfmac_f32_16x16x64_bf16 a[192:195], a[208:211], v[208:215], v254\n"
    "v_cvt_pk_bf16_f32 v216, v192, v193\n"
    "v_mfma_f32_16x16x32_bf16 v[204:207], a[112:115], a[16:19], v[228:231]\n"
    "v_exp_f32_e32 v201, v201\n"
    "v_mfma_f32_16x16x32_bf16 v[204:207], a[116:119], a[20:23], v[204:207]\n"
    "ds_read_b128 a[80:83], v240 offset:50944\n"
    "v_cvt_pk_bf16_f32 v217, v194, v195\n"
    "v_mfma_f32_16x16x32_bf16 v[204:207], a[120:123], a[24:27], v[204:207]\n"
    "v_exp_f32_e32 v202, v202\n"
    "v_mfma_f32_16x16x32_bf16 v[204:207], a[124:127], a[28:31], v[204:207]\n"
    "s_waitcnt lgkmcnt(14)\n"
    "ds_read_b128 a[84:87], v240 offset:51008\n"
    "ds_read_b128 a[88:91], v240 offset:51072\n"
    "v_mfma_f32_16x16x32_bf16 v[16:19], a[160:163], v[208:211], v[16:19]\n"
    "v_exp_f32_e32 v203, v203\n"
    "v_mfma_f32_16x16x32_bf16 v[16:19], a[164:167], v[212:215], v[16:19]\n"
    "s_waitcnt lgkmcnt(14)\n"
    "ds_read_b128 a[92:95], v240 offset:51136\n"
    "v_cvt_pk_bf16_f32 v218, v196, v197\n"
    "v_mfma_f32_16x16x32_bf16 v[20:23], a[168:171], v[208:211], v[20:23]\n"
    "v_exp_f32_e32 v204, v204\n"
    "v_mfma_f32_16x16x32_bf16 v[20:23], a[172:175], v[212:215], v[20:23]\n"
    "s_waitcnt lgkmcnt(12)\n"
    "ds_read_b128 a[96:99], v240 offset:51200\n"
    "v_cvt_pk_bf16_f32 v219, v198, v199\n"
    "v_mfma_f32_16x16x32_bf16 v[192:195], a[64:67], a[0:3], v[224:227]\n"
    "v_exp_f32_e32 v205, v205\n"
    "v_mfma_f32_16x16x32_bf16 v[192:195], a[68:71], a[4:7], v[192:195]\n"
    "ds_read_b128 a[100:103], v240 offset:51264\n"
    "ds_read_b128 a[104:107], v240 offset:51328\n"
    "v_mfma_f32_16x16x32_bf16 v[192:195], a[72:75], a[8:11], v[192:195]\n"
    "v_exp_f32_e32 v206, v206\n"
    "v_mfma_f32_16x16x32_bf16 v[192:195], a[76:79], a[12:15], v[192:195]\n"
    "s_waitcnt lgkmcnt(11)\n"
    "ds_read_b128 a[108:111], v240 offset:51392\n"
    "v_cvt_pk_bf16_f32 v220, v200, v201\n"
    "v_mfma_f32_16x16x32_bf16 v[24:27], a[176:179], v[208:211], v[24:27]\n"
    "v_exp_f32_e32 v207, v207\n"
    "v_mfma_f32_16x16x32_bf16 v[24:27], a[180:183], v[212:215], v[24:27]\n"
    "s_waitcnt lgkmcnt(8)\n"
    "v_cvt_pk_bf16_f32 v221, v202, v203\n"
    "v_cvt_pk_bf16_f32 v222, v204, v205\n"
    "v_mfma_f32_16x16x32_bf16 v[28:31], a[184:187], v[208:211], v[28:31]\n"
    "v_exp_f32_e32 v192, v192\n"
    "v_mfma_f32_16x16x32_bf16 v[28:31], a[188:191], v[212:215], v[28:31]\n"
    "s_waitcnt lgkmcnt(4)\n"
    "ds_read_b128 a[112:115], v240 offset:51456\n"
    "v_cvt_pk_bf16_f32 v223, v206, v207\n"
    "v_mfma_f32_16x16x32_bf16 v[196:199], a[80:83], a[0:3], v[224:227]\n"
    "v_exp_f32_e32 v193, v193\n"
    "v_mfma_f32_16x16x32_bf16 v[196:199], a[84:87], a[4:7], v[196:199]\n"
    "ds_read_b128 a[116:119], v240 offset:51520\n"
    "ds_read_b128 a[120:123], v240 offset:51584\n"
    "v_mfma_f32_16x16x32_bf16 v[196:199], a[88:91], a[8:11], v[196:199]\n"
    "v_exp_f32_e32 v194, v194\n"
    "v_mfma_f32_16x16x32_bf16 v[196:199], a[92:95], a[12:15], v[196:199]\n"
    "ds_read_b128 a[124:127], v240 offset:51648\n"
    "v_cvt_pk_bf16_f32 v208, v192, v193\n"
    "v_mfma_f32_16x16x32_bf16 v[32:35], a[128:131], v[216:219], v[32:35]\n"
    "v_exp_f32_e32 v195, v195\n"
    "v_mfma_f32_16x16x32_bf16 v[32:35], a[132:135], v[220:223], v[32:35]\n"
    "ds_read_b64_tr_b16 a[128:129], v241 offset:50688\n"
    "v_cvt_pk_bf16_f32 v209, v194, v195\n"
    "v_mfma_f32_16x16x32_bf16 v[36:39], a[136:139], v[216:219], v[36:39]\n"
    "v_exp_f32_e32 v196, v196\n"
    "v_mfma_f32_16x16x32_bf16 v[36:39], a[140:143], v[220:223], v[36:39]\n"
    "s_waitcnt lgkmcnt(5)\n"
    "ds_read_b64_tr_b16 a[130:131], v241 offset:50944\n"
    "ds_read_b64_tr_b16 a[132:133], v241 offset:51200\n"
    "v_mfma_f32_16x16x32_bf16 v[200:203], a[96:99], a[0:3], v[224:227]\n"
    "v_exp_f32_e32 v197, v197\n"
    "v_mfma_f32_16x16x32_bf16 v[200:203], a[100:103], a[4:7], v[200:203]\n"
    "ds_read_b64_tr_b16 a[134:135], v241 offset:51456\n"
    "ds_read_b64_tr_b16 a[136:137], v241 offset:50696\n"
    "v_mfma_f32_16x16x32_bf16 v[200:203], a[104:107], a[8:11], v[200:203]\n"
    "v_exp_f32_e32 v198, v198\n"
    "v_mfma_f32_16x16x32_bf16 v[200:203], a[108:111], a[12:15], v[200:203]\n"
    "ds_read_b64_tr_b16 a[138:139], v241 offset:50952\n"
    "ds_read_b64_tr_b16 a[140:141], v241 offset:51208\n"
    "v_mfma_f32_16x16x32_bf16 v[40:43], a[144:147], v[216:219], v[40:43]\n"
    "v_exp_f32_e32 v199, v199\n"
    "v_mfma_f32_16x16x32_bf16 v[40:43], a[148:151], v[220:223], v[40:43]\n"
    "ds_read_b64_tr_b16 a[142:143], v241 offset:51464\n"
    "v_cvt_pk_bf16_f32 v210, v196, v197\n"
    "v_mfma_f32_16x16x32_bf16 v[44:47], a[152:155], v[216:219], v[44:47]\n"
    "v_exp_f32_e32 v200, v200\n"
    "v_mfma_f32_16x16x32_bf16 v[44:47], a[156:159], v[220:223], v[44:47]\n"
    "ds_read_b64_tr_b16 a[144:145], v241 offset:50704\n"
    "v_cvt_pk_bf16_f32 v211, v198, v199\n"
    "v_smfmac_f32_16x16x64_bf16 a[196:199], a[208:211], v[216:223], v254\n"
    "s_waitcnt lgkmcnt(9)\n"
    "ds_read_b64_tr_b16 a[146:147], v241 offset:50960\n"
    "v_mfma_f32_16x16x32_bf16 v[204:207], a[112:115], a[0:3], v[224:227]\n"
    "v_exp_f32_e32 v201, v201\n"
    "v_mfma_f32_16x16x32_bf16 v[204:207], a[116:119], a[4:7], v[204:207]\n"
    "ds_read_b64_tr_b16 a[148:149], v241 offset:51216\n"
    "v_cvt_pk_bf16_f32 v212, v200, v201\n"
    "v_mfma_f32_16x16x32_bf16 v[204:207], a[120:123], a[8:11], v[204:207]\n"
    "v_exp_f32_e32 v202, v202\n"
    "v_mfma_f32_16x16x32_bf16 v[204:207], a[124:127], a[12:15], v[204:207]\n"
    "ds_read_b64_tr_b16 a[150:151], v241 offset:51472\n"
    "ds_read_b64_tr_b16 a[152:153], v241 offset:50712\n"
    "v_mfma_f32_16x16x32_bf16 v[48:51], a[160:163], v[216:219], v[48:51]\n"
    "v_exp_f32_e32 v203, v203\n"
    "v_mfma_f32_16x16x32_bf16 v[48:51], a[164:167], v[220:223], v[48:51]\n"
    "ds_read_b64_tr_b16 a[154:155], v241 offset:50968\n"
    "v_cvt_pk_bf16_f32 v213, v202, v203\n"
    "v_mfma_f32_16x16x32_bf16 v[52:55], a[168:171], v[216:219], v[52:55]\n"
    "v_exp_f32_e32 v204, v204\n"
    "v_mfma_f32_16x16x32_bf16 v[52:55], a[172:175], v[220:223], v[52:55]\n"
    "ds_read_b64_tr_b16 a[156:157], v241 offset:51224\n"
    "ds_read_b64_tr_b16 a[158:159], v241 offset:51480\n"
    "v_mfma_f32_16x16x32_bf16 v[192:195], a[64:67], a[16:19], v[228:231]\n"
    "v_exp_f32_e32 v205, v205\n"
    "v_mfma_f32_16x16x32_bf16 v[192:195], a[68:71], a[20:23], v[192:195]\n"
    "ds_read_b64_tr_b16 a[160:161], v241 offset:50816\n"
    "v_cvt_pk_bf16_f32 v214, v204, v205\n"
    "v_mfma_f32_16x16x32_bf16 v[192:195], a[72:75], a[24:27], v[192:195]\n"
    "v_exp_f32_e32 v206, v206\n"
    "v_mfma_f32_16x16x32_bf16 v[192:195], a[76:79], a[28:31], v[192:195]\n"
    "ds_read_b64_tr_b16 a[162:163], v241 offset:51072\n"
    "ds_read_b64_tr_b16 a[164:165], v241 offset:51328\n"
    "v_mfma_f32_16x16x32_bf16 v[56:59], a[176:179], v[216:219], v[56:59]\n"
    "v_exp_f32_e32 v207, v207\n"
    "v_mfma_f32_16x16x32_bf16 v[56:59], a[180:183], v[220:223], v[56:59]\n"
    "ds_read_b64_tr_b16 a[166:167], v241 offset:51584\n"
    "v_cvt_pk_bf16_f32 v215, v206, v207\n"
    "v_mfma_f32_16x16x32_bf16 v[60:63], a[184:187], v[216:219], v[60:63]\n"
    "v_exp_f32_e32 v192, v192\n"
    "v_mfma_f32_16x16x32_bf16 v[60:63], a[188:191], v[220:223], v[60:63]\n"
    "ds_read_b64_tr_b16 a[168:169], v241 offset:50824\n"
    "ds_read_b64_tr_b16 a[170:171], v241 offset:51080\n"
    "v_mfma_f32_16x16x32_bf16 v[196:199], a[80:83], a[16:19], v[228:231]\n"
    "v_exp_f32_e32 v193, v193\n"
    "v_mfma_f32_16x16x32_bf16 v[196:199], a[84:87], a[20:23], v[196:199]\n"
    "ds_read_b64_tr_b16 a[172:173], v241 offset:51336\n"
    "ds_read_b64_tr_b16 a[174:175], v241 offset:51592\n"
    "v_mfma_f32_16x16x32_bf16 v[196:199], a[88:91], a[24:27], v[196:199]\n"
    "v_exp_f32_e32 v194, v194\n"
    "v_mfma_f32_16x16x32_bf16 v[196:199], a[92:95], a[28:31], v[196:199]\n"
    "s_waitcnt lgkmcnt(14)\n"
    "ds_read_b64_tr_b16 a[176:177], v241 offset:50832\n"
    "ds_read_b64_tr_b16 a[178:179], v241 offset:51088\n"
    "v_mfma_f32_16x16x32_bf16 v[0:3], a[128:131], v[208:211], v[0:3]\n"
    "v_exp_f32_e32 v195, v195\n"
    "v_mfma_f32_16x16x32_bf16 v[0:3], a[132:135], v[212:215], v[0:3]\n"
    "s_waitcnt lgkmcnt(14)\n"
    "ds_read_b64_tr_b16 a[180:181], v241 offset:51344\n"
    "ds_read_b64_tr_b16 a[182:183], v241 offset:51600\n"
    "v_mfma_f32_16x16x32_bf16 v[4:7], a[136:139], v[208:211], v[4:7]\n"
    "v_exp_f32_e32 v196, v196\n"
    "v_mfma_f32_16x16x32_bf16 v[4:7], a[140:143], v[212:215], v[4:7]\n"
    "ds_read_b64_tr_b16 a[184:185], v241 offset:50840\n"
    "ds_read_b64_tr_b16 a[186:187], v241 offset:51096\n"
    "v_mfma_f32_16x16x32_bf16 v[200:203], a[96:99], a[16:19], v[228:231]\n"
    "v_exp_f32_e32 v197, v197\n"
    "v_mfma_f32_16x16x32_bf16 v[200:203], a[100:103], a[20:23], v[200:203]\n"
    "ds_read_b64_tr_b16 a[188:189], v241 offset:51352\n"
    "ds_read_b64_tr_b16 a[190:191], v241 offset:51608\n"
    "v_mfma_f32_16x16x32_bf16 v[200:203], a[104:107], a[24:27], v[200:203]\n"
    "v_exp_f32_e32 v198, v198\n"
    "v_mfma_f32_16x16x32_bf16 v[200:203], a[108:111], a[28:31], v[200:203]\n"
    "s_waitcnt lgkmcnt(14)\n"
    "v_cvt_pk_bf16_f32 v216, v192, v193\n"
    "v_cvt_pk_bf16_f32 v217, v194, v195\n"
    "v_mfma_f32_16x16x32_bf16 v[8:11], a[144:147], v[208:211], v[8:11]\n"
    "v_exp_f32_e32 v199, v199\n"
    "v_mfma_f32_16x16x32_bf16 v[8:11], a[148:151], v[212:215], v[8:11]\n"
    "s_waitcnt lgkmcnt(14)\n"
    "v_cvt_pk_bf16_f32 v218, v196, v197\n"
    "v_cvt_pk_bf16_f32 v219, v198, v199\n"
    "v_mfma_f32_16x16x32_bf16 v[12:15], a[152:155], v[208:211], v[12:15]\n"
    "v_exp_f32_e32 v200, v200\n"
    "v_mfma_f32_16x16x32_bf16 v[12:15], a[156:159], v[212:215], v[12:15]\n"
    "v_smfmac_f32_16x16x64_bf16 a[192:195], a[208:211], v[208:215], v254\n"
    "v_mfma_f32_16x16x32_bf16 v[204:207], a[112:115], a[16:19], v[228:231]\n"
    "v_exp_f32_e32 v201, v201\n"
    "v_mfma_f32_16x16x32_bf16 v[204:207], a[116:119], a[20:23], v[204:207]\n"
    "v_cvt_pk_bf16_f32 v220, v200, v201\n"
    "v_mfma_f32_16x16x32_bf16 v[204:207], a[120:123], a[24:27], v[204:207]\n"
    "v_exp_f32_e32 v202, v202\n"
    "v_mfma_f32_16x16x32_bf16 v[204:207], a[124:127], a[28:31], v[204:207]\n"
    "s_waitcnt lgkmcnt(12)\n"
    "v_accvgpr_read_b32 v252, a192\n"
    "v_accvgpr_read_b32 v246, a193\n"
    "v_mfma_f32_16x16x32_bf16 v[16:19], a[160:163], v[208:211], v[16:19]\n"
    "v_exp_f32_e32 v203, v203\n"
    "v_mfma_f32_16x16x32_bf16 v[16:19], a[164:167], v[212:215], v[16:19]\n"
    "s_waitcnt lgkmcnt(8)\n"
    "v_cvt_pk_bf16_f32 v221, v202, v203\n"
    "v_add_f32_e32 v252, v252, v246\n"
    "v_mfma_f32_16x16x32_bf16 v[20:23], a[168:171], v[208:211], v[20:23]\n"
    "v_exp_f32_e32 v204, v204\n"
    "v_mfma_f32_16x16x32_bf16 v[20:23], a[172:175], v[212:215], v[20:23]\n"
    "v_cmp_ngt_f32_e64 s[78:79], v254, v252\n"
    "v_mfma_f32_16x16x32_bf16 v[192:195], a[64:67], a[32:35], v[232:235]\n"
    "s_xor_b32 s78, s78, s67\n"
    "v_exp_f32_e32 v205, v205\n"
    "v_mfma_f32_16x16x32_bf16 v[192:195], a[68:71], a[36:39], v[192:195]\n"
    "s_lshr_b32 s79, s78, 16\n"
    "v_cvt_pk_bf16_f32 v222, v204, v205\n"
    "v_mfma_f32_16x16x32_bf16 v[192:195], a[72:75], a[40:43], v[192:195]\n"
    "s_or_b32 s78, s78, s79\n"
    "v_exp_f32_e32 v206, v206\n"
    "v_mfma_f32_16x16x32_bf16 v[192:195], a[76:79], a[44:47], v[192:195]\n"
    "s_waitcnt lgkmcnt(4)\n"
    "v_rcp_f32_e32 v252, v252\n"
    "v_mfma_f32_16x16x32_bf16 v[24:27], a[176:179], v[208:211], v[24:27]\n"
    "s_andn2_b32 s78, s78, s67\n"
    "v_exp_f32_e32 v207, v207\n"
    "v_mfma_f32_16x16x32_bf16 v[24:27], a[180:183], v[212:215], v[24:27]\n"
    "s_waitcnt lgkmcnt(0)\n"
    "v_cvt_pk_bf16_f32 v223, v206, v207\n"
    "v_mul_f32_e32 v0, v252, v0\n"
    "v_mfma_f32_16x16x32_bf16 v[28:31], a[184:187], v[208:211], v[28:31]\n"
    "s_lshl_b64 s[68:69], s[68:69], 16\n"
    "v_exp_f32_e32 v192, v192\n"
    "v_mfma_f32_16x16x32_bf16 v[28:31], a[188:191], v[212:215], v[28:31]\n"
    "s_or_b32 s68, s68, s78\n"
    "v_mul_f32_e32 v1, v252, v1\n"
    "v_cvt_pk_bf16_f32 v0, v0, v1\n"
    "v_mfma_f32_16x16x32_bf16 v[196:199], a[80:83], a[32:35], v[232:235]\n"
    "v_exp_f32_e32 v193, v193\n"
    "v_mfma_f32_16x16x32_bf16 v[196:199], a[84:87], a[36:39], v[196:199]\n"
    "v_cvt_pk_bf16_f32 v208, v192, v193\n"
    "v_mul_f32_e32 v2, v252, v2\n"
    "v_mfma_f32_16x16x32_bf16 v[196:199], a[88:91], a[40:43], v[196:199]\n"
    "v_exp_f32_e32 v194, v194\n"
    "v_mfma_f32_16x16x32_bf16 v[196:199], a[92:95], a[44:47], v[196:199]\n"
    "v_mul_f32_e64 v3, v252, v3\n"
    "v_cvt_pk_bf16_f32 v2, v2, v3\n"
    "v_pk_mul_f32 v[4:5], v[4:5], v[252:253] op_sel_hi:[1,0]\n"
    "v_pk_mul_f32 v[6:7], v[6:7], v[252:253] op_sel_hi:[1,0]\n"
    "v_cvt_pk_bf16_f32 v4, v4, v5\n"
    "v_pk_mul_f32 v[8:9], v[8:9], v[252:253] op_sel_hi:[1,0]\n"
    "v_cvt_pk_bf16_f32 v6, v6, v7\n"
    "v_pk_mul_f32 v[10:11], v[10:11], v[252:253] op_sel_hi:[1,0]\n"
    "v_cvt_pk_bf16_f32 v8, v8, v9\n"
    "v_pk_mul_f32 v[12:13], v[12:13], v[252:253] op_sel_hi:[1,0]\n"
    "v_cvt_pk_bf16_f32 v10, v10, v11\n"
    "v_pk_mul_f32 v[14:15], v[14:15], v[252:253] op_sel_hi:[1,0]\n"
    "v_cvt_pk_bf16_f32 v12, v12, v13\n"
    "v_pk_mul_f32 v[16:17], v[16:17], v[252:253] op_sel_hi:[1,0]\n"
    "v_cvt_pk_bf16_f32 v14, v14, v15\n"
    "v_pk_mul_f32 v[18:19], v[18:19], v[252:253] op_sel_hi:[1,0]\n"
    "v_cvt_pk_bf16_f32 v16, v16, v17\n"
    "v_pk_mul_f32 v[20:21], v[20:21], v[252:253] op_sel_hi:[1,0]\n"
    "v_cvt_pk_bf16_f32 v18, v18, v19\n"
    "v_pk_mul_f32 v[22:23], v[22:23], v[252:253] op_sel_hi:[1,0]\n"
    "v_cvt_pk_bf16_f32 v20, v20, v21\n"
    "v_pk_mul_f32 v[24:25], v[24:25], v[252:253] op_sel_hi:[1,0]\n"
    "v_cvt_pk_bf16_f32 v22, v22, v23\n"
    "v_pk_mul_f32 v[26:27], v[26:27], v[252:253] op_sel_hi:[1,0]\n"
    "v_cvt_pk_bf16_f32 v24, v24, v25\n"
    "v_pk_mul_f32 v[28:29], v[28:29], v[252:253] op_sel_hi:[1,0]\n"
    "v_cvt_pk_bf16_f32 v26, v26, v27\n"
    "v_pk_mul_f32 v[30:31], v[30:31], v[252:253] op_sel_hi:[1,0]\n"
    "v_cvt_pk_bf16_f32 v28, v28, v29\n"
    "v_cvt_pk_bf16_f32 v30, v30, v31\n"
    "v_mfma_f32_16x16x32_bf16 v[32:35], a[128:131], v[216:219], v[32:35]\n"
    "s_lshl_b64 vcc, s[96:97], 128\n"
    "v_exp_f32_e32 v195, v195\n"
    "v_mfma_f32_16x16x32_bf16 v[32:35], a[132:135], v[220:223], v[32:35]\n"
    "v_cvt_pk_bf16_f32 v209, v194, v195\n"
    "v_cndmask_b32_dpp v128, v8, v0, vcc quad_perm:[1,0,3,2] row_mask:0xf bank_mask:0xf\n"
    "v_mfma_f32_16x16x32_bf16 v[36:39], a[136:139], v[216:219], v[36:39]\n"
    "v_exp_f32_e32 v196, v196\n"
    "v_mfma_f32_16x16x32_bf16 v[36:39], a[140:143], v[220:223], v[36:39]\n"
    "v_cndmask_b32_dpp v129, v10, v2, vcc quad_perm:[1,0,3,2] row_mask:0xf bank_mask:0xf\n"
    "v_cndmask_b32_dpp v130, v12, v4, vcc quad_perm:[1,0,3,2] row_mask:0xf bank_mask:0xf\n"
    "v_mfma_f32_16x16x32_bf16 v[200:203], a[96:99], a[32:35], v[232:235]\n"
    "v_exp_f32_e32 v197, v197\n"
    "v_mfma_f32_16x16x32_bf16 v[200:203], a[100:103], a[36:39], v[200:203]\n"
    "v_cvt_pk_bf16_f32 v210, v196, v197\n"
    "v_cndmask_b32_dpp v131, v14, v6, vcc quad_perm:[1,0,3,2] row_mask:0xf bank_mask:0xf\n"
    "v_mfma_f32_16x16x32_bf16 v[200:203], a[104:107], a[40:43], v[200:203]\n"
    "v_exp_f32_e32 v198, v198\n"
    "v_mfma_f32_16x16x32_bf16 v[200:203], a[108:111], a[44:47], v[200:203]\n"
    "buffer_store_dwordx4 v[128:131], v250, s[48:51], 0 offen offset:0 sc0 sc1 nt\n"
    "v_cndmask_b32_dpp v136, v24, v16, vcc quad_perm:[1,0,3,2] row_mask:0xf bank_mask:0xf\n"
    "v_cndmask_b32_dpp v137, v26, v18, vcc quad_perm:[1,0,3,2] row_mask:0xf bank_mask:0xf\n"
    "v_cndmask_b32_dpp v138, v28, v20, vcc quad_perm:[1,0,3,2] row_mask:0xf bank_mask:0xf\n"
    "v_mfma_f32_16x16x32_bf16 v[40:43], a[144:147], v[216:219], v[40:43]\n"
    "v_exp_f32_e32 v199, v199\n"
    "v_mfma_f32_16x16x32_bf16 v[40:43], a[148:151], v[220:223], v[40:43]\n"
    "v_cvt_pk_bf16_f32 v211, v198, v199\n"
    "v_cndmask_b32_dpp v139, v30, v22, vcc quad_perm:[1,0,3,2] row_mask:0xf bank_mask:0xf\n"
    "v_mfma_f32_16x16x32_bf16 v[44:47], a[152:155], v[216:219], v[44:47]\n"
    "s_lshl_b64 vcc, s[96:97], 129\n"
    "v_exp_f32_e32 v200, v200\n"
    "v_mfma_f32_16x16x32_bf16 v[44:47], a[156:159], v[220:223], v[44:47]\n"
    "buffer_store_dwordx4 v[136:139], v250, s[48:51], 0 offen offset:128 sc0 sc1 nt\n"
    "v_cndmask_b32_dpp v132, v0, v8, vcc quad_perm:[1,0,3,2] row_mask:0xf bank_mask:0xf\n"
    "v_cndmask_b32_dpp v133, v2, v10, vcc quad_perm:[1,0,3,2] row_mask:0xf bank_mask:0xf\n"
    "v_cndmask_b32_dpp v134, v4, v12, vcc quad_perm:[1,0,3,2] row_mask:0xf bank_mask:0xf\n"
    "v_smfmac_f32_16x16x64_bf16 a[196:199], a[208:211], v[216:223], v254\n"
    "v_cndmask_b32_dpp v135, v6, v14, vcc quad_perm:[1,0,3,2] row_mask:0xf bank_mask:0xf\n"
    "v_mfma_f32_16x16x32_bf16 v[204:207], a[112:115], a[32:35], v[232:235]\n"
    "v_exp_f32_e32 v201, v201\n"
    "v_mfma_f32_16x16x32_bf16 v[204:207], a[116:119], a[36:39], v[204:207]\n"
    "buffer_store_dwordx4 v[132:135], v250, s[48:51], 0 offen offset:256 sc0 sc1 nt\n"
    "v_cvt_pk_bf16_f32 v212, v200, v201\n"
    "v_cndmask_b32_dpp v140, v16, v24, vcc quad_perm:[1,0,3,2] row_mask:0xf bank_mask:0xf\n"
    "v_cndmask_b32_dpp v141, v18, v26, vcc quad_perm:[1,0,3,2] row_mask:0xf bank_mask:0xf\n"
    "v_mfma_f32_16x16x32_bf16 v[204:207], a[120:123], a[40:43], v[204:207]\n"
    "v_exp_f32_e32 v202, v202\n"
    "v_mfma_f32_16x16x32_bf16 v[204:207], a[124:127], a[44:47], v[204:207]\n"
    "v_cndmask_b32_dpp v142, v20, v28, vcc quad_perm:[1,0,3,2] row_mask:0xf bank_mask:0xf\n"
    "v_cndmask_b32_dpp v143, v22, v30, vcc quad_perm:[1,0,3,2] row_mask:0xf bank_mask:0xf\n"
    "v_mfma_f32_16x16x32_bf16 v[48:51], a[160:163], v[216:219], v[48:51]\n"
    "v_exp_f32_e32 v203, v203\n"
    "v_mfma_f32_16x16x32_bf16 v[48:51], a[164:167], v[220:223], v[48:51]\n"
    "buffer_store_dwordx4 v[140:143], v250, s[48:51], 0 offen offset:384 sc0 sc1 nt\n"
    "v_cvt_pk_bf16_f32 v213, v202, v203\n"
    "v_accvgpr_read_b32 v252, a196\n"
    "v_accvgpr_read_b32 v246, a197\n"
    "v_mfma_f32_16x16x32_bf16 v[52:55], a[168:171], v[216:219], v[52:55]\n"
    "v_exp_f32_e32 v204, v204\n"
    "v_mfma_f32_16x16x32_bf16 v[52:55], a[172:175], v[220:223], v[52:55]\n"
    "v_add_f32_e32 v252, v252, v246\n"
    "v_cmp_ngt_f32_e64 s[78:79], v254, v252\n"
    "v_mfma_f32_16x16x32_bf16 v[192:195], a[64:67], a[48:51], v[236:239]\n"
    "s_xor_b32 s78, s78, s67\n"
    "v_exp_f32_e32 v205, v205\n"
    "v_mfma_f32_16x16x32_bf16 v[192:195], a[68:71], a[52:55], v[192:195]\n"
    "s_lshr_b32 s79, s78, 16\n"
    "ds_read_b128 a[64:67], v240 offset:33792\n"
    "v_cvt_pk_bf16_f32 v214, v204, v205\n"
    "v_mfma_f32_16x16x32_bf16 v[192:195], a[72:75], a[56:59], v[192:195]\n"
    "s_or_b32 s78, s78, s79\n"
    "v_exp_f32_e32 v206, v206\n"
    "v_mfma_f32_16x16x32_bf16 v[192:195], a[76:79], a[60:63], v[192:195]\n"
    "s_andn2_b32 s78, s78, s67\n"
    "ds_read_b128 a[68:71], v240 offset:33856\n"
    "ds_read_b128 a[72:75], v240 offset:33920\n"
    "v_mfma_f32_16x16x32_bf16 v[56:59], a[176:179], v[216:219], v[56:59]\n"
    "s_lshl_b64 s[86:87], s[86:87], 16\n"
    "v_exp_f32_e32 v207, v207\n"
    "v_mfma_f32_16x16x32_bf16 v[56:59], a[180:183], v[220:223], v[56:59]\n"
    "s_or_b32 s86, s86, s78\n"
    "ds_read_b128 a[76:79], v240 offset:33984\n"
    "v_cvt_pk_bf16_f32 v215, v206, v207\n"
    "v_mfma_f32_16x16x32_bf16 v[60:63], a[184:187], v[216:219], v[60:63]\n"
    "v_exp_f32_e32 v192, v192\n"
    "v_mfma_f32_16x16x32_bf16 v[60:63], a[188:191], v[220:223], v[60:63]\n"
    "v_rcp_f32_e32 v252, v252\n"
    "v_mfma_f32_16x16x32_bf16 v[196:199], a[80:83], a[48:51], v[236:239]\n"
    "v_exp_f32_e32 v193, v193\n"
    "v_mfma_f32_16x16x32_bf16 v[196:199], a[84:87], a[52:55], v[196:199]\n"
    "ds_read_b128 a[80:83], v240 offset:34048\n"
    "v_cvt_pk_bf16_f32 v216, v192, v193\n"
    "v_mfma_f32_16x16x32_bf16 v[196:199], a[88:91], a[56:59], v[196:199]\n"
    "v_exp_f32_e32 v194, v194\n"
    "v_mfma_f32_16x16x32_bf16 v[196:199], a[92:95], a[60:63], v[196:199]\n"
    "ds_read_b128 a[84:87], v240 offset:34112\n"
    "ds_read_b128 a[88:91], v240 offset:34176\n"
    "v_pk_mul_f32 v[32:33], v[32:33], v[252:253] op_sel_hi:[1,0]\n"
    "v_pk_mul_f32 v[34:35], v[34:35], v[252:253] op_sel_hi:[1,0]\n"
    "v_cvt_pk_bf16_f32 v32, v32, v33\n"
    "v_pk_mul_f32 v[36:37], v[36:37], v[252:253] op_sel_hi:[1,0]\n"
    "v_cvt_pk_bf16_f32 v34, v34, v35\n"
    "v_pk_mul_f32 v[38:39], v[38:39], v[252:253] op_sel_hi:[1,0]\n"
    "v_cvt_pk_bf16_f32 v36, v36, v37\n"
    "v_pk_mul_f32 v[40:41], v[40:41], v[252:253] op_sel_hi:[1,0]\n"
    "v_cvt_pk_bf16_f32 v38, v38, v39\n"
    "v_pk_mul_f32 v[42:43], v[42:43], v[252:253] op_sel_hi:[1,0]\n"
    "v_cvt_pk_bf16_f32 v40, v40, v41\n"
    "v_pk_mul_f32 v[44:45], v[44:45], v[252:253] op_sel_hi:[1,0]\n"
    "v_cvt_pk_bf16_f32 v42, v42, v43\n"
    "v_pk_mul_f32 v[46:47], v[46:47], v[252:253] op_sel_hi:[1,0]\n"
    "v_cvt_pk_bf16_f32 v44, v44, v45\n"
    "v_pk_mul_f32 v[48:49], v[48:49], v[252:253] op_sel_hi:[1,0]\n"
    "v_cvt_pk_bf16_f32 v46, v46, v47\n"
    "v_pk_mul_f32 v[50:51], v[50:51], v[252:253] op_sel_hi:[1,0]\n"
    "v_cvt_pk_bf16_f32 v48, v48, v49\n"
    "v_pk_mul_f32 v[52:53], v[52:53], v[252:253] op_sel_hi:[1,0]\n"
    "v_cvt_pk_bf16_f32 v50, v50, v51\n"
    "v_pk_mul_f32 v[54:55], v[54:55], v[252:253] op_sel_hi:[1,0]\n"
    "v_cvt_pk_bf16_f32 v52, v52, v53\n"
    "v_pk_mul_f32 v[56:57], v[56:57], v[252:253] op_sel_hi:[1,0]\n"
    "v_cvt_pk_bf16_f32 v54, v54, v55\n"
    "v_pk_mul_f32 v[58:59], v[58:59], v[252:253] op_sel_hi:[1,0]\n"
    "v_cvt_pk_bf16_f32 v56, v56, v57\n"
    "v_pk_mul_f32 v[60:61], v[60:61], v[252:253] op_sel_hi:[1,0]\n"
    "v_cvt_pk_bf16_f32 v58, v58, v59\n"
    "v_pk_mul_f32 v[62:63], v[62:63], v[252:253] op_sel_hi:[1,0]\n"
    "v_cvt_pk_bf16_f32 v60, v60, v61\n"
    "v_cvt_pk_bf16_f32 v62, v62, v63\n"
    "v_mfma_f32_16x16x32_bf16 v[64:67], a[128:131], v[208:211], v[64:67]\n"
    "s_lshl_b64 vcc, s[96:97], 128\n"
    "v_exp_f32_e32 v195, v195\n"
    "v_mfma_f32_16x16x32_bf16 v[64:67], a[132:135], v[212:215], v[64:67]\n"
    "ds_read_b128 a[92:95], v240 offset:34240\n"
    "v_cvt_pk_bf16_f32 v217, v194, v195\n"
    "v_mfma_f32_16x16x32_bf16 v[68:71], a[136:139], v[208:211], v[68:71]\n"
    "v_exp_f32_e32 v196, v196\n"
    "v_mfma_f32_16x16x32_bf16 v[68:71], a[140:143], v[212:215], v[68:71]\n"
    "v_cndmask_b32_dpp v144, v40, v32, vcc quad_perm:[1,0,3,2] row_mask:0xf bank_mask:0xf\n"
    "v_cndmask_b32_dpp v145, v42, v34, vcc quad_perm:[1,0,3,2] row_mask:0xf bank_mask:0xf\n"
    "v_mfma_f32_16x16x32_bf16 v[200:203], a[96:99], a[48:51], v[236:239]\n"
    "v_exp_f32_e32 v197, v197\n"
    "v_mfma_f32_16x16x32_bf16 v[200:203], a[100:103], a[52:55], v[200:203]\n"
    "ds_read_b128 a[96:99], v240 offset:34304\n"
    "v_cvt_pk_bf16_f32 v218, v196, v197\n"
    "v_mfma_f32_16x16x32_bf16 v[200:203], a[104:107], a[56:59], v[200:203]\n"
    "v_exp_f32_e32 v198, v198\n"
    "v_mfma_f32_16x16x32_bf16 v[200:203], a[108:111], a[60:63], v[200:203]\n"
    "ds_read_b128 a[100:103], v240 offset:34368\n"
    "ds_read_b128 a[104:107], v240 offset:34432\n"
    "v_mfma_f32_16x16x32_bf16 v[72:75], a[144:147], v[208:211], v[72:75]\n"
    "v_exp_f32_e32 v199, v199\n"
    "v_mfma_f32_16x16x32_bf16 v[72:75], a[148:151], v[212:215], v[72:75]\n"
    "ds_read_b128 a[108:111], v240 offset:34496\n"
    "v_cvt_pk_bf16_f32 v219, v198, v199\n"
    "v_mfma_f32_16x16x32_bf16 v[76:79], a[152:155], v[208:211], v[76:79]\n"
    "v_exp_f32_e32 v200, v200\n"
    "v_mfma_f32_16x16x32_bf16 v[76:79], a[156:159], v[212:215], v[76:79]\n"
    "v_cndmask_b32_dpp v146, v44, v36, vcc quad_perm:[1,0,3,2] row_mask:0xf bank_mask:0xf\n"
    "v_cndmask_b32_dpp v147, v46, v38, vcc quad_perm:[1,0,3,2] row_mask:0xf bank_mask:0xf\n"
    "v_smfmac_f32_16x16x64_bf16 a[200:203], a[208:211], v[208:215], v254\n"
    "v_cndmask_b32_dpp v152, v56, v48, vcc quad_perm:[1,0,3,2] row_mask:0xf bank_mask:0xf\n"
    "v_mfma_f32_16x16x32_bf16 v[204:207], a[112:115], a[48:51], v[236:239]\n"
    "v_exp_f32_e32 v201, v201\n"
    "v_mfma_f32_16x16x32_bf16 v[204:207], a[116:119], a[52:55], v[204:207]\n"
    "buffer_store_dwordx4 v[144:147], v250, s[48:51], s92 offen offset:0 sc0 sc1 nt\n"
    "ds_read_b128 a[112:115], v240 offset:34560\n"
    "ds_read_b128 a[116:119], v240 offset:34624\n"
    "v_cvt_pk_bf16_f32 v220, v200, v201\n"
    "v_mfma_f32_16x16x32_bf16 v[204:207], a[120:123], a[56:59], v[204:207]\n"
    "v_exp_f32_e32 v202, v202\n"
    "v_mfma_f32_16x16x32_bf16 v[204:207], a[124:127], a[60:63], v[204:207]\n"
    "ds_read_b128 a[120:123], v240 offset:34688\n"
    "ds_read_b128 a[124:127], v240 offset:34752\n"
    "v_mfma_f32_16x16x32_bf16 v[80:83], a[160:163], v[208:211], v[80:83]\n"
    "LW4_%=:\n"
    "s_cbranch_execz LWR_%=\n"
    "v_exp_f32_e32 v203, v203\n"
    "v_mfma_f32_16x16x32_bf16 v[80:83], a[164:167], v[212:215], v[80:83]\n"
    "v_cvt_pk_bf16_f32 v221, v202, v203\n"
    "v_cndmask_b32_dpp v153, v58, v50, vcc quad_perm:[1,0,3,2] row_mask:0xf bank_mask:0xf\n"
    "v_mfma_f32_16x16x32_bf16 v[84:87], a[168:171], v[208:211], v[84:87]\n"
    "v_exp_f32_e32 v204, v204\n"
    "v_mfma_f32_16x16x32_bf16 v[84:87], a[172:175], v[212:215], v[84:87]\n"
    "s_waitcnt lgkmcnt(12)\n"
    "v_cndmask_b32_dpp v154, v60, v52, vcc quad_perm:[1,0,3,2] row_mask:0xf bank_mask:0xf\n"
    "v_cndmask_b32_dpp v155, v62, v54, vcc quad_perm:[1,0,3,2] row_mask:0xf bank_mask:0xf\n"
    "v_mfma_f32_16x16x32_bf16 v[192:195], a[64:67], a[32:35], v[232:235]\n"
    "s_lshl_b64 vcc, s[96:97], 129\n"
    "v_exp_f32_e32 v205, v205\n"
    "v_mfma_f32_16x16x32_bf16 v[192:195], a[68:71], a[36:39], v[192:195]\n"
    "buffer_store_dwordx4 v[152:155], v250, s[48:51], s92 offen offset:128 sc0 sc1 nt\n"
    "v_cvt_pk_bf16_f32 v222, v204, v205\n"
    "v_cndmask_b32_dpp v148, v32, v40, vcc quad_perm:[1,0,3,2] row_mask:0xf bank_mask:0xf\n"
    "v_cndmask_b32_dpp v149, v34, v42, vcc quad_perm:[1,0,3,2] row_mask:0xf bank_mask:0xf\n"
    "v_mfma_f32_16x16x32_bf16 v[192:195], a[72:75], a[40:43], v[192:195]\n"
    "v_exp_f32_e32 v206, v206\n"
    "v_mfma_f32_16x16x32_bf16 v[192:195], a[76:79], a[44:47], v[192:195]\n"
    "v_cndmask_b32_dpp v150, v36, v44, vcc quad_perm:[1,0,3,2] row_mask:0xf bank_mask:0xf\n"
    "v_cndmask_b32_dpp v151, v38, v46, vcc quad_perm:[1,0,3,2] row_mask:0xf bank_mask:0xf\n"
    "v_mfma_f32_16x16x32_bf16 v[88:91], a[176:179], v[208:211], v[88:91]\n"
    "v_exp_f32_e32 v207, v207\n"
    "v_mfma_f32_16x16x32_bf16 v[88:91], a[180:183], v[212:215], v[88:91]\n"
    "buffer_store_dwordx4 v[148:151], v250, s[48:51], s92 offen offset:256 sc0 sc1 nt\n"
    "v_cvt_pk_bf16_f32 v223, v206, v207\n"
    "v_cndmask_b32_dpp v156, v48, v56, vcc quad_perm:[1,0,3,2] row_mask:0xf bank_mask:0xf\n"
    "v_cndmask_b32_dpp v157, v50, v58, vcc quad_perm:[1,0,3,2] row_mask:0xf bank_mask:0xf\n"
    "v_mfma_f32_16x16x32_bf16 v[92:95], a[184:187], v[208:211], v[92:95]\n"
    "v_exp_f32_e32 v192, v192\n"
    "v_mfma_f32_16x16x32_bf16 v[92:95], a[188:191], v[212:215], v[92:95]\n"
    "s_waitcnt lgkmcnt(8)\n"
    "v_cndmask_b32_dpp v158, v52, v60, vcc quad_perm:[1,0,3,2] row_mask:0xf bank_mask:0xf\n"
    "v_cndmask_b32_dpp v159, v54, v62, vcc quad_perm:[1,0,3,2] row_mask:0xf bank_mask:0xf\n"
    "v_mfma_f32_16x16x32_bf16 v[196:199], a[80:83], a[32:35], v[232:235]\n"
    "v_exp_f32_e32 v193, v193\n"
    "v_mfma_f32_16x16x32_bf16 v[196:199], a[84:87], a[36:39], v[196:199]\n"
    "buffer_store_dwordx4 v[156:159], v250, s[48:51], s92 offen offset:384 sc0 sc1 nt\n"
    "v_cvt_pk_bf16_f32 v208, v192, v193\n"
    "v_mfma_f32_16x16x32_bf16 v[196:199], a[88:91], a[40:43], v[196:199]\n"
    "v_exp_f32_e32 v194, v194\n"
    "v_mfma_f32_16x16x32_bf16 v[196:199], a[92:95], a[44:47], v[196:199]\n"
    "v_mfma_f32_16x16x32_bf16 v[96:99], a[128:131], v[216:219], v[96:99]\n"
    "v_exp_f32_e32 v195, v195\n"
    "v_mfma_f32_16x16x32_bf16 v[96:99], a[132:135], v[220:223], v[96:99]\n"
    "ds_read_b64_tr_b16 a[128:129], v241 offset:33792\n"
    "v_cvt_pk_bf16_f32 v209, v194, v195\n"
    "v_mfma_f32_16x16x32_bf16 v[100:103], a[136:139], v[216:219], v[100:103]\n"
    "v_exp_f32_e32 v196, v196\n"
    "v_mfma_f32_16x16x32_bf16 v[100:103], a[140:143], v[220:223], v[100:103]\n"
    "s_waitcnt lgkmcnt(5)\n"
    "ds_read_b64_tr_b16 a[130:131], v241 offset:34048\n"
    "ds_read_b64_tr_b16 a[132:133], v241 offset:34304\n"
    "v_mfma_f32_16x16x32_bf16 v[200:203], a[96:99], a[32:35], v[232:235]\n"
    "v_exp_f32_e32 v197, v197\n"
    "v_mfma_f32_16x16x32_bf16 v[200:203], a[100:103], a[36:39], v[200:203]\n"
    "ds_read_b64_tr_b16 a[134:135], v241 offset:34560\n"
    "v_cvt_pk_bf16_f32 v210, v196, v197\n"
    "v_mfma_f32_16x16x32_bf16 v[200:203], a[104:107], a[40:43], v[200:203]\n"
    "v_exp_f32_e32 v198, v198\n"
    "v_mfma_f32_16x16x32_bf16 v[200:203], a[108:111], a[44:47], v[200:203]\n"
    "ds_read_b64_tr_b16 a[136:137], v241 offset:33800\n"
    "ds_read_b64_tr_b16 a[138:139], v241 offset:34056\n"
    "v_mfma_f32_16x16x32_bf16 v[104:107], a[144:147], v[216:219], v[104:107]\n"
    "v_exp_f32_e32 v199, v199\n"
    "v_mfma_f32_16x16x32_bf16 v[104:107], a[148:151], v[220:223], v[104:107]\n"
    "ds_read_b64_tr_b16 a[140:141], v241 offset:34312\n"
    "v_cvt_pk_bf16_f32 v211, v198, v199\n"
    "v_mfma_f32_16x16x32_bf16 v[108:111], a[152:155], v[216:219], v[108:111]\n"
    "v_exp_f32_e32 v200, v200\n"
    "v_mfma_f32_16x16x32_bf16 v[108:111], a[156:159], v[220:223], v[108:111]\n"
    "ds_read_b64_tr_b16 a[142:143], v241 offset:34568\n"
    "ds_read_b64_tr_b16 a[144:145], v241 offset:33808\n"
    "v_smfmac_f32_16x16x64_bf16 a[204:207], a[208:211], v[216:223], v254\n"
    "s_waitcnt lgkmcnt(9)\n"
    "ds_read_b64_tr_b16 a[146:147], v241 offset:34064\n"
    "v_mfma_f32_16x16x32_bf16 v[204:207], a[112:115], a[32:35], v[232:235]\n"
    "v_exp_f32_e32 v201, v201\n"
    "v_mfma_f32_16x16x32_bf16 v[204:207], a[116:119], a[36:39], v[204:207]\n"
    "ds_read_b64_tr_b16 a[148:149], v241 offset:34320\n"
    "v_cvt_pk_bf16_f32 v212, v200, v201\n"
    "v_mfma_f32_16x16x32_bf16 v[204:207], a[120:123], a[40:43], v[204:207]\n"
    "v_exp_f32_e32 v202, v202\n"
    "v_mfma_f32_16x16x32_bf16 v[204:207], a[124:127], a[44:47], v[204:207]\n"
    "ds_read_b64_tr_b16 a[150:151], v241 offset:34576\n"
    "ds_read_b64_tr_b16 a[152:153], v241 offset:33816\n"
    "v_mfma_f32_16x16x32_bf16 v[112:115], a[160:163], v[216:219], v[112:115]\n"
    "v_exp_f32_e32 v203, v203\n"
    "v_mfma_f32_16x16x32_bf16 v[112:115], a[164:167], v[220:223], v[112:115]\n"
    "ds_read_b64_tr_b16 a[154:155], v241 offset:34072\n"
    "v_cvt_pk_bf16_f32 v213, v202, v203\n"
    "v_mfma_f32_16x16x32_bf16 v[116:119], a[168:171], v[216:219], v[116:119]\n"
    "v_exp_f32_e32 v204, v204\n"
    "v_mfma_f32_16x16x32_bf16 v[116:119], a[172:175], v[220:223], v[116:119]\n"
    "ds_read_b64_tr_b16 a[156:157], v241 offset:34328\n"
    "ds_read_b64_tr_b16 a[158:159], v241 offset:34584\n"
    "v_mfma_f32_16x16x32_bf16 v[192:195], a[64:67], a[48:51], v[236:239]\n"
    "v_exp_f32_e32 v205, v205\n"
    "v_mfma_f32_16x16x32_bf16 v[192:195], a[68:71], a[52:55], v[192:195]\n"
    "ds_read_b64_tr_b16 a[160:161], v241 offset:33920\n"
    "v_cvt_pk_bf16_f32 v214, v204, v205\n"
    "v_mfma_f32_16x16x32_bf16 v[192:195], a[72:75], a[56:59], v[192:195]\n"
    "v_exp_f32_e32 v206, v206\n"
    "v_mfma_f32_16x16x32_bf16 v[192:195], a[76:79], a[60:63], v[192:195]\n"
    "ds_read_b64_tr_b16 a[162:163], v241 offset:34176\n"
    "ds_read_b64_tr_b16 a[164:165], v241 offset:34432\n"
    "v_mfma_f32_16x16x32_bf16 v[120:123], a[176:179], v[216:219], v[120:123]\n"
    "v_exp_f32_e32 v207, v207\n"
    "v_mfma_f32_16x16x32_bf16 v[120:123], a[180:183], v[220:223], v[120:123]\n"
    "ds_read_b64_tr_b16 a[166:167], v241 offset:34688\n"
    "v_cvt_pk_bf16_f32 v215, v206, v207\n"
    "v_mfma_f32_16x16x32_bf16 v[124:127], a[184:187], v[216:219], v[124:127]\n"
    "v_exp_f32_e32 v192, v192\n"
    "v_mfma_f32_16x16x32_bf16 v[124:127], a[188:191], v[220:223], v[124:127]\n"
    "v_exp_f32_e32 v193, v193\n"
    "v_mfma_f32_16x16x32_bf16 v[196:199], a[80:83], a[48:51], v[236:239]\n"
    "v_exp_f32_e32 v194, v194\n"
    "v_mfma_f32_16x16x32_bf16 v[196:199], a[84:87], a[52:55], v[196:199]\n"
    "v_exp_f32_e32 v195, v195\n"
    "v_mfma_f32_16x16x32_bf16 v[196:199], a[88:91], a[56:59], v[196:199]\n"
    "ds_read_b64_tr_b16 a[168:169], v241 offset:33928\n"
    "ds_read_b64_tr_b16 a[170:171], v241 offset:34184\n"
    "v_mfma_f32_16x16x32_bf16 v[196:199], a[92:95], a[60:63], v[196:199]\n"
    "ds_read_b64_tr_b16 a[172:173], v241 offset:34440\n"
    "ds_read_b64_tr_b16 a[174:175], v241 offset:34696\n"
    "v_smfmac_f32_16x16x64_bf16 a[200:203], a[208:211], v[208:215], v254\n"
    "s_waitcnt lgkmcnt(14)\n"
    "ds_read_b64_tr_b16 a[176:177], v241 offset:33936\n"
    "v_mfma_f32_16x16x32_bf16 v[64:67], a[128:131], v[208:211], v[64:67]\n"
    "ds_read_b64_tr_b16 a[178:179], v241 offset:34192\n"
    "ds_read_b64_tr_b16 a[180:181], v241 offset:34448\n"
    "v_mfma_f32_16x16x32_bf16 v[64:67], a[132:135], v[212:215], v[64:67]\n"
    "v_exp_f32_e32 v196, v196\n"
    "v_mfma_f32_16x16x32_bf16 v[200:203], a[96:99], a[48:51], v[236:239]\n"
    "v_exp_f32_e32 v197, v197\n"
    "v_mfma_f32_16x16x32_bf16 v[200:203], a[100:103], a[52:55], v[200:203]\n"
    "v_exp_f32_e32 v198, v198\n"
    "v_mfma_f32_16x16x32_bf16 v[200:203], a[104:107], a[56:59], v[200:203]\n"
    "v_exp_f32_e32 v199, v199\n"
    "v_mfma_f32_16x16x32_bf16 v[200:203], a[108:111], a[60:63], v[200:203]\n"
    "s_waitcnt lgkmcnt(14)\n"
    "ds_read_b64_tr_b16 a[182:183], v241 offset:34704\n"
    "ds_read_b64_tr_b16 a[184:185], v241 offset:33944\n"
    "v_mfma_f32_16x16x32_bf16 v[68:71], a[136:139], v[208:211], v[68:71]\n"
    "ds_read_b64_tr_b16 a[186:187], v241 offset:34200\n"
    "ds_read_b64_tr_b16 a[188:189], v241 offset:34456\n"
    "v_mfma_f32_16x16x32_bf16 v[68:71], a[140:143], v[212:215], v[68:71]\n"
    "ds_read_b64_tr_b16 a[190:191], v241 offset:34712\n"
    "v_cvt_pk_bf16_f32 v216, v192, v193\n"
    "v_mfma_f32_16x16x32_bf16 v[204:207], a[112:115], a[48:51], v[236:239]\n"
    "v_exp_f32_e32 v200, v200\n"
    "v_mfma_f32_16x16x32_bf16 v[204:207], a[116:119], a[52:55], v[204:207]\n"
    "v_exp_f32_e32 v201, v201\n"
    "v_mfma_f32_16x16x32_bf16 v[204:207], a[120:123], a[56:59], v[204:207]\n"
    "v_exp_f32_e32 v202, v202\n"
    "v_mfma_f32_16x16x32_bf16 v[204:207], a[124:127], a[60:63], v[204:207]\n"
    "s_waitcnt lgkmcnt(14)\n"
    "v_exp_f32_e32 v203, v203\n"
    "v_mfma_f32_16x16x32_bf16 v[72:75], a[144:147], v[208:211], v[72:75]\n"
    "v_cvt_pk_bf16_f32 v217, v194, v195\n"
    "v_cvt_pk_bf16_f32 v218, v196, v197\n"
    "v_mfma_f32_16x16x32_bf16 v[72:75], a[148:151], v[212:215], v[72:75]\n"
    "s_waitcnt lgkmcnt(14)\n"
    "v_cvt_pk_bf16_f32 v219, v198, v199\n"
    "v_cvt_pk_bf16_f32 v220, v200, v201\n"
    "v_mfma_f32_16x16x32_bf16 v[76:79], a[152:155], v[208:211], v[76:79]\n"
    "v_exp_f32_e32 v204, v204\n"
    "v_mfma_f32_16x16x32_bf16 v[76:79], a[156:159], v[212:215], v[76:79]\n"
    "s_waitcnt lgkmcnt(12)\n"
    "v_exp_f32_e32 v205, v205\n"
    "v_mfma_f32_16x16x32_bf16 v[80:83], a[160:163], v[208:211], v[80:83]\n"
    "v_exp_f32_e32 v206, v206\n"
    "v_mfma_f32_16x16x32_bf16 v[80:83], a[164:167], v[212:215], v[80:83]\n"
    "s_waitcnt lgkmcnt(8)\n"
    "v_exp_f32_e32 v207, v207\n"
    "v_mfma_f32_16x16x32_bf16 v[84:87], a[168:171], v[208:211], v[84:87]\n"
    "v_cvt_pk_bf16_f32 v221, v202, v203\n"
    "v_cvt_pk_bf16_f32 v222, v204, v205\n"
    "v_mfma_f32_16x16x32_bf16 v[84:87], a[172:175], v[212:215], v[84:87]\n"
    "s_waitcnt lgkmcnt(4)\n"
    "v_cvt_pk_bf16_f32 v223, v206, v207\n"
    "v_accvgpr_read_b32 v252, a200\n"
    "v_mfma_f32_16x16x32_bf16 v[88:91], a[176:179], v[208:211], v[88:91]\n"
    "v_accvgpr_read_b32 v246, a201\n"
    "v_add_f32_e32 v252, v252, v246\n"
    "v_mfma_f32_16x16x32_bf16 v[88:91], a[180:183], v[212:215], v[88:91]\n"
    "s_waitcnt lgkmcnt(0)\n"
    "v_cmp_ngt_f32_e64 s[78:79], v254, v252\n"
    "v_mfma_f32_16x16x32_bf16 v[92:95], a[184:187], v[208:211], v[92:95]\n"
    "s_xor_b32 s78, s78, s67\n"
    "v_rcp_f32_e32 v252, v252\n"
    "v_mfma_f32_16x16x32_bf16 v[92:95], a[188:191], v[212:215], v[92:95]\n"
    "s_lshr_b32 s79, s78, 16\n"
    "v_mul_f32_e32 v64, v252, v64\n"
    "v_mul_f32_e32 v65, v252, v65\n"
    "v_smfmac_f32_16x16x64_bf16 a[204:207], a[208:211], v[216:223], v254\n"
    "s_or_b32 s78, s78, s79\n"
    "v_cvt_pk_bf16_f32 v64, v64, v65\n"
    "v_mfma_f32_16x16x32_bf16 v[96:99], a[128:131], v[216:219], v[96:99]\n"
    "s_andn2_b32 s78, s78, s67\n"
    "v_mul_f32_e32 v66, v252, v66\n"
    "v_mul_f32_e64 v67, v252, v67\n"
    "v_mfma_f32_16x16x32_bf16 v[96:99], a[132:135], v[220:223], v[96:99]\n"
    "s_lshl_b64 s[98:99], s[98:99], 16\n"
    "v_cvt_pk_bf16_f32 v66, v66, v67\n"
    "v_mul_f32_e32 v68, v252, v68\n"
    "v_mul_f32_e32 v69, v252, v69\n"
    "v_pk_mul_f32 v[70:71], v[70:71], v[252:253] op_sel_hi:[1,0]\n"
    "v_cvt_pk_bf16_f32 v68, v68, v69\n"
    "v_pk_mul_f32 v[72:73], v[72:73], v[252:253] op_sel_hi:[1,0]\n"
    "v_cvt_pk_bf16_f32 v70, v70, v71\n"
    "v_pk_mul_f32 v[74:75], v[74:75], v[252:253] op_sel_hi:[1,0]\n"
    "v_cvt_pk_bf16_f32 v72, v72, v73\n"
    "v_pk_mul_f32 v[76:77], v[76:77], v[252:253] op_sel_hi:[1,0]\n"
    "v_cvt_pk_bf16_f32 v74, v74, v75\n"
    "v_pk_mul_f32 v[78:79], v[78:79], v[252:253] op_sel_hi:[1,0]\n"
    "v_cvt_pk_bf16_f32 v76, v76, v77\n"
    "v_pk_mul_f32 v[80:81], v[80:81], v[252:253] op_sel_hi:[1,0]\n"
    "v_cvt_pk_bf16_f32 v78, v78, v79\n"
    "v_pk_mul_f32 v[82:83], v[82:83], v[252:253] op_sel_hi:[1,0]\n"
    "v_cvt_pk_bf16_f32 v80, v80, v81\n"
    "v_pk_mul_f32 v[84:85], v[84:85], v[252:253] op_sel_hi:[1,0]\n"
    "v_cvt_pk_bf16_f32 v82, v82, v83\n"
    "v_pk_mul_f32 v[86:87], v[86:87], v[252:253] op_sel_hi:[1,0]\n"
    "v_cvt_pk_bf16_f32 v84, v84, v85\n"
    "v_pk_mul_f32 v[88:89], v[88:89], v[252:253] op_sel_hi:[1,0]\n"
    "v_cvt_pk_bf16_f32 v86, v86, v87\n"
    "v_pk_mul_f32 v[90:91], v[90:91], v[252:253] op_sel_hi:[1,0]\n"
    "v_cvt_pk_bf16_f32 v88, v88, v89\n"
    "v_pk_mul_f32 v[92:93], v[92:93], v[252:253] op_sel_hi:[1,0]\n"
    "v_cvt_pk_bf16_f32 v90, v90, v91\n"
    "v_pk_mul_f32 v[94:95], v[94:95], v[252:253] op_sel_hi:[1,0]\n"
    "v_cvt_pk_bf16_f32 v92, v92, v93\n"
    "v_cvt_pk_bf16_f32 v94, v94, v95\n"
    "s_lshl_b64 vcc, s[96:97], 128\n"
    "v_cndmask_b32_dpp v160, v72, v64, vcc quad_perm:[1,0,3,2] row_mask:0xf bank_mask:0xf\n"
    "v_cndmask_b32_dpp v161, v74, v66, vcc quad_perm:[1,0,3,2] row_mask:0xf bank_mask:0xf\n"
    "v_cndmask_b32_dpp v162, v76, v68, vcc quad_perm:[1,0,3,2] row_mask:0xf bank_mask:0xf\n"
    "v_cndmask_b32_dpp v163, v78, v70, vcc quad_perm:[1,0,3,2] row_mask:0xf bank_mask:0xf\n"
    "v_cndmask_b32_dpp v168, v88, v80, vcc quad_perm:[1,0,3,2] row_mask:0xf bank_mask:0xf\n"
    "v_cndmask_b32_dpp v169, v90, v82, vcc quad_perm:[1,0,3,2] row_mask:0xf bank_mask:0xf\n"
    "v_cndmask_b32_dpp v170, v92, v84, vcc quad_perm:[1,0,3,2] row_mask:0xf bank_mask:0xf\n"
    "v_cndmask_b32_dpp v171, v94, v86, vcc quad_perm:[1,0,3,2] row_mask:0xf bank_mask:0xf\n"
    "s_lshl_b64 vcc, s[96:97], 129\n"
    "v_cndmask_b32_dpp v164, v64, v72, vcc quad_perm:[1,0,3,2] row_mask:0xf bank_mask:0xf\n"
    "v_cndmask_b32_dpp v165, v66, v74, vcc quad_perm:[1,0,3,2] row_mask:0xf bank_mask:0xf\n"
    "v_cndmask_b32_dpp v166, v68, v76, vcc quad_perm:[1,0,3,2] row_mask:0xf bank_mask:0xf\n"
    "v_cndmask_b32_dpp v167, v70, v78, vcc quad_perm:[1,0,3,2] row_mask:0xf bank_mask:0xf\n"
    "v_cndmask_b32_dpp v172, v80, v88, vcc quad_perm:[1,0,3,2] row_mask:0xf bank_mask:0xf\n"
    "v_cndmask_b32_dpp v173, v82, v90, vcc quad_perm:[1,0,3,2] row_mask:0xf bank_mask:0xf\n"
    "v_cndmask_b32_dpp v174, v84, v92, vcc quad_perm:[1,0,3,2] row_mask:0xf bank_mask:0xf\n"
    "v_cndmask_b32_dpp v175, v86, v94, vcc quad_perm:[1,0,3,2] row_mask:0xf bank_mask:0xf\n"
    "v_mfma_f32_16x16x32_bf16 v[100:103], a[136:139], v[216:219], v[100:103]\n"
    "buffer_store_dwordx4 v[160:163], v250, s[48:51], s93 offen offset:0 sc0 sc1 nt\n"
    "v_accvgpr_read_b32 v252, a204\n"
    "v_accvgpr_read_b32 v246, a205\n"
    "v_add_f32_e32 v252, v252, v246\n"
    "v_mfma_f32_16x16x32_bf16 v[100:103], a[140:143], v[220:223], v[100:103]\n"
    "s_or_b32 s98, s98, s78\n"
    "v_cmp_ngt_f32_e64 s[78:79], v254, v252\n"
    "v_mfma_f32_16x16x32_bf16 v[104:107], a[144:147], v[216:219], v[104:107]\n"
    "buffer_store_dwordx4 v[168:171], v250, s[48:51], s93 offen offset:128 sc0 sc1 nt\n"
    "v_rcp_f32_e32 v252, v252\n"
    "v_mfma_f32_16x16x32_bf16 v[104:107], a[148:151], v[220:223], v[104:107]\n"
    "s_xor_b32 s78, s78, s67\n"
    "v_mul_f32_e32 v96, v252, v96\n"
    "v_mul_f32_e32 v97, v252, v97\n"
    "v_mfma_f32_16x16x32_bf16 v[108:111], a[152:155], v[216:219], v[108:111]\n"
    "buffer_store_dwordx4 v[164:167], v250, s[48:51], s93 offen offset:256 sc0 sc1 nt\n"
    "v_cvt_pk_bf16_f32 v96, v96, v97\n"
    "v_mul_f32_e32 v98, v252, v98\n"
    "v_mul_f32_e32 v99, v252, v99\n"
    "v_mfma_f32_16x16x32_bf16 v[108:111], a[156:159], v[220:223], v[108:111]\n"
    "s_lshr_b32 s79, s78, 16\n"
    "v_cvt_pk_bf16_f32 v98, v98, v99\n"
    "v_mul_f32_e32 v100, v252, v100\n"
    "v_mfma_f32_16x16x32_bf16 v[112:115], a[160:163], v[216:219], v[112:115]\n"
    "buffer_store_dwordx4 v[172:175], v250, s[48:51], s93 offen offset:384 sc0 sc1 nt\n"
    "v_mul_f32_e32 v101, v252, v101\n"
    "v_cvt_pk_bf16_f32 v100, v100, v101\n"
    "v_mul_f32_e32 v102, v252, v102\n"
    "v_mfma_f32_16x16x32_bf16 v[112:115], a[164:167], v[220:223], v[112:115]\n"
    "s_or_b32 s78, s78, s79\n"
    "v_mul_f32_e32 v103, v252, v103\n"
    "v_cvt_pk_bf16_f32 v102, v102, v103\n"
    "v_mfma_f32_16x16x32_bf16 v[116:119], a[168:171], v[216:219], v[116:119]\n"
    "s_andn2_b32 s78, s78, s67\n"
    "v_mul_f32_e32 v104, v252, v104\n"
    "v_mul_f32_e32 v105, v252, v105\n"
    "v_mfma_f32_16x16x32_bf16 v[116:119], a[172:175], v[220:223], v[116:119]\n"
    "s_lshl_b64 s[90:91], s[90:91], 16\n"
    "v_cvt_pk_bf16_f32 v104, v104, v105\n"
    "v_mul_f32_e32 v106, v252, v106\n"
    "v_mfma_f32_16x16x32_bf16 v[120:123], a[176:179], v[216:219], v[120:123]\n"
    "s_or_b32 s90, s90, s78\n"
    "v_mul_f32_e32 v107, v252, v107\n"
    "v_cvt_pk_bf16_f32 v106, v106, v107\n"
    "v_mfma_f32_16x16x32_bf16 v[120:123], a[180:183], v[220:223], v[120:123]\n"
    "v_mul_f32_e32 v108, v252, v108\n"
    "v_mul_f32_e32 v109, v252, v109\n"
    "v_mfma_f32_16x16x32_bf16 v[124:127], a[184:187], v[216:219], v[124:127]\n"
    "v_cvt_pk_bf16_f32 v108, v108, v109\n"
    "v_mul_f32_e32 v110, v252, v110\n"
    "v_mfma_f32_16x16x32_bf16 v[124:127], a[188:191], v[220:223], v[124:127]\n"
    "v_mul_f32_e64 v111, v252, v111\n"
    "v_cvt_pk_bf16_f32 v110, v110, v111\n"
    "v_pk_mul_f32 v[112:113], v[112:113], v[252:253] op_sel_hi:[1,0]\n"
    "v_pk_mul_f32 v[114:115], v[114:115], v[252:253] op_sel_hi:[1,0]\n"
    "v_cvt_pk_bf16_f32 v112, v112, v113\n"
    "v_pk_mul_f32 v[116:117], v[116:117], v[252:253] op_sel_hi:[1,0]\n"
    "v_cvt_pk_bf16_f32 v114, v114, v115\n"
    "v_pk_mul_f32 v[118:119], v[118:119], v[252:253] op_sel_hi:[1,0]\n"
    "v_cvt_pk_bf16_f32 v116, v116, v117\n"
    "v_pk_mul_f32 v[120:121], v[120:121], v[252:253] op_sel_hi:[1,0]\n"
    "v_cvt_pk_bf16_f32 v118, v118, v119\n"
    "v_pk_mul_f32 v[122:123], v[122:123], v[252:253] op_sel_hi:[1,0]\n"
    "v_cvt_pk_bf16_f32 v120, v120, v121\n"
    "v_pk_mul_f32 v[124:125], v[124:125], v[252:253] op_sel_hi:[1,0]\n"
    "v_cvt_pk_bf16_f32 v122, v122, v123\n"
    "v_pk_mul_f32 v[126:127], v[126:127], v[252:253] op_sel_hi:[1,0]\n"
    "v_cvt_pk_bf16_f32 v124, v124, v125\n"
    "v_cvt_pk_bf16_f32 v126, v126, v127\n"
    "s_lshl_b64 vcc, s[96:97], 128\n"
    "v_cndmask_b32_dpp v176, v104, v96, vcc quad_perm:[1,0,3,2] row_mask:0xf bank_mask:0xf\n"
    "v_cndmask_b32_dpp v177, v106, v98, vcc quad_perm:[1,0,3,2] row_mask:0xf bank_mask:0xf\n"
    "v_cndmask_b32_dpp v178, v108, v100, vcc quad_perm:[1,0,3,2] row_mask:0xf bank_mask:0xf\n"
    "v_cndmask_b32_dpp v179, v110, v102, vcc quad_perm:[1,0,3,2] row_mask:0xf bank_mask:0xf\n"
    "buffer_store_dwordx4 v[176:179], v250, s[48:51], s94 offen offset:0 sc0 sc1 nt\n"
    "v_cndmask_b32_dpp v184, v120, v112, vcc quad_perm:[1,0,3,2] row_mask:0xf bank_mask:0xf\n"
    "v_cndmask_b32_dpp v185, v122, v114, vcc quad_perm:[1,0,3,2] row_mask:0xf bank_mask:0xf\n"
    "v_cndmask_b32_dpp v186, v124, v116, vcc quad_perm:[1,0,3,2] row_mask:0xf bank_mask:0xf\n"
    "v_cndmask_b32_dpp v187, v126, v118, vcc quad_perm:[1,0,3,2] row_mask:0xf bank_mask:0xf\n"
    "buffer_store_dwordx4 v[184:187], v250, s[48:51], s94 offen offset:128 sc0 sc1 nt\n"
    "s_lshl_b64 vcc, s[96:97], 129\n"
    "v_cndmask_b32_dpp v180, v96, v104, vcc quad_perm:[1,0,3,2] row_mask:0xf bank_mask:0xf\n"
    "v_cndmask_b32_dpp v181, v98, v106, vcc quad_perm:[1,0,3,2] row_mask:0xf bank_mask:0xf\n"
    "v_cndmask_b32_dpp v182, v100, v108, vcc quad_perm:[1,0,3,2] row_mask:0xf bank_mask:0xf\n"
    "v_cndmask_b32_dpp v183, v102, v110, vcc quad_perm:[1,0,3,2] row_mask:0xf bank_mask:0xf\n"
    "buffer_store_dwordx4 v[180:183], v250, s[48:51], s94 offen offset:256 sc0 sc1 nt\n"
    "v_cndmask_b32_dpp v188, v112, v120, vcc quad_perm:[1,0,3,2] row_mask:0xf bank_mask:0xf\n"
    "v_cndmask_b32_dpp v189, v114, v122, vcc quad_perm:[1,0,3,2] row_mask:0xf bank_mask:0xf\n"
    "v_cndmask_b32_dpp v190, v116, v124, vcc quad_perm:[1,0,3,2] row_mask:0xf bank_mask:0xf\n"
    "v_cndmask_b32_dpp v191, v118, v126, vcc quad_perm:[1,0,3,2] row_mask:0xf bank_mask:0xf\n"
    "buffer_store_dwordx4 v[188:191], v250, s[48:51], s94 offen offset:384 sc0 sc1 nt\n"
    "s_cmp_eq_u32 s89, 0\n"
    "s_cselect_b64 s[90:91], s[90:91], 0\n"
    "s_or_b64 s[72:73], s[68:69], s[86:87]\n"
    "s_or_b64 vcc, s[98:99], s[90:91]\n"
    "s_or_b32 s72, s72, s66\n"
    "s_or_b64 vcc, vcc, s[72:73]\n"
    "s_cbranch_scc1 LOUT_%=\n"
    "s_endpgm\n"
    "LOUT_%=:\n"
    "s_cmp_lg_u32 s66, 0\n"
    "s_cselect_b64 %[bad0], -1, s[68:69]\n"
    "s_cselect_b64 %[bad1], -1, s[86:87]\n"
    "s_cselect_b64 %[bad2], -1, s[98:99]\n"
    "s_cselect_b64 %[bad3], -1, s[90:91]\n"
    "s_waitcnt vmcnt(0)\n"
    "s_branch LEND_%=\n"
    "LSPIN0_%=:\n"
    "s_cselect_b32 s73, 1, 0\n"
    "s_mov_b32 s72, 0x40000\n"
    "LSP0_%=:\n"
    "ds_read_b32 v253, v251\n"
    "s_waitcnt lgkmcnt(0)\n"
    "v_cmp_le_u32_e32 vcc, s76, v253\n"
    "s_sub_u32 s72, s72, 1\n"
    "s_cselect_b32 s66, 1, s66\n"
    "s_cbranch_scc1 LSO0_%=\n"
    "s_nop 1\n"
    "s_cbranch_vccz LSP0_%=\n"
    "LSO0_%=:\n"
    "s_cmp_lg_u32 s73, 0\n"
    "s_branch LRET0_%=\n"
    "LSPIN1_%=:\n"
    "s_cselect_b32 s73, 1, 0\n"
    "s_mov_b32 s72, 0x40000\n"
    "LSP1_%=:\n"
    "ds_read_b32 v253, v251\n"
    "s_waitcnt lgkmcnt(0)\n"
    "v_cmp_le_u32_e32 vcc, s76, v253\n"
    "s_sub_u32 s72, s72, 1\n"
    "s_cselect_b32 s66, 1, s66\n"
    "s_cbranch_scc1 LSO1_%=\n"
    "s_nop 1\n"
    "s_cbranch_vccz LSP1_%=\n"
    "LSO1_%=:\n"
    "s_cmp_lg_u32 s73, 0\n"
    "s_branch LRET1_%=\n"
    "LSPIN2_%=:\n"
    "s_cselect_b32 s73, 1, 0\n"
    "s_mov_b32 s72, 0x40000\n"
    "LSP2_%=:\n"
    "ds_read_b32 v253, v251\n"
    "s_waitcnt lgkmcnt(0)\n"
    "v_cmp_le_u32_e32 vcc, s76, v253\n"
    "s_sub_u32 s72, s72, 1\n"
    "s_cselect_b32 s66, 1, s66\n"
    "s_cbranch_scc1 LSO2_%=\n"
    "s_nop 1\n"
    "s_cbranch_vccz LSP2_%=\n"
    "LSO2_%=:\n"
    "s_cmp_lg_u32 s73, 0\n"
    "s_branch LRET2_%=\n"
    "LSPIN3_%=:\n"
    "s_cselect_b32 s73, 1, 0\n"
    "s_mov_b32 s72, 0x40000\n"
    "LSP3_%=:\n"
    "ds_read_b32 v253, v251\n"
    "s_waitcnt lgkmcnt(0)\n"
    "v_cmp_le_u32_e32 vcc, s76, v253\n"
    "s_sub_u32 s72, s72, 1\n"
    "s_cselect_b32 s66, 1, s66\n"
    "s_cbranch_scc1 LSO3_%=\n"
    "s_nop 1\n"
    "s_cbranch_vccz LSP3_%=\n"
    "LSO3_%=:\n"
    "s_cmp_lg_u32 s73, 0\n"
    "s_branch LRET3_%=\n"
    "LSPIN4_%=:\n"
    "s_cselect_b32 s73, 1, 0\n"
    "s_mov_b32 s72, 0x40000\n"
    "LSP4_%=:\n"
    "ds_read_b32 v253, v251\n"
    "s_waitcnt lgkmcnt(0)\n"
    "v_cmp_le_u32_e32 vcc, s76, v253\n"
    "s_sub_u32 s72, s72, 1\n"
    "s_cselect_b32 s66, 1, s66\n"
    "s_cbranch_scc1 LSO4_%=\n"
    "s_nop 1\n"
    "s_cbranch_vccz LSP4_%=\n"
    "LSO4_%=:\n"
    "s_cmp_lg_u32 s73, 0\n"
    "s_branch LRET4_%=\n"
    "LSPIN5_%=:\n"
    "s_cselect_b32 s73, 1, 0\n"
    "s_mov_b32 s72, 0x40000\n"
    "LSP5_%=:\n"
    "ds_read_b32 v253, v251\n"
    "s_waitcnt lgkmcnt(0)\n"
    "v_cmp_le_u32_e32 vcc, s76, v253\n"
    "s_sub_u32 s72, s72, 1\n"
    "s_cselect_b32 s66, 1, s66\n"
    "s_cbranch_scc1 LSO5_%=\n"
    "s_nop 1\n"
    "s_cbranch_vccz LSP5_%=\n"
    "LSO5_%=:\n"
    "s_cmp_lg_u32 s73, 0\n"
    "s_branch LRET5_%=\n"
    "LSPIN6_%=:\n"
    "s_cselect_b32 s73, 1, 0\n"
    "s_mov_b32 s72, 0x40000\n"
    "LSP6_%=:\n"
    "ds_read_b32 v253, v251\n"
    "s_waitcnt lgkmcnt(0)\n"
    "v_cmp_le_u32_e32 vcc, s76, v253\n"
    "s_sub_u32 s72, s72, 1\n"
    "s_cselect_b32 s66, 1, s66\n"
    "s_cbranch_scc1 LSO6_%=\n"
    "s_nop 1\n"
    "s_cbranch_vccz LSP6_%=\n"
    "LSO6_%=:\n"
    "s_cmp_lg_u32 s73, 0\n"
    "s_branch LRET6_%=\n"
    "LSPIN7_%=:\n"
    "s_cselect_b32 s73, 1, 0\n"
    "s_mov_b32 s72, 0x40000\n"
    "LSP7_%=:\n"
    "ds_read_b32 v253, v251\n"
    "s_waitcnt lgkmcnt(0)\n"
    "v_cmp_le_u32_e32 vcc, s76, v253\n"
    "s_sub_u32 s72, s72, 1\n"
    "s_cselect_b32 s66, 1, s66\n"
    "s_cbranch_scc1 LSO7_%=\n"
    "s_nop 1\n"
    "s_cbranch_vccz LSP7_%=\n"
    "LSO7_%=:\n"
    "s_cmp_lg_u32 s73, 0\n"
    "s_branch LRET7_%=\n"
    "LSPIN8_%=:\n"
    "s_cselect_b32 s73, 1, 0\n"
    "s_mov_b32 s72, 0x40000\n"
    "LSP8_%=:\n"
    "ds_read_b32 v253, v251\n"
    "s_waitcnt lgkmcnt(0)\n"
    "v_cmp_le_u32_e32 vcc, s76, v253\n"
    "s_sub_u32 s72, s72, 1\n"
    "s_cselect_b32 s66, 1, s66\n"
    "s_cbranch_scc1 LSO8_%=\n"
    "s_nop 1\n"
    "s_cbranch_vccz LSP8_%=\n"
    "LSO8_%=:\n"
    "s_cmp_lg_u32 s73, 0\n"
    "s_branch LRET8_%=\n"
    "LSPIN9_%=:\n"
    "s_cselect_b32 s73, 1, 0\n"
    "s_mov_b32 s72, 0x40000\n"
    "LSP9_%=:\n"
    "ds_read_b32 v253, v251\n"
    "s_waitcnt lgkmcnt(0)\n"
    "v_cmp_le_u32_e32 vcc, s76, v253\n"
    "s_sub_u32 s72, s72, 1\n"
    "s_cselect_b32 s66, 1, s66\n"
    "s_cbranch_scc1 LSO9_%=\n"
    "s_nop 1\n"
    "s_cbranch_vccz LSP9_%=\n"
    "LSO9_%=:\n"
    "s_cmp_lg_u32 s73, 0\n"
    "s_branch LRET9_%=\n"
    "LSPIN10_%=:\n"
    "s_cselect_b32 s73, 1, 0\n"
    "s_mov_b32 s72, 0x40000\n"
    "LSP10_%=:\n"
    "ds_read_b32 v253, v251\n"
    "s_waitcnt lgkmcnt(0)\n"
    "v_cmp_le_u32_e32 vcc, s76, v253\n"
    "s_sub_u32 s72, s72, 1\n"
    "s_cselect_b32 s66, 1, s66\n"
    "s_cbranch_scc1 LSO10_%=\n"
    "s_nop 1\n"
    "s_cbranch_vccz LSP10_%=\n"
    "LSO10_%=:\n"
    "s_cmp_lg_u32 s73, 0\n"
    "s_branch LRET10_%=\n"
    "LSPIN11_%=:\n"
    "s_cselect_b32 s73, 1, 0\n"
    "s_mov_b32 s72, 0x40000\n"
    "LSP11_%=:\n"
    "ds_read_b32 v253, v251\n"
    "s_waitcnt lgkmcnt(0)\n"
    "v_cmp_le_u32_e32 vcc, s76, v253\n"
    "s_sub_u32 s72, s72, 1\n"
    "s_cselect_b32 s66, 1, s66\n"
    "s_cbranch_scc1 LSO11_%=\n"
    "s_nop 1\n"
    "s_cbranch_vccz LSP11_%=\n"
    "LSO11_%=:\n"
    "s_cmp_lg_u32 s73, 0\n"
    "s_branch LRET11_%=\n"
    ".p2align 6\n"
    ".rept 554\n"
    "s_nop 0\n"
    ".endr\n"
    "LW5_%=:\n"
    "s_setpc_b64 s[72:73]\n"
    "s_nop 0\n"
    "LTOP_%=:\n"
    "v_mfma_f32_16x16x32_bf16 v[192:195], a[64:67], a[0:3], v[224:227]\n"
    "v_exp_f32_e32 v205, v205\n"
    "v_mfma_f32_16x16x32_bf16 v[192:195], a[68:71], a[4:7], v[192:195]\n"
    "v_cvt_pk_bf16_f32 v222, v204, v205\n"
    "v_lshlrev_b32_e32 v244, 16, v166\n"
    "v_mfma_f32_16x16x32_bf16 v[192:195], a[72:75], a[8:11], v[192:195]\n"
    "v_exp_f32_e32 v206, v206\n"
    "v_mfma_f32_16x16x32_bf16 v[192:195], a[76:79], a[12:15], v[192:195]\n"
    "v_and_b32_e32 v245, s67, v166\n"
    "v_mul_f32_e32 v244, s64, v244\n"
    "v_mfma_f32_16x16x32_bf16 v[88:91], a[176:179], v[208:211], v[88:91]\n"
    "v_exp_f32_e32 v207, v207\n"
    "v_mfma_f32_16x16x32_bf16 v[88:91], a[180:183], v[212:215], v[88:91]\n"
    "v_cvt_pk_bf16_f32 v223, v206, v207\n"
    "v_mul_f32_e32 v245, s64, v245\n"
    "v_mfma_f32_16x16x32_bf16 v[92:95], a[184:187], v[208:211], v[92:95]\n"
    "s_waitcnt lgkmcnt(8)\n"
    "v_exp_f32_e64 v192, v192\n"
    "v_mfma_f32_16x16x32_bf16 v[92:95], a[188:191], v[212:215], v[92:95]\n"
    "buffer_store_dwordx4 v[180:183], v250, s[48:51], 0 offen offset:256 sc0 sc1 nt\n"
    "v_cvt_pk_bf16_f32 v166, v244, v245\n"
    "v_lshlrev_b32_e32 v244, 16, v167\n"
    "v_and_b32_e32 v245, s67, v167\n"
    "v_mul_f32_e64 v244, s64, v244\n"
    "v_mfma_f32_16x16x32_bf16 v[196:199], a[80:83], a[0:3], v[224:227]\n"
    "v_exp_f32_e32 v193, v193\n"
    "v_mfma_f32_16x16x32_bf16 v[196:199], a[84:87], a[4:7], v[196:199]\n"
    "v_cvt_pk_bf16_f32 v208, v192, v193\n"
    "v_mul_f32_e32 v245, s64, v245\n"
    "v_mfma_f32_16x16x32_bf16 v[196:199], a[88:91], a[8:11], v[196:199]\n"
    "v_exp_f32_e32 v194, v194\n"
    "v_mfma_f32_16x16x32_bf16 v[196:199], a[92:95], a[12:15], v[196:199]\n"
    "v_cvt_pk_bf16_f32 v167, v244, v245\n"
    "v_mfma_f32_16x16x32_bf16 v[96:99], a[128:131], v[216:219], v[96:99]\n"
    "v_exp_f32_e32 v195, v195\n"
    "v_mfma_f32_16x16x32_bf16 v[96:99], a[132:135], v[220:223], v[96:99]\n"
    "ds_read_b64_tr_b16 a[128:129], v241 offset:33792\n"
    "v_cvt_pk_bf16_f32 v209, v194, v195\n"
    "v_mfma_f32_16x16x32_bf16 v[100:103], a[136:139], v[216:219], v[100:103]\n"
    "v_exp_f32_e32 v196, v196\n"
    "v_mfma_f32_16x16x32_bf16 v[100:103], a[140:143], v[220:223], v[100:103]\n"
    "s_waitcnt lgkmcnt(5)\n"
    "ds_read_b64_tr_b16 a[130:131], v241 offset:34048\n"
    "ds_read_b64_tr_b16 a[132:133], v241 offset:34304\n"
    "v_mfma_f32_16x16x32_bf16 v[200:203], a[96:99], a[0:3], v[224:227]\n"
    "s_add_u32 m0, s62, 16992\n"
    "v_exp_f32_e32 v197, v197\n"
    "v_mfma_f32_16x16x32_bf16 v[200:203], a[100:103], a[4:7], v[200:203]\n"
    "buffer_load_dwordx4 v242, s[36:39], 0 offen offset:3072 nt lds\n"
    "ds_read_b64_tr_b16 a[134:135], v241 offset:34560\n"
    "v_mfma_f32_16x16x32_bf16 v[200:203], a[104:107], a[8:11], v[200:203]\n"
    "s_addk_i32 s59, 16384\n"
    "v_exp_f32_e32 v198, v198\n"
    "v_mfma_f32_16x16x32_bf16 v[200:203], a[108:111], a[12:15], v[200:203]\n"
    "s_xor_b32 s71, s59, s63\n"
    "ds_read_b64_tr_b16 a[136:137], v241 offset:33800\n"
    "ds_read_b64_tr_b16 a[138:139], v241 offset:34056\n"
    "v_mfma_f32_16x16x32_bf16 v[104:107], a[144:147], v[216:219], v[104:107]\n"
    "s_add_u32 s36, s84, s71\n"
    "v_exp_f32_e32 v199, v199\n"
    "v_mfma_f32_16x16x32_bf16 v[104:107], a[148:151], v[220:223], v[104:107]\n"
    "s_addc_u32 s37, s85, 0\n"
    "ds_read_b64_tr_b16 a[140:141], v241 offset:34312\n"
    "ds_read_b64_tr_b16 a[142:143], v241 offset:34568\n"
    "v_mfma_f32_16x16x32_bf16 v[108:111], a[152:155], v[216:219], v[108:111]\n"
    "s_addk_i32 s58, -16384\n"
    "v_exp_f32_e32 v200, v200\n"
    "v_mfma_f32_16x16x32_bf16 v[108:111], a[156:159], v[220:223], v[108:111]\n"
    "s_max_i32 s38, s58, 0\n"
    "v_cvt_pk_bf16_f32 v210, v196, v197\n"
    "v_cvt_pk_bf16_f32 v211, v198, v199\n"
    "v_smfmac_f32_16x16x64_bf16 a[204:207], a[208:211], v[216:223], v254\n"
    "s_waitcnt lgkmcnt(8)\n"
    "ds_read_b64_tr_b16 a[144:145], v241 offset:33808\n"
    "v_mfma_f32_16x16x32_bf16 v[204:207], a[112:115], a[0:3], v[224:227]\n"
    "v_exp_f32_e32 v201, v201\n"
    "v_mfma_f32_16x16x32_bf16 v[204:207], a[116:119], a[4:7], v[204:207]\n"
    "ds_read_b64_tr_b16 a[146:147], v241 offset:34064\n"
    "ds_read_b64_tr_b16 a[148:149], v241 offset:34320\n"
    "v_mfma_f32_16x16x32_bf16 v[204:207], a[120:123], a[8:11], v[204:207]\n"
    "s_add_u32 m0, s62, 84480\n"
    "v_exp_f32_e32 v202, v202\n"
    "v_mfma_f32_16x16x32_bf16 v[204:207], a[124:127], a[12:15], v[204:207]\n"
    "buffer_load_dwordx4 v242, s[40:43], 0 offen offset:0 nt lds\n"
    "ds_read_b64_tr_b16 a[150:151], v241 offset:34576\n"
    "v_mfma_f32_16x16x32_bf16 v[112:115], a[160:163], v[216:219], v[112:115]\n"
    "v_exp_f32_e32 v203, v203\n"
    "v_mfma_f32_16x16x32_bf16 v[112:115], a[164:167], v[220:223], v[112:115]\n"
    "v_cvt_pk_bf16_f32 v212, v200, v201\n"
    "v_cvt_pk_bf16_f32 v213, v202, v203\n"
    "v_mfma_f32_16x16x32_bf16 v[116:119], a[168:171], v[216:219], v[116:119]\n"
    "v_exp_f32_e32 v204, v204\n"
    "v_mfma_f32_16x16x32_bf16 v[116:119], a[172:175], v[220:223], v[116:119]\n"
    "ds_read_b64_tr_b16 a[152:153], v241 offset:33816\n"
    "ds_read_b64_tr_b16 a[154:155], v241 offset:34072\n"
    "v_mfma_f32_16x16x32_bf16 v[192:195], a[64:67], a[16:19], v[228:231]\n"
    "v_exp_f32_e32 v205, v205\n"
    "v_mfma_f32_16x16x32_bf16 v[192:195], a[68:71], a[20:23], v[192:195]\n"
    "ds_read_b64_tr_b16 a[156:157], v241 offset:34328\n"
    "v_cvt_pk_bf16_f32 v214, v204, v205\n"
    "v_mfma_f32_16x16x32_bf16 v[192:195], a[72:75], a[24:27], v[192:195]\n"
    "v_exp_f32_e32 v206, v206\n"
    "v_mfma_f32_16x16x32_bf16 v[192:195], a[76:79], a[28:31], v[192:195]\n"
    "ds_read_b64_tr_b16 a[158:159], v241 offset:34584\n"
    "ds_read_b64_tr_b16 a[160:161], v241 offset:33920\n"
    "v_mfma_f32_16x16x32_bf16 v[120:123], a[176:179], v[216:219], v[120:123]\n"
    "v_exp_f32_e32 v207, v207\n"
    "v_mfma_f32_16x16x32_bf16 v[120:123], a[180:183], v[220:223], v[120:123]\n"
    "ds_read_b64_tr_b16 a[162:163], v241 offset:34176\n"
    "v_cvt_pk_bf16_f32 v215, v206, v207\n"
    "v_mfma_f32_16x16x32_bf16 v[124:127], a[184:187], v[216:219], v[124:127]\n"
    "s_add_u32 m0, s62, 84488\n"
    "v_exp_f32_e32 v192, v192\n"
    "v_mfma_f32_16x16x32_bf16 v[124:127], a[188:191], v[220:223], v[124:127]\n"
    "buffer_load_dwordx4 v242, s[40:43], 0 offen offset:1024 nt lds\n"
    "ds_read_b64_tr_b16 a[164:165], v241 offset:34432\n"
    "v_mfma_f32_16x16x32_bf16 v[196:199], a[80:83], a[16:19], v[228:231]\n"
    "v_exp_f32_e32 v193, v193\n"
    "v_mfma_f32_16x16x32_bf16 v[196:199], a[84:87], a[20:23], v[196:199]\n"
    "ds_read_b64_tr_b16 a[166:167], v241 offset:34688\n"
    "ds_read_b64_tr_b16 a[168:169], v241 offset:33928\n"
    "v_mfma_f32_16x16x32_bf16 v[196:199], a[88:91], a[24:27], v[196:199]\n"
    "v_exp_f32_e32 v194, v194\n"
    "v_mfma_f32_16x16x32_bf16 v[196:199], a[92:95], a[28:31], v[196:199]\n"
    "s_waitcnt lgkmcnt(14)\n"
    "ds_read_b64_tr_b16 a[170:171], v241 offset:34184\n"
    "ds_read_b64_tr_b16 a[172:173], v241 offset:34440\n"
    "v_mfma_f32_16x16x32_bf16 v[0:3], a[128:131], v[208:211], v[0:3]\n"
    "s_waitcnt lgkmcnt(14)\n"
    "v_exp_f32_e32 v195, v195\n"
    "v_mfma_f32_16x16x32_bf16 v[0:3], a[132:135], v[212:215], v[0:3]\n"
    "s_waitcnt vmcnt(18)\n"
    "ds_read_b64_tr_b16 a[174:175], v241 offset:34696\n"
    "ds_read_b128 v[168:171], v255 offset:0\n"
    "v_mfma_f32_16x16x32_bf16 v[4:7], a[136:139], v[208:211], v[4:7]\n"
    "v_exp_f32_e32 v196, v196\n"
    "v_mfma_f32_16x16x32_bf16 v[4:7], a[140:143], v[212:215], v[4:7]\n"
    "ds_read_b128 v[172:175], v255 offset:64\n"
    "ds_read_b64_tr_b16 a[176:177], v241 offset:33936\n"
    "v_mfma_f32_16x16x32_bf16 v[200:203], a[96:99], a[16:19], v[228:231]\n"
    "s_add_u32 m0, s62, 84496\n"
    "v_exp_f32_e32 v197, v197\n"
    "v_mfma_f32_16x16x32_bf16 v[200:203], a[100:103], a[20:23], v[200:203]\n"
    "buffer_load_dwordx4 v242, s[40:43], 0 offen offset:2048 nt lds\n"
    "ds_read_b64_tr_b16 a[178:179], v241 offset:34192\n"
    "v_mfma_f32_16x16x32_bf16 v[200:203], a[104:107], a[24:27], v[200:203]\n"
    "v_exp_f32_e32 v198, v198\n"
    "v_mfma_f32_16x16x32_bf16 v[200:203], a[108:111], a[28:31], v[200:203]\n"
    "s_waitcnt lgkmcnt(14)\n"
    "ds_read_b64_tr_b16 a[180:181], v241 offset:34448\n"
    "ds_read_b64_tr_b16 a[182:183], v241 offset:34704\n"
    "v_mfma_f32_16x16x32_bf16 v[8:11], a[144:147], v[208:211], v[8:11]\n"
    "s_waitcnt lgkmcnt(14)\n"
    "v_exp_f32_e32 v199, v199\n"
    "v_mfma_f32_16x16x32_bf16 v[8:11], a[148:151], v[212:215], v[8:11]\n"
    "s_waitcnt lgkmcnt(4)\n"
    "ds_read_b64_tr_b16 a[184:185], v241 offset:33944\n"
    "ds_read_b64_tr_b16 a[186:187], v241 offset:34200\n"
    "v_mfma_f32_16x16x32_bf16 v[12:15], a[152:155], v[208:211], v[12:15]\n"
    "v_exp_f32_e32 v200, v200\n"
    "v_mfma_f32_16x16x32_bf16 v[12:15], a[156:159], v[212:215], v[12:15]\n"
    "ds_read_b64_tr_b16 a[188:189], v241 offset:34456\n"
    "ds_read_b64_tr_b16 a[190:191], v241 offset:34712\n"
    "v_smfmac_f32_16x16x64_bf16 a[192:195], a[208:211], v[208:215], v254\n"
    "v_cvt_pk_bf16_f32 v216, v192, v193\n"
    "v_mfma_f32_16x16x32_bf16 v[204:207], a[112:115], a[16:19], v[228:231]\n"
    "s_add_u32 m0, s62, 84504\n"
    "v_exp_f32_e32 v201, v201\n"
    "v_mfma_f32_16x16x32_bf16 v[204:207], a[116:119], a[20:23], v[204:207]\n"
    "s_waitcnt vmcnt(23)\n"
    "ds_append v253\n"
    "v_cvt_pk_bf16_f32 v217, v194, v195\n"
    "v_mfma_f32_16x16x32_bf16 v[204:207], a[120:123], a[24:27], v[204:207]\n"
    "s_waitcnt lgkmcnt(14)\n"
    "v_exp_f32_e32 v202, v202\n"
    "v_mfma_f32_16x16x32_bf16 v[204:207], a[124:127], a[28:31], v[204:207]\n"
    "buffer_load_dwordx4 v242, s[40:43], 0 offen offset:3072 nt lds\n"
    "v_cvt_pk_bf16_f32 v218, v196, v197\n"
    "v_mfma_f32_16x16x32_bf16 v[16:19], a[160:163], v[208:211], v[16:19]\n"
    "s_add_u32 s40, s60, s71\n"
    "v_exp_f32_e32 v203, v203\n"
    "v_mfma_f32_16x16x32_bf16 v[16:19], a[164:167], v[212:215], v[16:19]\n"
    "s_waitcnt lgkmcnt(11)\n"
    "v_cvt_pk_bf16_f32 v219, v198, v199\n"
    "v_cvt_pk_bf16_f32 v220, v200, v201\n"
    "v_mfma_f32_16x16x32_bf16 v[20:23], a[168:171], v[208:211], v[20:23]\n"
    "s_addc_u32 s41, s61, 0\n"
    "v_exp_f32_e32 v204, v204\n"
    "v_mfma_f32_16x16x32_bf16 v[20:23], a[172:175], v[212:215], v[20:23]\n"
    "s_max_i32 s42, s58, 0\n"
    "v_cvt_pk_bf16_f32 v221, v202, v203\n"
    "v_lshlrev_b32_e32 v244, 16, v168\n"
    "v_mfma_f32_16x16x32_bf16 v[192:195], a[64:67], a[32:35], v[232:235]\n"
    "v_exp_f32_e32 v205, v205\n"
    "v_mfma_f32_16x16x32_bf16 v[192:195], a[68:71], a[36:39], v[192:195]\n"
    "v_cvt_pk_bf16_f32 v222, v204, v205\n"
    "v_and_b32_e32 v245, s67, v168\n"
    "v_mfma_f32_16x16x32_bf16 v[192:195], a[72:75], a[40:43], v[192:195]\n"
    "v_exp_f32_e32 v206, v206\n"
    "v_mfma_f32_16x16x32_bf16 v[192:195], a[76:79], a[44:47], v[192:195]\n"
    "s_waitcnt lgkmcnt(5)\n"
    "v_mul_f32_e32 v244, s64, v244\n"
    "v_mul_f32_e32 v245, s64, v245\n"
    "v_mfma_f32_16x16x32_bf16 v[24:27], a[176:179], v[208:211], v[24:27]\n"
    "v_exp_f32_e32 v207, v207\n"
    "v_mfma_f32_16x16x32_bf16 v[24:27], a[180:183], v[212:215], v[24:27]\n"
    "s_waitcnt lgkmcnt(1)\n"
    "v_cvt_pk_bf16_f32 v223, v206, v207\n"
    "v_cvt_pk_bf16_f32 v168, v244, v245\n"
    "v_mfma_f32_16x16x32_bf16 v[28:31], a[184:187], v[208:211], v[28:31]\n"
    "v_exp_f32_e32 v192, v192\n"
    "v_mfma_f32_16x16x32_bf16 v[28:31], a[188:191], v[212:215], v[28:31]\n"
    "ds_read_b32 v253, v251\n"
    "v_lshlrev_b32_e32 v244, 16, v169\n"
    "v_mfma_f32_16x16x32_bf16 v[196:199], a[80:83], a[32:35], v[232:235]\n"
    "v_exp_f32_e64 v193, v193\n"
    "v_mfma_f32_16x16x32_bf16 v[196:199], a[84:87], a[36:39], v[196:199]\n"
    "buffer_store_dwordx4 v[184:187], v250, s[48:51], 0 offen offset:128 sc0 sc1 nt\n"
    "v_cvt_pk_bf16_f32 v208, v192, v193\n"
    "v_and_b32_e32 v245, s67, v169\n"
    "v_mul_f32_e32 v244, s64, v244\n"
    "v_mul_f32_e64 v245, s64, v245\n"
    "v_mfma_f32_16x16x32_bf16 v[196:199], a[88:91], a[40:43], v[196:199]\n"
    "v_exp_f32_e32 v194, v194\n"
    "v_mfma_f32_16x16x32_bf16 v[196:199], a[92:95], a[44:47], v[196:199]\n"
    "v_cvt_pk_bf16_f32 v169, v244, v245\n"
    "v_lshlrev_b32_e32 v244, 16, v170\n"
    "v_mfma_f32_16x16x32_bf16 v[32:35], a[128:131], v[216:219], v[32:35]\n"
    "v_exp_f32_e32 v195, v195\n"
    "v_mfma_f32_16x16x32_bf16 v[32:35], a[132:135], v[220:223], v[32:35]\n"
    "s_waitcnt lgkmcnt(0)\n"
    "v_cmp_le_u32_e32 vcc, s76, v253\n"
    "v_cvt_pk_bf16_f32 v209, v194, v195\n"
    "v_mfma_f32_16x16x32_bf16 v[36:39], a[136:139], v[216:219], v[36:39]\n"
    "v_exp_f32_e32 v196, v196\n"
    "v_mfma_f32_16x16x32_bf16 v[36:39], a[140:143], v[220:223], v[36:39]\n"
    "v_and_b32_e32 v245, s67, v170\n"
    "v_mul_f32_e32 v244, s64, v244\n"
    "v_mfma_f32_16x16x32_bf16 v[200:203], a[96:99], a[32:35], v[232:235]\n"
    "s_cbranch_vccz LSPIN9_%=\n"
    "LRET9_%=:\n"
    "v_exp_f32_e32 v197, v197\n"
    "v_mfma_f32_16x16x32_bf16 v[200:203], a[100:103], a[36:39], v[200:203]\n"
    "s_addk_i32 s76, 256\n"
    "v_cvt_pk_bf16_f32 v210, v196, v197\n"
    "v_mul_f32_e32 v245, s64, v245\n"
    "v_mfma_f32_16x16x32_bf16 v[200:203], a[104:107], a[40:43], v[200:203]\n"
    "s_add_u32 m0, s62, 33792\n"
    "v_exp_f32_e32 v198, v198\n"
    "v_mfma_f32_16x16x32_bf16 v[200:203], a[108:111], a[44:47], v[200:203]\n"
    "buffer_load_dwordx4 v242, s[36:39], 0 offen offset:0 nt lds\n"
    "v_cvt_pk_bf16_f32 v170, v244, v245\n"
    "v_mfma_f32_16x16x32_bf16 v[40:43], a[144:147], v[216:219], v[40:43]\n"
    "v_exp_f32_e32 v199, v199\n"
    "v_mfma_f32_16x16x32_bf16 v[40:43], a[148:151], v[220:223], v[40:43]\n"
    "v_cvt_pk_bf16_f32 v211, v198, v199\n"
    "v_lshlrev_b32_e32 v244, 16, v171\n"
    "v_mfma_f32_16x16x32_bf16 v[44:47], a[152:155], v[216:219], v[44:47]\n"
    "v_exp_f32_e32 v200, v200\n"
    "v_mfma_f32_16x16x32_bf16 v[44:47], a[156:159], v[220:223], v[44:47]\n"
    "v_and_b32_e32 v245, s67, v171\n"
    "v_mul_f32_e32 v244, s64, v244\n"
    "v_smfmac_f32_16x16x64_bf16 a[196:199], a[208:211], v[216:223], v254\n"
    "v_mul_f32_e32 v245, s64, v245\n"
    "v_mfma_f32_16x16x32_bf16 v[204:207], a[112:115], a[32:35], v[232:235]\n"
    "v_exp_f32_e32 v201, v201\n"
    "v_mfma_f32_16x16x32_bf16 v[204:207], a[116:119], a[36:39], v[204:207]\n"
    "v_cvt_pk_bf16_f32 v212, v200, v201\n"
    "v_cvt_pk_bf16_f32 v171, v244, v245\n"
    "v_mfma_f32_16x16x32_bf16 v[204:207], a[120:123], a[40:43], v[204:207]\n"
    "v_exp_f32_e32 v202, v202\n"
    "v_mfma_f32_16x16x32_bf16 v[204:207], a[124:127], a[44:47], v[204:207]\n"
    "v_lshlrev_b32_e32 v244, 16, v172\n"
    "v_and_b32_e32 v245, s67, v172\n"
    "v_mfma_f32_16x16x32_bf16 v[48:51], a[160:163], v[216:219], v[48:51]\n"
    "s_sub_u32 m0, s95, 128\n"
    "v_exp_f32_e32 v203, v203\n"
    "v_mfma_f32_16x16x32_bf16 v[48:51], a[164:167], v[220:223], v[48:51]\n"
    "buffer_load_dwordx4 v243, s[44:47], 0 offen offset:128 lds\n"
    "v_cvt_pk_bf16_f32 v213, v202, v203\n"
    "v_mfma_f32_16x16x32_bf16 v[52:55], a[168:171], v[216:219], v[52:55]\n"
    "v_exp_f32_e32 v204, v204\n"
    "v_mfma_f32_16x16x32_bf16 v[52:55], a[172:175], v[220:223], v[52:55]\n"
    "v_mul_f32_e32 v244, s64, v244\n"
    "v_mul_f32_e32 v245, s64, v245\n"
    "v_mfma_f32_16x16x32_bf16 v[192:195], a[64:67], a[48:51], v[236:239]\n"
    "v_exp_f32_e32 v205, v205\n"
    "v_mfma_f32_16x16x32_bf16 v[192:195], a[68:71], a[52:55], v[192:195]\n"
    "ds_read_b128 a[64:67], v240 offset:50688\n"
    "v_cvt_pk_bf16_f32 v214, v204, v205\n"
    "v_mfma_f32_16x16x32_bf16 v[192:195], a[72:75], a[56:59], v[192:195]\n"
    "v_exp_f32_e32 v206, v206\n"
    "v_mfma_f32_16x16x32_bf16 v[192:195], a[76:79], a[60:63], v[192:195]\n"
    "ds_read_b128 a[68:71], v240 offset:50752\n"
    "ds_read_b128 a[72:75], v240 offset:50816\n"
    "v_mfma_f32_16x16x32_bf16 v[56:59], a[176:179], v[216:219], v[56:59]\n"
    "v_exp_f32_e32 v207, v207\n"
    "v_mfma_f32_16x16x32_bf16 v[56:59], a[180:183], v[220:223], v[56:59]\n"
    "ds_read_b128 a[76:79], v240 offset:50880\n"
    "v_cvt_pk_bf16_f32 v215, v206, v207\n"
    "v_mfma_f32_16x16x32_bf16 v[60:63], a[184:187], v[216:219], v[60:63]\n"
    "s_sub_u32 m0, s95, 1088\n"
    "v_exp_f32_e32 v192, v192\n"
    "v_mfma_f32_16x16x32_bf16 v[60:63], a[188:191], v[220:223], v[60:63]\n"
    "buffer_load_dwordx4 v243, s[44:47], 0 offen offset:2176 lds\n"
    "v_cvt_pk_bf16_f32 v172, v244, v245\n"
    "v_mfma_f32_16x16x32_bf16 v[196:199], a[80:83], a[48:51], v[236:239]\n"
    "s_add_u32 s44, s44, 204800\n"
    "v_exp_f32_e32 v193, v193\n"
    "v_mfma_f32_16x16x32_bf16 v[196:199], a[84:87], a[52:55], v[196:199]\n"
    "s_addc_u32 s45, s45, 0\n"
    "ds_read_b128 a[80:83], v240 offset:50944\n"
    "v_cvt_pk_bf16_f32 v216, v192, v193\n"
    "v_mfma_f32_16x16x32_bf16 v[196:199], a[88:91], a[56:59], v[196:199]\n"
    "s_cmp_lt_u32 s70, 2\n"
    "v_exp_f32_e32 v194, v194\n"
    "v_mfma_f32_16x16x32_bf16 v[196:199], a[92:95], a[60:63], v[196:199]\n"
    "s_cselect_b32 s56, 1, 0\n"
    "ds_read_b128 a[84:87], v240 offset:51008\n"
    "ds_read_b128 a[88:91], v240 offset:51072\n"
    "v_mfma_f32_16x16x32_bf16 v[64:67], a[128:131], v[208:211], v[64:67]\n"
    "s_mul_i32 s46, s52, s56\n"
    "v_exp_f32_e32 v195, v195\n"
    "v_mfma_f32_16x16x32_bf16 v[64:67], a[132:135], v[212:215], v[64:67]\n"
    "ds_read_b128 a[92:95], v240 offset:51136\n"
    "v_cvt_pk_bf16_f32 v217, v194, v195\n"
    "v_mfma_f32_16x16x32_bf16 v[68:71], a[136:139], v[208:211], v[68:71]\n"
    "v_exp_f32_e32 v196, v196\n"
    "v_mfma_f32_16x16x32_bf16 v[68:71], a[140:143], v[212:215], v[68:71]\n"
    "v_lshlrev_b32_e32 v244, 16, v173\n"
    "v_and_b32_e32 v245, s67, v173\n"
    "v_mfma_f32_16x16x32_bf16 v[200:203], a[96:99], a[48:51], v[236:239]\n"
    "s_add_u32 m0, s62, 33824\n"
    "v_exp_f32_e32 v197, v197\n"
    "v_mfma_f32_16x16x32_bf16 v[200:203], a[100:103], a[52:55], v[200:203]\n"
    "buffer_load_dwordx4 v242, s[36:39], 0 offen offset:1024 nt lds\n"
    "ds_read_b128 a[96:99], v240 offset:51200\n"
    "v_mfma_f32_16x16x32_bf16 v[200:203], a[104:107], a[56:59], v[200:203]\n"
    "v_exp_f32_e32 v198, v198\n"
    "v_mfma_f32_16x16x32_bf16 v[200:203], a[108:111], a[60:63], v[200:203]\n"
    "ds_read_b128 a[100:103], v240 offset:51264\n"
    "v_cvt_pk_bf16_f32 v218, v196, v197\n"
    "v_mfma_f32_16x16x32_bf16 v[72:75], a[144:147], v[208:211], v[72:75]\n"
    "v_exp_f32_e32 v199, v199\n"
    "v_mfma_f32_16x16x32_bf16 v[72:75], a[148:151], v[212:215], v[72:75]\n"
    "ds_read_b128 a[104:107], v240 offset:51328\n"
    "ds_read_b128 a[108:111], v240 offset:51392\n"
    "v_mfma_f32_16x16x32_bf16 v[76:79], a[152:155], v[208:211], v[76:79]\n"
    "v_exp_f32_e32 v200, v200\n"
    "v_mfma_f32_16x16x32_bf16 v[76:79], a[156:159], v[212:215], v[76:79]\n"
    "v_cvt_pk_bf16_f32 v219, v198, v199\n"
    "v_mul_f32_e32 v244, s64, v244\n"
    "v_smfmac_f32_16x16x64_bf16 a[200:203], a[208:211], v[208:215], v254\n"
    "v_mul_f32_e32 v245, s64, v245\n"
    "v_mfma_f32_16x16x32_bf16 v[204:207], a[112:115], a[48:51], v[236:239]\n"
    "v_exp_f32_e32 v201, v201\n"
    "v_mfma_f32_16x16x32_bf16 v[204:207], a[116:119], a[52:55], v[204:207]\n"
    "ds_read_b128 a[112:115], v240 offset:51456\n"
    "v_cvt_pk_bf16_f32 v220, v200, v201\n"
    "v_mfma_f32_16x16x32_bf16 v[204:207], a[120:123], a[56:59], v[204:207]\n"
    "s_add_u32 m0, s62, 33856\n"
    "v_exp_f32_e32 v202, v202\n"
    "v_mfma_f32_16x16x32_bf16 v[204:207], a[124:127], a[60:63], v[204:207]\n"
    "buffer_load_dwordx4 v242, s[36:39], 0 offen offset:2048 nt lds\n"
    "ds_read_b128 a[116:119], v240 offset:51520\n"
    "v_mfma_f32_16x16x32_bf16 v[80:83], a[160:163], v[208:211], v[80:83]\n"
    "v_exp_f32_e32 v203, v203\n"
    "v_mfma_f32_16x16x32_bf16 v[80:83], a[164:167], v[212:215], v[80:83]\n"
    "ds_read_b128 a[120:123], v240 offset:51584\n"
    "v_cvt_pk_bf16_f32 v221, v202, v203\n"
    "v_mfma_f32_16x16x32_bf16 v[84:87], a[168:171], v[208:211], v[84:87]\n"
    "v_exp_f32_e32 v204, v204\n"
    "v_mfma_f32_16x16x32_bf16 v[84:87], a[172:175], v[212:215], v[84:87]\n"
    "s_waitcnt lgkmcnt(11)\n"
    "ds_read_b128 a[124:127], v240 offset:51648\n"
    "v_cvt_pk_bf16_f32 v173, v244, v245\n"
    "v_mfma_f32_16x16x32_bf16 v[192:195], a[64:67], a[0:3], v[224:227]\n"
    "v_exp_f32_e32 v205, v205\n"
    "v_mfma_f32_16x16x32_bf16 v[192:195], a[68:71], a[4:7], v[192:195]\n"
    "v_cvt_pk_bf16_f32 v222, v204, v205\n"
    "v_lshlrev_b32_e32 v244, 16, v174\n"
    "v_mfma_f32_16x16x32_bf16 v[192:195], a[72:75], a[8:11], v[192:195]\n"
    "v_exp_f32_e32 v206, v206\n"
    "v_mfma_f32_16x16x32_bf16 v[192:195], a[76:79], a[12:15], v[192:195]\n"
    "v_and_b32_e32 v245, s67, v174\n"
    "v_mul_f32_e32 v244, s64, v244\n"
    "v_mfma_f32_16x16x32_bf16 v[88:91], a[176:179], v[208:211], v[88:91]\n"
    "v_exp_f32_e32 v207, v207\n"
    "v_mfma_f32_16x16x32_bf16 v[88:91], a[180:183], v[212:215], v[88:91]\n"
    "v_cvt_pk_bf16_f32 v223, v206, v207\n"
    "v_mul_f32_e32 v245, s64, v245\n"
    "v_mfma_f32_16x16x32_bf16 v[92:95], a[184:187], v[208:211], v[92:95]\n"
    "s_waitcnt lgkmcnt(8)\n"
    "v_exp_f32_e64 v192, v192\n"
    "v_mfma_f32_16x16x32_bf16 v[92:95], a[188:191], v[212:215], v[92:95]\n"
    "buffer_store_dwordx4 v[188:191], v250, s[48:51], 0 offen offset:384 sc0 sc1 nt\n"
    "v_cvt_pk_bf16_f32 v174, v244, v245\n"
    "v_lshlrev_b32_e32 v244, 16, v175\n"
    "v_and_b32_e32 v245, s67, v175\n"
    "v_mul_f32_e64 v244, s64, v244\n"
    "v_mfma_f32_16x16x32_bf16 v[196:199], a[80:83], a[0:3], v[224:227]\n"
    "s_add_u32 s48, s48, 204800\n"
    "v_exp_f32_e32 v193, v193\n"
    "v_mfma_f32_16x16x32_bf16 v[196:199], a[84:87], a[4:7], v[196:199]\n"
    "s_addc_u32 s49, s49, 0\n"
    "v_cvt_pk_bf16_f32 v208, v192, v193\n"
    "v_mul_f32_e32 v245, s64, v245\n"
    "v_mfma_f32_16x16x32_bf16 v[196:199], a[88:91], a[8:11], v[196:199]\n"
    "s_mov_b32 s57, 1\n"
    "v_exp_f32_e32 v194, v194\n"
    "v_mfma_f32_16x16x32_bf16 v[196:199], a[92:95], a[12:15], v[196:199]\n"
    "s_mul_i32 s50, s52, s57\n"
    "v_cvt_pk_bf16_f32 v175, v244, v245\n"
    "v_mfma_f32_16x16x32_bf16 v[96:99], a[128:131], v[216:219], v[96:99]\n"
    "v_exp_f32_e32 v195, v195\n"
    "v_mfma_f32_16x16x32_bf16 v[96:99], a[132:135], v[220:223], v[96:99]\n"
    "ds_read_b64_tr_b16 a[128:129], v241 offset:50688\n"
    "v_cvt_pk_bf16_f32 v209, v194, v195\n"
    "v_mfma_f32_16x16x32_bf16 v[100:103], a[136:139], v[216:219], v[100:103]\n"
    "LW6_%=:\n"
    "s_cbranch_execz LWR_%=\n"
    "v_exp_f32_e32 v196, v196\n"
    "v_mfma_f32_16x16x32_bf16 v[100:103], a[140:143], v[220:223], v[100:103]\n"
    "s_waitcnt lgkmcnt(5)\n"
    "ds_read_b64_tr_b16 a[130:131], v241 offset:50944\n"
    "ds_read_b64_tr_b16 a[132:133], v241 offset:51200\n"
    "v_mfma_f32_16x16x32_bf16 v[200:203], a[96:99], a[0:3], v[224:227]\n"
    "s_add_u32 m0, s62, 33888\n"
    "v_exp_f32_e32 v197, v197\n"
    "v_mfma_f32_16x16x32_bf16 v[200:203], a[100:103], a[4:7], v[200:203]\n"
    "buffer_load_dwordx4 v242, s[36:39], 0 offen offset:3072 nt lds\n"
    "ds_read_b64_tr_b16 a[134:135], v241 offset:51456\n"
    "v_mfma_f32_16x16x32_bf16 v[200:203], a[104:107], a[8:11], v[200:203]\n"
    "s_addk_i32 s59, 16384\n"
    "v_exp_f32_e32 v198, v198\n"
    "v_mfma_f32_16x16x32_bf16 v[200:203], a[108:111], a[12:15], v[200:203]\n"
    "s_xor_b32 s71, s59, s63\n"
    "ds_read_b64_tr_b16 a[136:137], v241 offset:50696\n"
    "ds_read_b64_tr_b16 a[138:139], v241 offset:50952\n"
    "v_mfma_f32_16x16x32_bf16 v[104:107], a[144:147], v[216:219], v[104:107]\n"
    "s_add_u32 s36, s84, s71\n"
    "v_exp_f32_e32 v199, v199\n"
    "v_mfma_f32_16x16x32_bf16 v[104:107], a[148:151], v[220:223], v[104:107]\n"
    "s_addc_u32 s37, s85, 0\n"
    "ds_read_b64_tr_b16 a[140:141], v241 offset:51208\n"
    "ds_read_b64_tr_b16 a[142:143], v241 offset:51464\n"
    "v_mfma_f32_16x16x32_bf16 v[108:111], a[152:155], v[216:219], v[108:111]\n"
    "s_addk_i32 s58, -16384\n"
    "v_exp_f32_e32 v200, v200\n"
    "v_mfma_f32_16x16x32_bf16 v[108:111], a[156:159], v[220:223], v[108:111]\n"
    "s_max_i32 s38, s58, 0\n"
    "v_cvt_pk_bf16_f32 v210, v196, v197\n"
    "v_cvt_pk_bf16_f32 v211, v198, v199\n"
    "v_smfmac_f32_16x16x64_bf16 a[204:207], a[208:211], v[216:223], v254\n"
    "s_waitcnt lgkmcnt(8)\n"
    "ds_read_b64_tr_b16 a[144:145], v241 offset:50704\n"
    "v_mfma_f32_16x16x32_bf16 v[204:207], a[112:115], a[0:3], v[224:227]\n"
    "v_exp_f32_e32 v201, v201\n"
    "v_mfma_f32_16x16x32_bf16 v[204:207], a[116:119], a[4:7], v[204:207]\n"
    "ds_read_b64_tr_b16 a[146:147], v241 offset:50960\n"
    "ds_read_b64_tr_b16 a[148:149], v241 offset:51216\n"
    "v_mfma_f32_16x16x32_bf16 v[204:207], a[120:123], a[8:11], v[204:207]\n"
    "s_add_u32 m0, s62, 101376\n"
    "v_exp_f32_e32 v202, v202\n"
    "v_mfma_f32_16x16x32_bf16 v[204:207], a[124:127], a[12:15], v[204:207]\n"
    "buffer_load_dwordx4 v242, s[40:43], 0 offen offset:0 nt lds\n"
    "ds_read_b64_tr_b16 a[150:151], v241 offset:51472\n"
    "v_mfma_f32_16x16x32_bf16 v[112:115], a[160:163], v[216:219], v[112:115]\n"
    "v_exp_f32_e32 v203, v203\n"
    "v_mfma_f32_16x16x32_bf16 v[112:115], a[164:167], v[220:223], v[112:115]\n"
    "v_cvt_pk_bf16_f32 v212, v200, v201\n"
    "v_cvt_pk_bf16_f32 v213, v202, v203\n"
    "v_mfma_f32_16x16x32_bf16 v[116:119], a[168:171], v[216:219], v[116:119]\n"
    "v_exp_f32_e32 v204, v204\n"
    "v_mfma_f32_16x16x32_bf16 v[116:119], a[172:175], v[220:223], v[116:119]\n"
    "ds_read_b64_tr_b16 a[152:153], v241 offset:50712\n"
    "ds_read_b64_tr_b16 a[154:155], v241 offset:50968\n"
    "v_mfma_f32_16x16x32_bf16 v[192:195], a[64:67], a[16:19], v[228:231]\n"
    "v_exp_f32_e32 v205, v205\n"
    "v_mfma_f32_16x16x32_bf16 v[192:195], a[68:71], a[20:23], v[192:195]\n"
    "ds_read_b64_tr_b16 a[156:157], v241 offset:51224\n"
    "v_cvt_pk_bf16_f32 v214, v204, v205\n"
    "v_mfma_f32_16x16x32_bf16 v[192:195], a[72:75], a[24:27], v[192:195]\n"
    "v_exp_f32_e32 v206, v206\n"
    "v_mfma_f32_16x16x32_bf16 v[192:195], a[76:79], a[28:31], v[192:195]\n"
    "ds_read_b64_tr_b16 a[158:159], v241 offset:51480\n"
    "ds_read_b64_tr_b16 a[160:161], v241 offset:50816\n"
    "v_mfma_f32_16x16x32_bf16 v[120:123], a[176:179], v[216:219], v[120:123]\n"
    "v_exp_f32_e32 v207, v207\n"
    "v_mfma_f32_16x16x32_bf16 v[120:123], a[180:183], v[220:223], v[120:123]\n"
    "ds_read_b64_tr_b16 a[162:163], v241 offset:51072\n"
    "v_cvt_pk_bf16_f32 v215, v206, v207\n"
    "v_mfma_f32_16x16x32_bf16 v[124:127], a[184:187], v[216:219], v[124:127]\n"
    "s_add_u32 m0, s62, 101384\n"
    "v_exp_f32_e32 v192, v192\n"
    "v_mfma_f32_16x16x32_bf16 v[124:127], a[188:191], v[220:223], v[124:127]\n"
    "buffer_load_dwordx4 v242, s[40:43], 0 offen offset:1024 nt lds\n"
    "ds_read_b64_tr_b16 a[164:165], v241 offset:51328\n"
    "v_mfma_f32_16x16x32_bf16 v[196:199], a[80:83], a[16:19], v[228:231]\n"
    "v_exp_f32_e32 v193, v193\n"
    "v_mfma_f32_16x16x32_bf16 v[196:199], a[84:87], a[20:23], v[196:199]\n"
    "ds_read_b64_tr_b16 a[166:167], v241 offset:51584\n"
    "ds_read_b64_tr_b16 a[168:169], v241 offset:50824\n"
    "v_mfma_f32_16x16x32_bf16 v[196:199], a[88:91], a[24:27], v[196:199]\n"
    "v_exp_f32_e32 v194, v194\n"
    "v_mfma_f32_16x16x32_bf16 v[196:199], a[92:95], a[28:31], v[196:199]\n"
    "s_waitcnt lgkmcnt(14)\n"
    "ds_read_b64_tr_b16 a[170:171], v241 offset:51080\n"
    "ds_read_b64_tr_b16 a[172:173], v241 offset:51336\n"
    "v_mfma_f32_16x16x32_bf16 v[0:3], a[128:131], v[208:211], v[0:3]\n"
    "s_waitcnt lgkmcnt(14)\n"
    "v_exp_f32_e32 v195, v195\n"
    "v_mfma_f32_16x16x32_bf16 v[0:3], a[132:135], v[212:215], v[0:3]\n"
    "s_waitcnt vmcnt(18)\n"
    "ds_read_b64_tr_b16 a[174:175], v241 offset:51592\n"
    "ds_read_b128 v[176:179], v255 offset:2112\n"
    "v_mfma_f32_16x16x32_bf16 v[4:7], a[136:139], v[208:211], v[4:7]\n"
    "v_exp_f32_e32 v196, v196\n"
    "v_mfma_f32_16x16x32_bf16 v[4:7], a[140:143], v[212:215], v[4:7]\n"
    "ds_read_b128 v[180:183], v255 offset:2176\n"
    "ds_read_b64_tr_b16 a[176:177], v241 offset:50832\n"
    "v_mfma_f32_16x16x32_bf16 v[200:203], a[96:99], a[16:19], v[228:231]\n"
    "s_add_u32 m0, s62, 101392\n"
    "v_exp_f32_e32 v197, v197\n"
    "v_mfma_f32_16x16x32_bf16 v[200:203], a[100:103], a[20:23], v[200:203]\n"
    "buffer_load_dwordx4 v242, s[40:43], 0 offen offset:2048 nt lds\n"
    "ds_read_b64_tr_b16 a[178:179], v241 offset:51088\n"
    "v_mfma_f32_16x16x32_bf16 v[200:203], a[104:107], a[24:27], v[200:203]\n"
    "v_exp_f32_e32 v198, v198\n"
    "v_mfma_f32_16x16x32_bf16 v[200:203], a[108:111], a[28:31], v[200:203]\n"
    "s_waitcnt lgkmcnt(14)\n"
    "ds_read_b64_tr_b16 a[180:181], v241 offset:51344\n"
    "ds_read_b64_tr_b16 a[182:183], v241 offset:51600\n"
    "v_mfma_f32_16x16x32_bf16 v[8:11], a[144:147], v[208:211], v[8:11]\n"
    "s_waitcnt lgkmcnt(14)\n"
    "v_exp_f32_e32 v199, v199\n"
    "v_mfma_f32_16x16x32_bf16 v[8:11], a[148:151], v[212:215], v[8:11]\n"
    "s_waitcnt lgkmcnt(4)\n"
    "ds_read_b64_tr_b16 a[184:185], v241 offset:50840\n"
    "ds_read_b64_tr_b16 a[186:187], v241 offset:51096\n"
    "v_mfma_f32_16x16x32_bf16 v[12:15], a[152:155], v[208:211], v[12:15]\n"
    "v_exp_f32_e32 v200, v200\n"
    "v_mfma_f32_16x16x32_bf16 v[12:15], a[156:159], v[212:215], v[12:15]\n"
    "ds_read_b64_tr_b16 a[188:189], v241 offset:51352\n"
    "ds_read_b64_tr_b16 a[190:191], v241 offset:51608\n"
    "v_smfmac_f32_16x16x64_bf16 a[192:195], a[208:211], v[208:215], v254\n"
    "v_cvt_pk_bf16_f32 v216, v192, v193\n"
    "v_mfma_f32_16x16x32_bf16 v[204:207], a[112:115], a[16:19], v[228:231]\n"
    "s_add_u32 m0, s62, 101400\n"
    "v_exp_f32_e32 v201, v201\n"
    "v_mfma_f32_16x16x32_bf16 v[204:207], a[116:119], a[20:23], v[204:207]\n"
    "s_waitcnt vmcnt(23)\n"
    "ds_append v253\n"
    "v_cvt_pk_bf16_f32 v217, v194, v195\n"
    "v_mfma_f32_16x16x32_bf16 v[204:207], a[120:123], a[24:27], v[204:207]\n"
    "s_waitcnt lgkmcnt(14)\n"
    "v_exp_f32_e32 v202, v202\n"
    "v_mfma_f32_16x16x32_bf16 v[204:207], a[124:127], a[28:31], v[204:207]\n"
    "buffer_load_dwordx4 v242, s[40:43], 0 offen offset:3072 nt lds\n"
    "v_accvgpr_read_b32 v252, a192\n"
    "v_mfma_f32_16x16x32_bf16 v[16:19], a[160:163], v[208:211], v[16:19]\n"
    "s_add_u32 s40, s60, s71\n"
    "v_exp_f32_e32 v203, v203\n"
    "v_mfma_f32_16x16x32_bf16 v[16:19], a[164:167], v[212:215], v[16:19]\n"
    "s_waitcnt lgkmcnt(11)\n"
    "v_accvgpr_read_b32 v246, a193\n"
    "v_cvt_pk_bf16_f32 v218, v196, v197\n"
    "v_mfma_f32_16x16x32_bf16 v[20:23], a[168:171], v[208:211], v[20:23]\n"
    "s_addc_u32 s41, s61, 0\n"
    "v_exp_f32_e32 v204, v204\n"
    "v_mfma_f32_16x16x32_bf16 v[20:23], a[172:175], v[212:215], v[20:23]\n"
    "s_max_i32 s42, s58, 0\n"
    "v_add_f32_e32 v252, v252, v246\n"
    "v_cvt_pk_bf16_f32 v219, v198, v199\n"
    "v_mfma_f32_16x16x32_bf16 v[192:195], a[64:67], a[32:35], v[232:235]\n"
    "v_exp_f32_e32 v205, v205\n"
    "v_mfma_f32_16x16x32_bf16 v[192:195], a[68:71], a[36:39], v[192:195]\n"
    "s_waitcnt lgkmcnt(1)\n"
    "v_cmp_ngt_f32_e64 s[78:79], v254, v252\n"
    "v_cvt_pk_bf16_f32 v220, v200, v201\n"
    "v_mfma_f32_16x16x32_bf16 v[192:195], a[72:75], a[40:43], v[192:195]\n"
    "s_waitcnt lgkmcnt(5)\n"
    "v_exp_f32_e32 v206, v206\n"
    "v_mfma_f32_16x16x32_bf16 v[192:195], a[76:79], a[44:47], v[192:195]\n"
    "s_xor_b32 s78, s78, s67\n"
    "v_rcp_f32_e32 v252, v252\n"
    "v_mfma_f32_16x16x32_bf16 v[24:27], a[176:179], v[208:211], v[24:27]\n"
    "s_lshr_b32 s79, s78, 16\n"
    "v_exp_f32_e32 v207, v207\n"
    "v_mfma_f32_16x16x32_bf16 v[24:27], a[180:183], v[212:215], v[24:27]\n"
    "s_or_b32 s78, s78, s79\n"
    "v_cvt_pk_bf16_f32 v221, v202, v203\n"
    "v_cvt_pk_bf16_f32 v222, v204, v205\n"
    "v_mfma_f32_16x16x32_bf16 v[28:31], a[184:187], v[208:211], v[28:31]\n"
    "s_andn2_b32 s78, s78, s67\n"
    "v_exp_f32_e32 v192, v192\n"
    "v_mfma_f32_16x16x32_bf16 v[28:31], a[188:191], v[212:215], v[28:31]\n"
    "s_lshl_b64 s[68:69], s[68:69], 16\n"
    "ds_read_b32 v253, v251\n"
    "v_cvt_pk_bf16_f32 v223, v206, v207\n"
    "v_mfma_f32_16x16x32_bf16 v[196:199], a[80:83], a[32:35], v[232:235]\n"
    "s_or_b32 s68, s68, s78\n"
    "v_exp_f32_e32 v193, v193\n"
    "v_mfma_f32_16x16x32_bf16 v[196:199], a[84:87], a[36:39], v[196:199]\n"
    "v_cvt_pk_bf16_f32 v208, v192, v193\n"
    "v_accvgpr_write_b32 a0, v128\n"
    "v_mfma_f32_16x16x32_bf16 v[196:199], a[88:91], a[40:43], v[196:199]\n"
    "v_exp_f32_e64 v194, v194\n"
    "v_mfma_f32_16x16x32_bf16 v[196:199], a[92:95], a[44:47], v[196:199]\n"
    "v_accvgpr_write_b32 a192, 0\n"
    "v_accvgpr_write_b32 a193, 0\n"
    "v_pk_mul_f32 v[0:1], v[0:1], v[252:253] op_sel_hi:[1,0]\n"
    "v_pk_mul_f32 v[2:3], v[2:3], v[252:253] op_sel_hi:[1,0]\n"
    "v_pk_mul_f32 v[4:5], v[4:5], v[252:253] op_sel_hi:[1,0]\n"
    "v_pk_mul_f32 v[6:7], v[6:7], v[252:253] op_sel_hi:[1,0]\n"
    "v_pk_mul_f32 v[8:9], v[8:9], v[252:253] op_sel_hi:[1,0]\n"
    "v_pk_mul_f32 v[10:11], v[10:11], v[252:253] op_sel_hi:[1,0]\n"
    "v_pk_mul_f32 v[12:13], v[12:13], v[252:253] op_sel_hi:[1,0]\n"
    "v_pk_mul_f32 v[14:15], v[14:15], v[252:253] op_sel_hi:[1,0]\n"
    "v_pk_mul_f32 v[16:17], v[16:17], v[252:253] op_sel_hi:[1,0]\n"
    "v_pk_mul_f32 v[18:19], v[18:19], v[252:253] op_sel_hi:[1,0]\n"
    "v_pk_mul_f32 v[20:21], v[20:21], v[252:253] op_sel_hi:[1,0]\n"
    "v_pk_mul_f32 v[22:23], v[22:23], v[252:253] op_sel_hi:[1,0]\n"
    "v_pk_mul_f32 v[24:25], v[24:25], v[252:253] op_sel_hi:[1,0]\n"
    "v_pk_mul_f32 v[26:27], v[26:27], v[252:253] op_sel_hi:[1,0]\n"
    "v_pk_mul_f32 v[28:29], v[28:29], v[252:253] op_sel_hi:[1,0]\n"
    "v_pk_mul_f32 v[30:31], v[30:31], v[252:253] op_sel_hi:[1,0]\n"
    "v_mfma_f32_16x16x32_bf16 v[32:35], a[128:131], v[216:219], v[32:35]\n"
    "v_exp_f32_e32 v195, v195\n"
    "v_mfma_f32_16x16x32_bf16 v[32:35], a[132:135], v[220:223], v[32:35]\n"
    "s_waitcnt lgkmcnt(0)\n"
    "v_cmp_le_u32_e32 vcc, s76, v253\n"
    "v_cvt_pk_bf16_f32 v209, v194, v195\n"
    "v_mfma_f32_16x16x32_bf16 v[36:39], a[136:139], v[216:219], v[36:39]\n"
    "v_exp_f32_e32 v196, v196\n"
    "v_mfma_f32_16x16x32_bf16 v[36:39], a[140:143], v[220:223], v[36:39]\n"
    "s_add_u32 m0, s62, 50688\n"
    "v_accvgpr_write_b32 a1, v129\n"
    "v_accvgpr_write_b32 a2, v130\n"
    "v_mfma_f32_16x16x32_bf16 v[200:203], a[96:99], a[32:35], v[232:235]\n"
    "s_cbranch_vccz LSPIN10_%=\n"
    "LRET10_%=:\n"
    "v_exp_f32_e32 v197, v197\n"
    "v_mfma_f32_16x16x32_bf16 v[200:203], a[100:103], a[36:39], v[200:203]\n"
    "buffer_load_dwordx4 v242, s[36:39], 0 offen offset:0 nt lds\n"
    "v_accvgpr_read_b32 v253, a228\n"
    "v_mfma_f32_16x16x32_bf16 v[200:203], a[104:107], a[40:43], v[200:203]\n"
    "s_addk_i32 s76, 256\n"
    "v_exp_f32_e32 v198, v198\n"
    "v_mfma_f32_16x16x32_bf16 v[200:203], a[108:111], a[44:47], v[200:203]\n"
    "ds_read_b128 a[212:215], v253 offset:0\n"
    "v_cvt_pk_bf16_f32 v210, v196, v197\n"
    "v_mfma_f32_16x16x32_bf16 v[40:43], a[144:147], v[216:219], v[40:43]\n"
    "v_exp_f32_e32 v199, v199\n"
    "v_mfma_f32_16x16x32_bf16 v[40:43], a[148:151], v[220:223], v[40:43]\n"
    "ds_read_b128 a[216:219], v253 offset:64\n"
    "ds_read_b128 a[220:223], v253 offset:128\n"
    "v_mfma_f32_16x16x32_bf16 v[44:47], a[152:155], v[216:219], v[44:47]\n"
    "v_exp_f32_e32 v200, v200\n"
    "v_mfma_f32_16x16x32_bf16 v[44:47], a[156:159], v[220:223], v[44:47]\n"
    "ds_read_b128 a[224:227], v253 offset:192\n"
    "v_cvt_pk_bf16_f32 v211, v198, v199\n"
    "v_smfmac_f32_16x16x64_bf16 a[196:199], a[208:211], v[216:223], v254\n"
    "v_accvgpr_write_b32 a3, v131\n"
    "v_mfma_f32_16x16x32_bf16 v[204:207], a[112:115], a[32:35], v[232:235]\n"
    "v_exp_f32_e32 v201, v201\n"
    "v_mfma_f32_16x16x32_bf16 v[204:207], a[116:119], a[36:39], v[204:207]\n"
    "v_cvt_pk_bf16_f32 v212, v200, v201\n"
    "v_accvgpr_write_b32 a4, v132\n"
    "v_mfma_f32_16x16x32_bf16 v[204:207], a[120:123], a[40:43], v[204:207]\n"
    "s_add_u32 m0, s62, 50720\n"
    "v_exp_f32_e32 v202, v202\n"
    "v_mfma_f32_16x16x32_bf16 v[204:207], a[124:127], a[44:47], v[204:207]\n"
    "buffer_load_dwordx4 v242, s[36:39], 0 offen offset:1024 nt lds\n"
    "v_accvgpr_read_b32 v252, a196\n"
    "v_mfma_f32_16x16x32_bf16 v[48:51], a[160:163], v[216:219], v[48:51]\n"
    "v_exp_f32_e32 v203, v203\n"
    "v_mfma_f32_16x16x32_bf16 v[48:51], a[164:167], v[220:223], v[48:51]\n"
    "v_accvgpr_read_b32 v246, a197\n"
    "v_cvt_pk_bf16_f32 v213, v202, v203\n"
    "v_mfma_f32_16x16x32_bf16 v[52:55], a[168:171], v[216:219], v[52:55]\n"
    "v_exp_f32_e32 v204, v204\n"
    "v_mfma_f32_16x16x32_bf16 v[52:55], a[172:175], v[220:223], v[52:55]\n"
    "v_add_f32_e32 v252, v252, v246\n"
    "v_accvgpr_write_b32 a5, v133\n"
    "v_mfma_f32_16x16x32_bf16 v[192:195], a[64:67], a[48:51], v[236:239]\n"
    "v_exp_f32_e32 v205, v205\n"
    "v_mfma_f32_16x16x32_bf16 v[192:195], a[68:71], a[52:55], v[192:195]\n"
    "v_cmp_ngt_f32_e64 s[78:79], v254, v252\n"
    "v_cvt_pk_bf16_f32 v214, v204, v205\n"
    "v_mfma_f32_16x16x32_bf16 v[192:195], a[72:75], a[56:59], v[192:195]\n"
    "s_add_u32 m0, s62, 50752\n"
    "v_exp_f32_e32 v206, v206\n"
    "v_mfma_f32_16x16x32_bf16 v[192:195], a[76:79], a[60:63], v[192:195]\n"
    "s_xor_b32 s78, s78, s67\n"
    "v_rcp_f32_e32 v252, v252\n"
    "v_mfma_f32_16x16x32_bf16 v[56:59], a[176:179], v[216:219], v[56:59]\n"
    "s_lshr_b32 s79, s78, 16\n"
    "v_exp_f32_e32 v207, v207\n"
    "v_mfma_f32_16x16x32_bf16 v[56:59], a[180:183], v[220:223], v[56:59]\n"
    "s_or_b32 s78, s78, s79\n"
    "ds_read_b128 a[64:67], v240 offset:0\n"
    "v_cvt_pk_bf16_f32 v215, v206, v207\n"
    "v_mfma_f32_16x16x32_bf16 v[60:63], a[184:187], v[216:219], v[60:63]\n"
    "s_andn2_b32 s78, s78, s67\n"
    "v_exp_f32_e32 v192, v192\n"
    "v_mfma_f32_16x16x32_bf16 v[60:63], a[188:191], v[220:223], v[60:63]\n"
    "buffer_load_dwordx4 v242, s[36:39], 0 offen offset:2048 nt lds\n"
    "ds_read_b128 a[68:71], v240 offset:64\n"
    "v_mfma_f32_16x16x32_bf16 v[196:199], a[80:83], a[48:51], v[236:239]\n"
    "s_lshl_b64 s[86:87], s[86:87], 16\n"
    "v_exp_f32_e32 v193, v193\n"
    "v_mfma_f32_16x16x32_bf16 v[196:199], a[84:87], a[52:55], v[196:199]\n"
    "s_or_b32 s86, s86, s78\n"
    "ds_read_b128 a[72:75], v240 offset:128\n"
    "ds_read_b128 a[76:79], v240 offset:192\n"
    "v_mfma_f32_16x16x32_bf16 v[196:199], a[88:91], a[56:59], v[196:199]\n"
    "v_exp_f32_e32 v194, v194\n"
    "v_mfma_f32_16x16x32_bf16 v[196:199], a[92:95], a[60:63], v[196:199]\n"
    "ds_read_b128 a[80:83], v240 offset:256\n"
    "v_cvt_pk_bf16_f32 v216, v192, v193\n"
    "v_accvgpr_write_b32 a6, v134\n"
    "v_accvgpr_write_b32 a7, v135\n"
    "v_accvgpr_write_b32 a8, v136\n"
    "v_accvgpr_write_b32 a9, v137\n"
    "v_accvgpr_write_b32 a10, v138\n"
    "v_accvgpr_write_b32 a11, v139\n"
    "v_accvgpr_write_b32 a12, v140\n"
    "v_accvgpr_write_b32 a13, v141\n"
    "v_accvgpr_write_b32 a14, v142\n"
    "v_accvgpr_write_b32 a15, v143\n"
    "v_cvt_pk_bf16_f32 v0, v0, v1\n"
    "v_cvt_pk_bf16_f32 v2, v2, v3\n"
    "v_cvt_pk_bf16_f32 v4, v4, v5\n"
    "v_cvt_pk_bf16_f32 v6, v6, v7\n"
    "v_cvt_pk_bf16_f32 v8, v8, v9\n"
    "v_cvt_pk_bf16_f32 v10, v10, v11\n"
    "v_cvt_pk_bf16_f32 v12, v12, v13\n"
    "v_cvt_pk_bf16_f32 v14, v14, v15\n"
    "v_cvt_pk_bf16_f32 v16, v16, v17\n"
    "v_cvt_pk_bf16_f32 v18, v18, v19\n"
    "v_cvt_pk_bf16_f32 v20, v20, v21\n"
    "v_cvt_pk_bf16_f32 v22, v22, v23\n"
    "v_cvt_pk_bf16_f32 v24, v24, v25\n"
    "v_cvt_pk_bf16_f32 v26, v26, v27\n"
    "v_cvt_pk_bf16_f32 v28, v28, v29\n"
    "v_cvt_pk_bf16_f32 v30, v30, v31\n"
    "s_lshl_b64 vcc, s[96:97], 128\n"
    "v_cndmask_b32_dpp v128, v8, v0, vcc quad_perm:[1,0,3,2] row_mask:0xf bank_mask:0xf\n"
    "v_cndmask_b32_dpp v129, v10, v2, vcc quad_perm:[1,0,3,2] row_mask:0xf bank_mask:0xf\n"
    "v_cndmask_b32_dpp v130, v12, v4, vcc quad_perm:[1,0,3,2] row_mask:0xf bank_mask:0xf\n"
    "v_cndmask_b32_dpp v131, v14, v6, vcc quad_perm:[1,0,3,2] row_mask:0xf bank_mask:0xf\n"
    "v_cndmask_b32_dpp v136, v24, v16, vcc quad_perm:[1,0,3,2] row_mask:0xf bank_mask:0xf\n"
    "v_cndmask_b32_dpp v137, v26, v18, vcc quad_perm:[1,0,3,2] row_mask:0xf bank_mask:0xf\n"
    "v_cndmask_b32_dpp v138, v28, v20, vcc quad_perm:[1,0,3,2] row_mask:0xf bank_mask:0xf\n"
    "v_cndmask_b32_dpp v139, v30, v22, vcc quad_perm:[1,0,3,2] row_mask:0xf bank_mask:0xf\n"
    "s_lshl_b64 vcc, s[96:97], 129\n"
    "v_cndmask_b32_dpp v132, v0, v8, vcc quad_perm:[1,0,3,2] row_mask:0xf bank_mask:0xf\n"
    "v_cndmask_b32_dpp v133, v2, v10, vcc quad_perm:[1,0,3,2] row_mask:0xf bank_mask:0xf\n"
    "v_cndmask_b32_dpp v134, v4, v12, vcc quad_perm:[1,0,3,2] row_mask:0xf bank_mask:0xf\n"
    "v_cndmask_b32_dpp v135, v6, v14, vcc quad_perm:[1,0,3,2] row_mask:0xf bank_mask:0xf\n"
    "v_cndmask_b32_dpp v140, v16, v24, vcc quad_perm:[1,0,3,2] row_mask:0xf bank_mask:0xf\n"
    "v_cndmask_b32_dpp v141, v18, v26, vcc quad_perm:[1,0,3,2] row_mask:0xf bank_mask:0xf\n"
    "v_cndmask_b32_dpp v142, v20, v28, vcc quad_perm:[1,0,3,2] row_mask:0xf bank_mask:0xf\n"
    "v_cndmask_b32_dpp v143, v22, v30, vcc quad_perm:[1,0,3,2] row_mask:0xf bank_mask:0xf\n"
    "v_pk_mul_f32 v[32:33], v[32:33], v[252:253] op_sel_hi:[1,0]\n"
    "v_pk_mul_f32 v[34:35], v[34:35], v[252:253] op_sel_hi:[1,0]\n"
    "v_pk_mul_f32 v[36:37], v[36:37], v[252:253] op_sel_hi:[1,0]\n"
    "v_pk_mul_f32 v[38:39], v[38:39], v[252:253] op_sel_hi:[1,0]\n"
    "v_pk_mul_f32 v[40:41], v[40:41], v[252:253] op_sel_hi:[1,0]\n"
    "v_pk_mul_f32 v[42:43], v[42:43], v[252:253] op_sel_hi:[1,0]\n"
    "v_pk_mul_f32 v[44:45], v[44:45], v[252:253] op_sel_hi:[1,0]\n"
    "v_pk_mul_f32 v[46:47], v[46:47], v[252:253] op_sel_hi:[1,0]\n"
    "v_pk_mul_f32 v[48:49], v[48:49], v[252:253] op_sel_hi:[1,0]\n"
    "v_pk_mul_f32 v[50:51], v[50:51], v[252:253] op_sel_hi:[1,0]\n"
    "v_pk_mul_f32 v[52:53], v[52:53], v[252:253] op_sel_hi:[1,0]\n"
    "v_pk_mul_f32 v[54:55], v[54:55], v[252:253] op_sel_hi:[1,0]\n"
    "v_pk_mul_f32 v[56:57], v[56:57], v[252:253] op_sel_hi:[1,0]\n"
    "v_pk_mul_f32 v[58:59], v[58:59], v[252:253] op_sel_hi:[1,0]\n"
    "v_pk_mul_f32 v[60:61], v[60:61], v[252:253] op_sel_hi:[1,0]\n"
    "v_pk_mul_f32 v[62:63], v[62:63], v[252:253] op_sel_hi:[1,0]\n"
    "v_mfma_f32_16x16x32_bf16 v[64:67], a[128:131], v[208:211], v[64:67]\n"
    "v_exp_f32_e32 v195, v195\n"
    "v_mfma_f32_16x16x32_bf16 v[64:67], a[132:135], v[212:215], v[64:67]\n"
    "ds_read_b128 a[84:87], v240 offset:320\n"
    "ds_read_b128 a[88:91], v240 offset:384\n"
    "v_mfma_f32_16x16x32_bf16 v[68:71], a[136:139], v[208:211], v[68:71]\n"
    "v_exp_f32_e32 v196, v196\n"
    "v_mfma_f32_16x16x32_bf16 v[68:71], a[140:143], v[212:215], v[68:71]\n"
    "s_waitcnt lgkmcnt(7)\n"
    "ds_read_b128 a[92:95], v240 offset:448\n"
    "v_cvt_pk_bf16_f32 v217, v194, v195\n"
    "v_mfma_f32_16x16x32_bf16 v[224:227], a[212:215], a[0:3], 0\n"
    "s_add_u32 m0, s62, 50784\n"
    "v_exp_f32_e32 v197, v197\n"
    "v_mfma_f32_16x16x32_bf16 v[224:227], a[216:219], a[4:7], v[224:227]\n"
    "buffer_load_dwordx4 v242, s[36:39], 0 offen offset:3072 nt lds\n"
    "v_cvt_pk_bf16_f32 v218, v196, v197\n"
    "v_mfma_f32_16x16x32_bf16 v[224:227], a[220:223], a[8:11], v[224:227]\n"
    "s_addk_i32 s59, 16384\n"
    "v_exp_f32_e32 v198, v198\n"
    "v_mfma_f32_16x16x32_bf16 v[224:227], a[224:227], a[12:15], v[224:227]\n"
    "s_xor_b32 s71, s59, s63\n"
    "v_accvgpr_write_b32 a196, 0\n"
    "v_accvgpr_write_b32 a197, 0\n"
    "v_mfma_f32_16x16x32_bf16 v[200:203], a[96:99], a[48:51], v[236:239]\n"
    "s_add_u32 s36, s84, s71\n"
    "v_exp_f32_e32 v199, v199\n"
    "v_mfma_f32_16x16x32_bf16 v[200:203], a[100:103], a[52:55], v[200:203]\n"
    "s_addc_u32 s37, s85, 0\n"
    "ds_read_b128 a[96:99], v240 offset:512\n"
    "v_cvt_pk_bf16_f32 v219, v198, v199\n"
    "v_mfma_f32_16x16x32_bf16 v[200:203], a[104:107], a[56:59], v[200:203]\n"
    "s_addk_i32 s58, -16384\n"
    "v_sub_f32_e32 v227, 0, v224\n"
    "v_sub_f32_e32 v226, 0, v224\n"
    "v_mfma_f32_16x16x32_bf16 v[200:203], a[108:111], a[60:63], v[200:203]\n"
    "s_max_i32 s38, s58, 0\n"
    "v_sub_f32_e32 v225, 0, v224\n"
    "v_sub_f32_e32 v224, 0, v224\n"
    "v_mfma_f32_16x16x32_bf16 v[72:75], a[144:147], v[208:211], v[72:75]\n"
    "ds_read_b128 a[100:103], v240 offset:576\n"
    "ds_read_b128 a[104:107], v240 offset:640\n"
    "v_mfma_f32_16x16x32_bf16 v[72:75], a[148:151], v[212:215], v[72:75]\n"
    "ds_read_b128 a[108:111], v240 offset:704\n"
    "v_accvgpr_write_b32 a16, v144\n"
    "v_mfma_f32_16x16x32_bf16 v[76:79], a[152:155], v[208:211], v[76:79]\n"
    "s_add_u32 m0, s62, 118272\n"
    "v_exp_f32_e32 v200, v200\n"
    "v_mfma_f32_16x16x32_bf16 v[76:79], a[156:159], v[212:215], v[76:79]\n"
    "buffer_load_dwordx4 v242, s[40:43], 0 offen offset:0 nt lds\n"
    "v_accvgpr_write_b32 a17, v145\n"
    "v_smfmac_f32_16x16x64_bf16 a[200:203], a[208:211], v[208:215], v254\n"
    "v_accvgpr_write_b32 a18, v146\n"
    "v_mfma_f32_16x16x32_bf16 v[204:207], a[112:115], a[48:51], v[236:239]\n"
    "v_exp_f32_e32 v201, v201\n"
    "v_mfma_f32_16x16x32_bf16 v[204:207], a[116:119], a[52:55], v[204:207]\n"
    "ds_read_b128 a[112:115], v240 offset:768\n"
    "v_cvt_pk_bf16_f32 v220, v200, v201\n"
    "v_mfma_f32_16x16x32_bf16 v[204:207], a[120:123], a[56:59], v[204:207]\n"
    "v_exp_f32_e32 v202, v202\n"
    "v_mfma_f32_16x16x32_bf16 v[204:207], a[124:127], a[60:63], v[204:207]\n"
    "ds_read_b128 a[116:119], v240 offset:832\n"
    "v_accvgpr_read_b32 v252, a200\n"
    "v_mfma_f32_16x16x32_bf16 v[80:83], a[160:163], v[208:211], v[80:83]\n"
    "v_exp_f32_e32 v203, v203\n"
    "v_mfma_f32_16x16x32_bf16 v[80:83], a[164:167], v[212:215], v[80:83]\n"
    "v_accvgpr_read_b32 v246, a201\n"
    "v_cvt_pk_bf16_f32 v221, v202, v203\n"
    "v_mfma_f32_16x16x32_bf16 v[84:87], a[168:171], v[208:211], v[84:87]\n"
    "v_exp_f32_e32 v204, v204\n"
    "v_mfma_f32_16x16x32_bf16 v[84:87], a[172:175], v[212:215], v[84:87]\n"
    "s_waitcnt lgkmcnt(10)\n"
    "ds_read_b128 a[120:123], v240 offset:896\n"
    "v_add_f32_e32 v252, v252, v246\n"
    "s_add_u32 s70, s70, 1\n"
    "v_mfma_f32_16x16x32_bf16 v[192:195], a[64:67], a[0:3], v[224:227]\n"
    "v_exp_f32_e64 v205, v205\n"
    "v_mfma_f32_16x16x32_bf16 v[192:195], a[68:71], a[4:7], v[192:195]\n"
    "buffer_store_dwordx4 v[128:131], v250, s[48:51], 0 offen offset:0 sc0 sc1 nt\n"
    "ds_read_b128 a[124:127], v240 offset:960\n"
    "v_cmp_ngt_f32_e64 s[78:79], v254, v252\n"
    "v_cvt_pk_bf16_f32 v222, v204, v205\n"
    "v_accvgpr_write_b32 a19, v147\n"
    "v_mfma_f32_16x16x32_bf16 v[192:195], a[72:75], a[8:11], v[192:195]\n"
    "s_waitcnt lgkmcnt(8)\n"
    "v_exp_f32_e32 v206, v206\n"
    "v_mfma_f32_16x16x32_bf16 v[192:195], a[76:79], a[12:15], v[192:195]\n"
    "s_xor_b32 s78, s78, s67\n"
    "v_rcp_f32_e32 v252, v252\n"
    "v_mfma_f32_16x16x32_bf16 v[88:91], a[176:179], v[208:211], v[88:91]\n"
    "s_lshr_b32 s79, s78, 16\n"
    "v_exp_f32_e32 v207, v207\n"
    "v_mfma_f32_16x16x32_bf16 v[88:91], a[180:183], v[212:215], v[88:91]\n"
    "s_or_b32 s78, s78, s79\n"
    "v_cvt_pk_bf16_f32 v223, v206, v207\n"
    "v_accvgpr_write_b32 a20, v148\n"
    "v_mfma_f32_16x16x32_bf16 v[92:95], a[184:187], v[208:211], v[92:95]\n"
    "s_andn2_b32 s78, s78, s67\n"
    "v_exp_f32_e32 v192, v192\n"
    "v_mfma_f32_16x16x32_bf16 v[92:95], a[188:191], v[212:215], v[92:95]\n"
    "s_lshl_b64 s[98:99], s[98:99], 16\n"
    "v_accvgpr_write_b32 a21, v149\n"
    "v_accvgpr_write_b32 a22, v150\n"
    "v_mfma_f32_16x16x32_bf16 v[196:199], a[80:83], a[0:3], v[224:227]\n"
    "s_or_b32 s98, s98, s78\n"
    "v_exp_f32_e32 v193, v193\n"
    "v_mfma_f32_16x16x32_bf16 v[196:199], a[84:87], a[4:7], v[196:199]\n"
    "v_accvgpr_write_b32 a23, v151\n"
    "v_cvt_pk_bf16_f32 v208, v192, v193\n"
    "v_mfma_f32_16x16x32_bf16 v[196:199], a[88:91], a[8:11], v[196:199]\n"
    "v_exp_f32_e64 v194, v194\n"
    "v_mfma_f32_16x16x32_bf16 v[196:199], a[92:95], a[12:15], v[196:199]\n"
    "v_accvgpr_write_b32 a200, 0\n"
    "v_accvgpr_write_b32 a201, 0\n"
    "v_accvgpr_write_b32 a24, v152\n"
    "v_accvgpr_write_b32 a25, v153\n"
    "v_accvgpr_write_b32 a26, v154\n"
    "v_accvgpr_write_b32 a27, v155\n"
    "v_accvgpr_write_b32 a28, v156\n"
    "v_accvgpr_write_b32 a29, v157\n"
    "v_accvgpr_write_b32 a30, v158\n"
    "v_accvgpr_write_b32 a31, v159\n"
    "v_cvt_pk_bf16_f32 v32, v32, v33\n"
    "v_cvt_pk_bf16_f32 v34, v34, v35\n"
    "v_cvt_pk_bf16_f32 v36, v36, v37\n"
    "v_cvt_pk_bf16_f32 v38, v38, v39\n"
    "v_cvt_pk_bf16_f32 v40, v40, v41\n"
    "v_cvt_pk_bf16_f32 v42, v42, v43\n"
    "v_cvt_pk_bf16_f32 v44, v44, v45\n"
    "v_cvt_pk_bf16_f32 v46, v46, v47\n"
    "v_cvt_pk_bf16_f32 v48, v48, v49\n"
    "v_cvt_pk_bf16_f32 v50, v50, v51\n"
    "v_cvt_pk_bf16_f32 v52, v52, v53\n"
    "v_cvt_pk_bf16_f32 v54, v54, v55\n"
    "v_cvt_pk_bf16_f32 v56, v56, v57\n"
    "v_cvt_pk_bf16_f32 v58, v58, v59\n"
    "v_cvt_pk_bf16_f32 v60, v60, v61\n"
    "v_cvt_pk_bf16_f32 v62, v62, v63\n"
    "s_lshl_b64 vcc, s[96:97], 128\n"
    "v_cndmask_b32_dpp v144, v40, v32, vcc quad_perm:[1,0,3,2] row_mask:0xf bank_mask:0xf\n"
    "v_cndmask_b32_dpp v145, v42, v34, vcc quad_perm:[1,0,3,2] row_mask:0xf bank_mask:0xf\n"
    "v_cndmask_b32_dpp v146, v44, v36, vcc quad_perm:[1,0,3,2] row_mask:0xf bank_mask:0xf\n"
    "v_cndmask_b32_dpp v147, v46, v38, vcc quad_perm:[1,0,3,2] row_mask:0xf bank_mask:0xf\n"
    "v_cndmask_b32_dpp v152, v56, v48, vcc quad_perm:[1,0,3,2] row_mask:0xf bank_mask:0xf\n"
    "v_cndmask_b32_dpp v153, v58, v50, vcc quad_perm:[1,0,3,2] row_mask:0xf bank_mask:0xf\n"
    "v_cndmask_b32_dpp v154, v60, v52, vcc quad_perm:[1,0,3,2] row_mask:0xf bank_mask:0xf\n"
    "v_cndmask_b32_dpp v155, v62, v54, vcc quad_perm:[1,0,3,2] row_mask:0xf bank_mask:0xf\n"
    "s_lshl_b64 vcc, s[96:97], 129\n"
    "v_cndmask_b32_dpp v148, v32, v40, vcc quad_perm:[1,0,3,2] row_mask:0xf bank_mask:0xf\n"
    "v_cndmask_b32_dpp v149, v34, v42, vcc quad_perm:[1,0,3,2] row_mask:0xf bank_mask:0xf\n"
    "v_cndmask_b32_dpp v150, v36, v44, vcc quad_perm:[1,0,3,2] row_mask:0xf bank_mask:0xf\n"
    "v_cndmask_b32_dpp v151, v38, v46, vcc quad_perm:[1,0,3,2] row_mask:0xf bank_mask:0xf\n"
    "v_cndmask_b32_dpp v156, v48, v56, vcc quad_perm:[1,0,3,2] row_mask:0xf bank_mask:0xf\n"
    "v_cndmask_b32_dpp v157, v50, v58, vcc quad_perm:[1,0,3,2] row_mask:0xf bank_mask:0xf\n"
    "v_cndmask_b32_dpp v158, v52, v60, vcc quad_perm:[1,0,3,2] row_mask:0xf bank_mask:0xf\n"
    "v_cndmask_b32_dpp v159, v54, v62, vcc quad_perm:[1,0,3,2] row_mask:0xf bank_mask:0xf\n"
    "v_pk_mul_f32 v[64:65], v[64:65], v[252:253] op_sel_hi:[1,0]\n"
    "v_pk_mul_f32 v[66:67], v[66:67], v[252:253] op_sel_hi:[1,0]\n"
    "v_pk_mul_f32 v[68:69], v[68:69], v[252:253] op_sel_hi:[1,0]\n"
    "v_pk_mul_f32 v[70:71], v[70:71], v[252:253] op_sel_hi:[1,0]\n"
    "v_pk_mul_f32 v[72:73], v[72:73], v[252:253] op_sel_hi:[1,0]\n"
    "v_pk_mul_f32 v[74:75], v[74:75], v[252:253] op_sel_hi:[1,0]\n"
    "v_pk_mul_f32 v[76:77], v[76:77], v[252:253] op_sel_hi:[1,0]\n"
    "v_pk_mul_f32 v[78:79], v[78:79], v[252:253] op_sel_hi:[1,0]\n"
    "v_pk_mul_f32 v[80:81], v[80:81], v[252:253] op_sel_hi:[1,0]\n"
    "v_pk_mul_f32 v[82:83], v[82:83], v[252:253] op_sel_hi:[1,0]\n"
    "v_pk_mul_f32 v[84:85], v[84:85], v[252:253] op_sel_hi:[1,0]\n"
    "v_pk_mul_f32 v[86:87], v[86:87], v[252:253] op_sel_hi:[1,0]\n"
    "v_pk_mul_f32 v[88:89], v[88:89], v[252:253] op_sel_hi:[1,0]\n"
    "v_pk_mul_f32 v[90:91], v[90:91], v[252:253] op_sel_hi:[1,0]\n"
    "v_pk_mul_f32 v[92:93], v[92:93], v[252:253] op_sel_hi:[1,0]\n"
    "v_pk_mul_f32 v[94:95], v[94:95], v[252:253] op_sel_hi:[1,0]\n"
    "v_mfma_f32_16x16x32_bf16 v[96:99], a[128:131], v[216:219], v[96:99]\n"
    "s_add_u32 m0, s95, 2112\n"
    "v_exp_f32_e32 v195, v195\n"
    "v_mfma_f32_16x16x32_bf16 v[96:99], a[132:135], v[220:223], v[96:99]\n"
    "buffer_load_dwordx4 v243, s[44:47], 0 offen offset:0 lds\n"
    "v_cvt_pk_bf16_f32 v209, v194, v195\n"
    "v_mfma_f32_16x16x32_bf16 v[100:103], a[136:139], v[216:219], v[100:103]\n"
    "v_exp_f32_e32 v196, v196\n"
    "v_mfma_f32_16x16x32_bf16 v[100:103], a[140:143], v[220:223], v[100:103]\n"
    "ds_read_b64_tr_b16 a[128:129], v241 offset:0\n"
    "ds_read_b64_tr_b16 a[130:131], v241 offset:256\n"
    "v_mfma_f32_16x16x32_bf16 v[228:231], a[212:215], a[16:19], 0\n"
    "v_exp_f32_e32 v197, v197\n"
    "v_mfma_f32_16x16x32_bf16 v[228:231], a[216:219], a[20:23], v[228:231]\n"
    "ds_read_b64_tr_b16 a[132:133], v241 offset:512\n"
    "ds_read_b64_tr_b16 a[134:135], v241 offset:768\n"
    "v_mfma_f32_16x16x32_bf16 v[228:231], a[220:223], a[24:27], v[228:231]\n"
    "v_exp_f32_e32 v198, v198\n"
    "v_mfma_f32_16x16x32_bf16 v[228:231], a[224:227], a[28:31], v[228:231]\n"
    "s_waitcnt lgkmcnt(8)\n"
    "ds_read_b64_tr_b16 a[136:137], v241 offset:8\n"
    "ds_read_b64_tr_b16 a[138:139], v241 offset:264\n"
    "v_mfma_f32_16x16x32_bf16 v[200:203], a[96:99], a[0:3], v[224:227]\n"
    "v_exp_f32_e32 v199, v199\n"
    "v_mfma_f32_16x16x32_bf16 v[200:203], a[100:103], a[4:7], v[200:203]\n"
    "ds_read_b64_tr_b16 a[140:141], v241 offset:520\n"
    "ds_read_b64_tr_b16 a[142:143], v241 offset:776\n"
    "v_mfma_f32_16x16x32_bf16 v[200:203], a[104:107], a[8:11], v[200:203]\n"
    "s_add_u32 m0, s95, 1152\n"
    "v_sub_f32_e32 v231, 0, v228\n"
    "v_sub_f32_e32 v230, 0, v228\n"
    "v_mfma_f32_16x16x32_bf16 v[200:203], a[108:111], a[12:15], v[200:203]\n"
    "s_waitcnt vmcnt(15)\n"
    "ds_read_b128 v[184:187], v255 offset:0\n"
    "ds_read_b128 v[188:191], v255 offset:64\n"
    "v_mfma_f32_16x16x32_bf16 v[104:107], a[144:147], v[216:219], v[104:107]\n"
    "buffer_load_dwordx4 v243, s[44:47], 0 offen offset:2048 lds\n"
    "v_sub_f32_e32 v229, 0, v228\n"
    "v_mfma_f32_16x16x32_bf16 v[104:107], a[148:151], v[220:223], v[104:107]\n"
    "v_sub_f32_e32 v228, 0, v228\n"
    "v_cvt_pk_bf16_f32 v210, v196, v197\n"
    "v_mfma_f32_16x16x32_bf16 v[108:111], a[152:155], v[216:219], v[108:111]\n"
    "v_exp_f32_e32 v200, v200\n"
    "v_mfma_f32_16x16x32_bf16 v[108:111], a[156:159], v[220:223], v[108:111]\n"
    "ds_read_b64_tr_b16 a[144:145], v241 offset:16\n"
    "v_cvt_pk_bf16_f32 v211, v198, v199\n"
    "v_smfmac_f32_16x16x64_bf16 a[204:207], a[208:211], v[216:223], v254\n"
    "s_waitcnt lgkmcnt(11)\n"
    "ds_read_b64_tr_b16 a[146:147], v241 offset:272\n"
    "v_mfma_f32_16x16x32_bf16 v[204:207], a[112:115], a[0:3], v[224:227]\n"
    "s_waitcnt lgkmcnt(2)\n"
    "v_exp_f32_e32 v201, v201\n"
    "v_mfma_f32_16x16x32_bf16 v[204:207], a[116:119], a[4:7], v[204:207]\n"
    "ds_read_b64_tr_b16 a[148:149], v241 offset:528\n"
    "v_cvt_pk_bf16_f32 v212, v200, v201\n"
    "v_mfma_f32_16x16x32_bf16 v[204:207], a[120:123], a[8:11], v[204:207]\n"
    "LW7_%=:\n"
    "s_cbranch_execz LWR_%=\n"
    "v_exp_f32_e32 v202, v202\n"
    "v_mfma_f32_16x16x32_bf16 v[204:207], a[124:127], a[12:15], v[204:207]\n"
    "ds_read_b64_tr_b16 a[150:151], v241 offset:784\n"
    "v_accvgpr_read_b32 v252, a204\n"
    "v_mfma_f32_16x16x32_bf16 v[112:115], a[160:163], v[216:219], v[112:115]\n"
    "v_exp_f32_e32 v203, v203\n"
    "v_mfma_f32_16x16x32_bf16 v[112:115], a[164:167], v[220:223], v[112:115]\n"
    "buffer_store_dwordx4 v[132:135], v250, s[48:51], 0 offen offset:256 sc0 sc1 nt\n"
    "ds_read_b64_tr_b16 a[152:153], v241 offset:24\n"
    "ds_read_b64_tr_b16 a[154:155], v241 offset:280\n"
    "v_accvgpr_read_b32 v246, a205\n"
    "v_cvt_pk_bf16_f32 v213, v202, v203\n"
    "v_mfma_f32_16x16x32_bf16 v[116:119], a[168:171], v[216:219], v[116:119]\n"
    "v_exp_f32_e32 v204, v204\n"
    "v_mfma_f32_16x16x32_bf16 v[116:119], a[172:175], v[220:223], v[116:119]\n"
    "ds_read_b64_tr_b16 a[156:157], v241 offset:536\n"
    "v_add_f32_e32 v252, v252, v246\n"
    "v_mfma_f32_16x16x32_bf16 v[192:195], a[64:67], a[16:19], v[228:231]\n"
    "v_exp_f32_e32 v205, v205\n"
    "v_mfma_f32_16x16x32_bf16 v[192:195], a[68:71], a[20:23], v[192:195]\n"
    "v_cmp_ngt_f32_e64 s[78:79], v254, v252\n"
    "v_cvt_pk_bf16_f32 v214, v204, v205\n"
    "v_mfma_f32_16x16x32_bf16 v[192:195], a[72:75], a[24:27], v[192:195]\n"
    "s_add_u32 m0, s62, 118280\n"
    "v_exp_f32_e32 v206, v206\n"
    "v_mfma_f32_16x16x32_bf16 v[192:195], a[76:79], a[28:31], v[192:195]\n"
    "s_xor_b32 s78, s78, s67\n"
    "v_rcp_f32_e32 v252, v252\n"
    "v_mfma_f32_16x16x32_bf16 v[120:123], a[176:179], v[216:219], v[120:123]\n"
    "s_lshr_b32 s79, s78, 16\n"
    "v_exp_f32_e32 v207, v207\n"
    "v_mfma_f32_16x16x32_bf16 v[120:123], a[180:183], v[220:223], v[120:123]\n"
    "s_or_b32 s78, s78, s79\n"
    "ds_read_b64_tr_b16 a[158:159], v241 offset:792\n"
    "v_cvt_pk_bf16_f32 v215, v206, v207\n"
    "v_mfma_f32_16x16x32_bf16 v[124:127], a[184:187], v[216:219], v[124:127]\n"
    "s_andn2_b32 s78, s78, s67\n"
    "v_exp_f32_e32 v192, v192\n"
    "v_mfma_f32_16x16x32_bf16 v[124:127], a[188:191], v[220:223], v[124:127]\n"
    "buffer_load_dwordx4 v242, s[40:43], 0 offen offset:1024 nt lds\n"
    "ds_read_b64_tr_b16 a[160:161], v241 offset:128\n"
    "v_mfma_f32_16x16x32_bf16 v[196:199], a[80:83], a[16:19], v[228:231]\n"
    "s_lshl_b64 s[90:91], s[90:91], 16\n"
    "v_exp_f32_e32 v193, v193\n"
    "v_mfma_f32_16x16x32_bf16 v[196:199], a[84:87], a[20:23], v[196:199]\n"
    "s_or_b32 s90, s90, s78\n"
    "ds_read_b64_tr_b16 a[162:163], v241 offset:384\n"
    "ds_read_b64_tr_b16 a[164:165], v241 offset:640\n"
    "v_mfma_f32_16x16x32_bf16 v[196:199], a[88:91], a[24:27], v[196:199]\n"
    "v_exp_f32_e32 v194, v194\n"
    "v_mfma_f32_16x16x32_bf16 v[196:199], a[92:95], a[28:31], v[196:199]\n"
    "s_waitcnt lgkmcnt(14)\n"
    "ds_read_b64_tr_b16 a[166:167], v241 offset:896\n"
    "ds_read_b64_tr_b16 a[168:169], v241 offset:136\n"
    "v_accvgpr_write_b32 a32, v160\n"
    "v_accvgpr_write_b32 a33, v161\n"
    "v_accvgpr_write_b32 a34, v162\n"
    "v_accvgpr_write_b32 a35, v163\n"
    "v_accvgpr_write_b32 a36, v164\n"
    "v_accvgpr_write_b32 a37, v165\n"
    "v_accvgpr_write_b32 a38, v166\n"
    "v_accvgpr_write_b32 a39, v167\n"
    "v_accvgpr_write_b32 a40, v168\n"
    "v_accvgpr_write_b32 a41, v169\n"
    "v_accvgpr_write_b32 a42, v170\n"
    "v_accvgpr_write_b32 a43, v171\n"
    "v_accvgpr_write_b32 a44, v172\n"
    "v_accvgpr_write_b32 a45, v173\n"
    "v_accvgpr_write_b32 a46, v174\n"
    "v_accvgpr_write_b32 a47, v175\n"
    "v_lshlrev_b32_e32 v246, 16, v176\n"
    "v_and_b32_e32 v247, s67, v176\n"
    "v_lshlrev_b32_e32 v248, 16, v177\n"
    "v_and_b32_e32 v249, s67, v177\n"
    "v_pk_mul_f32 v[246:247], v[246:247], s[64:65]\n"
    "v_pk_mul_f32 v[248:249], v[248:249], s[64:65]\n"
    "v_cvt_pk_bf16_f32 v176, v246, v247\n"
    "v_cvt_pk_bf16_f32 v177, v248, v249\n"
    "v_lshlrev_b32_e32 v246, 16, v178\n"
    "v_and_b32_e32 v247, s67, v178\n"
    "v_lshlrev_b32_e32 v248, 16, v179\n"
    "v_and_b32_e32 v249, s67, v179\n"
    "v_pk_mul_f32 v[246:247], v[246:247], s[64:65]\n"
    "v_pk_mul_f32 v[248:249], v[248:249], s[64:65]\n"
    "v_cvt_pk_bf16_f32 v178, v246, v247\n"
    "v_cvt_pk_bf16_f32 v179, v248, v249\n"
    "v_accvgpr_write_b32 a48, v176\n"
    "v_accvgpr_write_b32 a49, v177\n"
    "v_accvgpr_write_b32 a50, v178\n"
    "v_accvgpr_write_b32 a51, v179\n"
    "v_lshlrev_b32_e32 v246, 16, v180\n"
    "v_and_b32_e32 v247, s67, v180\n"
    "v_lshlrev_b32_e32 v248, 16, v181\n"
    "v_and_b32_e32 v249, s67, v181\n"
    "v_pk_mul_f32 v[246:247], v[246:247], s[64:65]\n"
    "v_pk_mul_f32 v[248:249], v[248:249], s[64:65]\n"
    "v_cvt_pk_bf16_f32 v180, v246, v247\n"
    "v_cvt_pk_bf16_f32 v181, v248, v249\n"
    "v_lshlrev_b32_e32 v246, 16, v182\n"
    "v_and_b32_e32 v247, s67, v182\n"
    "v_lshlrev_b32_e32 v248, 16, v183\n"
    "v_and_b32_e32 v249, s67, v183\n"
    "v_pk_mul_f32 v[246:247], v[246:247], s[64:65]\n"
    "v_pk_mul_f32 v[248:249], v[248:249], s[64:65]\n"
    "v_cvt_pk_bf16_f32 v182, v246, v247\n"
    "v_cvt_pk_bf16_f32 v183, v248, v249\n"
    "v_accvgpr_write_b32 a52, v180\n"
    "v_accvgpr_write_b32 a53, v181\n"
    "v_accvgpr_write_b32 a54, v182\n"
    "v_accvgpr_write_b32 a55, v183\n"
    "v_lshlrev_b32_e32 v246, 16, v184\n"
    "v_and_b32_e32 v247, s67, v184\n"
    "v_lshlrev_b32_e32 v248, 16, v185\n"
    "v_and_b32_e32 v249, s67, v185\n"
    "v_pk_mul_f32 v[246:247], v[246:247], s[64:65]\n"
    "v_pk_mul_f32 v[248:249], v[248:249], s[64:65]\n"
    "v_cvt_pk_bf16_f32 v184, v246, v247\n"
    "v_cvt_pk_bf16_f32 v185, v248, v249\n"
    "v_lshlrev_b32_e32 v246, 16, v186\n"
    "v_and_b32_e32 v247, s67, v186\n"
    "v_lshlrev_b32_e32 v248, 16, v187\n"
    "v_and_b32_e32 v249, s67, v187\n"
    "v_pk_mul_f32 v[246:247], v[246:247], s[64:65]\n"
    "v_pk_mul_f32 v[248:249], v[248:249], s[64:65]\n"
    "v_cvt_pk_bf16_f32 v186, v246, v247\n"
    "v_cvt_pk_bf16_f32 v187, v248, v249\n"
    "v_accvgpr_write_b32 a56, v184\n"
    "v_accvgpr_write_b32 a57, v185\n"
    "v_accvgpr_write_b32 a58, v186\n"
    "v_accvgpr_write_b32 a59, v187\n"
    "v_lshlrev_b32_e32 v246, 16, v188\n"
    "v_and_b32_e32 v247, s67, v188\n"
    "v_lshlrev_b32_e32 v248, 16, v189\n"
    "v_and_b32_e32 v249, s67, v189\n"
    "v_pk_mul_f32 v[246:247], v[246:247], s[64:65]\n"
    "v_pk_mul_f32 v[248:249], v[248:249], s[64:65]\n"
    "v_cvt_pk_bf16_f32 v188, v246, v247\n"
    "v_cvt_pk_bf16_f32 v189, v248, v249\n"
    "v_lshlrev_b32_e32 v246, 16, v190\n"
    "v_and_b32_e32 v247, s67, v190\n"
    "v_lshlrev_b32_e32 v248, 16, v191\n"
    "v_and_b32_e32 v249, s67, v191\n"
    "v_pk_mul_f32 v[246:247], v[246:247], s[64:65]\n"
    "v_pk_mul_f32 v[248:249], v[248:249], s[64:65]\n"
    "v_cvt_pk_bf16_f32 v190, v246, v247\n"
    "v_cvt_pk_bf16_f32 v191, v248, v249\n"
    "v_accvgpr_write_b32 a60, v188\n"
    "v_accvgpr_write_b32 a61, v189\n"
    "v_accvgpr_write_b32 a62, v190\n"
    "v_accvgpr_write_b32 a63, v191\n"
    "v_pk_mul_f32 v[96:97], v[96:97], v[252:253] op_sel_hi:[1,0]\n"
    "v_pk_mul_f32 v[98:99], v[98:99], v[252:253] op_sel_hi:[1,0]\n"
    "v_pk_mul_f32 v[100:101], v[100:101], v[252:253] op_sel_hi:[1,0]\n"
    "v_pk_mul_f32 v[102:103], v[102:103], v[252:253] op_sel_hi:[1,0]\n"
    "v_pk_mul_f32 v[104:105], v[104:105], v[252:253] op_sel_hi:[1,0]\n"
    "v_pk_mul_f32 v[106:107], v[106:107], v[252:253] op_sel_hi:[1,0]\n"
    "v_pk_mul_f32 v[108:109], v[108:109], v[252:253] op_sel_hi:[1,0]\n"
    "v_pk_mul_f32 v[110:111], v[110:111], v[252:253] op_sel_hi:[1,0]\n"
    "v_pk_mul_f32 v[112:113], v[112:113], v[252:253] op_sel_hi:[1,0]\n"
    "v_pk_mul_f32 v[114:115], v[114:115], v[252:253] op_sel_hi:[1,0]\n"
    "v_pk_mul_f32 v[116:117], v[116:117], v[252:253] op_sel_hi:[1,0]\n"
    "v_pk_mul_f32 v[118:119], v[118:119], v[252:253] op_sel_hi:[1,0]\n"
    "v_pk_mul_f32 v[120:121], v[120:121], v[252:253] op_sel_hi:[1,0]\n"
    "v_pk_mul_f32 v[122:123], v[122:123], v[252:253] op_sel_hi:[1,0]\n"
    "v_pk_mul_f32 v[124:125], v[124:125], v[252:253] op_sel_hi:[1,0]\n"
    "v_pk_mul_f32 v[126:127], v[126:127], v[252:253] op_sel_hi:[1,0]\n"
    "v_mfma_f32_16x16x32_bf16 v[0:3], a[128:131], v[208:211], 0\n"
    "v_exp_f32_e32 v195, v195\n"
    "v_mfma_f32_16x16x32_bf16 v[0:3], a[132:135], v[212:215], v[0:3]\n"
    "s_waitcnt lgkmcnt(14)\n"
    "ds_read_b64_tr_b16 a[170:171], v241 offset:392\n"
    "ds_read_b64_tr_b16 a[172:173], v241 offset:648\n"
    "v_mfma_f32_16x16x32_bf16 v[4:7], a[136:139], v[208:211], 0\n"
    "v_exp_f32_e32 v196, v196\n"
    "v_mfma_f32_16x16x32_bf16 v[4:7], a[140:143], v[212:215], v[4:7]\n"
    "ds_read_b64_tr_b16 a[174:175], v241 offset:904\n"
    "ds_read_b64_tr_b16 a[176:177], v241 offset:144\n"
    "v_mfma_f32_16x16x32_bf16 v[232:235], a[212:215], a[32:35], 0\n"
    "s_add_u32 m0, s62, 118288\n"
    "v_exp_f32_e32 v197, v197\n"
    "v_mfma_f32_16x16x32_bf16 v[232:235], a[216:219], a[36:39], v[232:235]\n"
    "buffer_load_dwordx4 v242, s[40:43], 0 offen offset:2048 nt lds\n"
    "ds_read_b64_tr_b16 a[178:179], v241 offset:400\n"
    "v_mfma_f32_16x16x32_bf16 v[232:235], a[220:223], a[40:43], v[232:235]\n"
    "v_exp_f32_e32 v198, v198\n"
    "v_mfma_f32_16x16x32_bf16 v[232:235], a[224:227], a[44:47], v[232:235]\n"
    "ds_read_b64_tr_b16 a[180:181], v241 offset:656\n"
    "ds_read_b64_tr_b16 a[182:183], v241 offset:912\n"
    "v_mfma_f32_16x16x32_bf16 v[200:203], a[96:99], a[16:19], v[228:231]\n"
    "v_exp_f32_e32 v199, v199\n"
    "v_mfma_f32_16x16x32_bf16 v[200:203], a[100:103], a[20:23], v[200:203]\n"
    "ds_read_b64_tr_b16 a[184:185], v241 offset:152\n"
    "ds_read_b64_tr_b16 a[186:187], v241 offset:408\n"
    "v_mfma_f32_16x16x32_bf16 v[200:203], a[104:107], a[24:27], v[200:203]\n"
    "ds_read_b64_tr_b16 a[188:189], v241 offset:664\n"
    "ds_read_b64_tr_b16 a[190:191], v241 offset:920\n"
    "v_mfma_f32_16x16x32_bf16 v[200:203], a[108:111], a[28:31], v[200:203]\n"
    "s_waitcnt lgkmcnt(14)\n"
    "v_sub_f32_e32 v235, 0, v232\n"
    "v_sub_f32_e32 v234, 0, v232\n"
    "v_mfma_f32_16x16x32_bf16 v[8:11], a[144:147], v[208:211], 0\n"
    "v_sub_f32_e32 v233, 0, v232\n"
    "v_sub_f32_e32 v232, 0, v232\n"
    "v_mfma_f32_16x16x32_bf16 v[8:11], a[148:151], v[212:215], v[8:11]\n"
    "s_waitcnt lgkmcnt(14)\n"
    "v_cvt_pk_bf16_f32 v216, v192, v193\n"
    "v_cvt_pk_bf16_f32 v217, v194, v195\n"
    "v_mfma_f32_16x16x32_bf16 v[12:15], a[152:155], v[208:211], 0\n"
    "s_add_u32 m0, s62, 118296\n"
    "v_exp_f32_e32 v200, v200\n"
    "v_mfma_f32_16x16x32_bf16 v[12:15], a[156:159], v[212:215], v[12:15]\n"
    "buffer_load_dwordx4 v242, s[40:43], 0 offen offset:3072 nt lds\n"
    "v_cvt_pk_bf16_f32 v218, v196, v197\n"
    "v_smfmac_f32_16x16x64_bf16 a[192:195], a[208:211], v[208:215], v254\n"
    "s_add_u32 s40, s60, s71\n"
    "v_cvt_pk_bf16_f32 v219, v198, v199\n"
    "v_mfma_f32_16x16x32_bf16 v[204:207], a[112:115], a[16:19], v[228:231]\n"
    "s_addc_u32 s41, s61, 0\n"
    "v_exp_f32_e32 v201, v201\n"
    "v_mfma_f32_16x16x32_bf16 v[204:207], a[116:119], a[20:23], v[204:207]\n"
    "s_waitcnt vmcnt(24)\n"
    "ds_append v253\n"
    "v_cvt_pk_bf16_f32 v220, v200, v201\n"
    "v_mfma_f32_16x16x32_bf16 v[204:207], a[120:123], a[24:27], v[204:207]\n"
    "s_max_i32 s42, s58, 0\n"
    "v_exp_f32_e32 v202, v202\n"
    "v_mfma_f32_16x16x32_bf16 v[204:207], a[124:127], a[28:31], v[204:207]\n"
    "s_waitcnt lgkmcnt(13)\n"
    "v_accvgpr_write_b32 a204, 0\n"
    "v_accvgpr_write_b32 a205, 0\n"
    "v_mfma_f32_16x16x32_bf16 v[16:19], a[160:163], v[208:211], 0\n"
    "v_exp_f32_e32 v203, v203\n"
    "v_mfma_f32_16x16x32_bf16 v[16:19], a[164:167], v[212:215], v[16:19]\n"
    "s_waitcnt lgkmcnt(9)\n"
    "v_cvt_pk_bf16_f32 v221, v202, v203\n"
    "v_cvt_pk_bf16_f32 v64, v64, v65\n"
    "v_mfma_f32_16x16x32_bf16 v[20:23], a[168:171], v[208:211], 0\n"
    "v_exp_f32_e32 v204, v204\n"
    "v_mfma_f32_16x16x32_bf16 v[20:23], a[172:175], v[212:215], v[20:23]\n"
    "v_cvt_pk_bf16_f32 v66, v66, v67\n"
    "v_cvt_pk_bf16_f32 v68, v68, v69\n"
    "v_mfma_f32_16x16x32_bf16 v[192:195], a[64:67], a[32:35], v[232:235]\n"
    "v_exp_f32_e32 v205, v205\n"
    "v_mfma_f32_16x16x32_bf16 v[192:195], a[68:71], a[36:39], v[192:195]\n"
    "v_cvt_pk_bf16_f32 v222, v204, v205\n"
    "v_cvt_pk_bf16_f32 v70, v70, v71\n"
    "v_mfma_f32_16x16x32_bf16 v[192:195], a[72:75], a[40:43], v[192:195]\n"
    "v_exp_f32_e32 v206, v206\n"
    "v_mfma_f32_16x16x32_bf16 v[192:195], a[76:79], a[44:47], v[192:195]\n"
    "s_waitcnt lgkmcnt(5)\n"
    "v_cvt_pk_bf16_f32 v72, v72, v73\n"
    "v_cvt_pk_bf16_f32 v74, v74, v75\n"
    "v_mfma_f32_16x16x32_bf16 v[24:27], a[176:179], v[208:211], 0\n"
    "v_exp_f32_e32 v207, v207\n"
    "v_mfma_f32_16x16x32_bf16 v[24:27], a[180:183], v[212:215], v[24:27]\n"
    "s_waitcnt lgkmcnt(1)\n"
    "v_cvt_pk_bf16_f32 v223, v206, v207\n"
    "v_cvt_pk_bf16_f32 v76, v76, v77\n"
    "v_mfma_f32_16x16x32_bf16 v[28:31], a[184:187], v[208:211], 0\n"
    "v_exp_f32_e32 v192, v192\n"
    "v_mfma_f32_16x16x32_bf16 v[28:31], a[188:191], v[212:215], v[28:31]\n"
    "ds_read_b32 v253, v251\n"
    "v_cvt_pk_bf16_f32 v78, v78, v79\n"
    "v_mfma_f32_16x16x32_bf16 v[196:199], a[80:83], a[32:35], v[232:235]\n"
    "v_exp_f32_e32 v193, v193\n"
    "v_mfma_f32_16x16x32_bf16 v[196:199], a[84:87], a[36:39], v[196:199]\n"
    "buffer_store_dwordx4 v[136:139], v250, s[48:51], 0 offen offset:128 sc0 sc1 nt\n"
    "v_cvt_pk_bf16_f32 v208, v192, v193\n"
    "v_cvt_pk_bf16_f32 v80, v80, v81\n"
    "v_cvt_pk_bf16_f32 v82, v82, v83\n"
    "v_cvt_pk_bf16_f32 v84, v84, v85\n"
    "v_mfma_f32_16x16x32_bf16 v[196:199], a[88:91], a[40:43], v[196:199]\n"
    "v_exp_f32_e32 v194, v194\n"
    "v_mfma_f32_16x16x32_bf16 v[196:199], a[92:95], a[44:47], v[196:199]\n"
    "v_cvt_pk_bf16_f32 v86, v86, v87\n"
    "v_cvt_pk_bf16_f32 v88, v88, v89\n"
    "v_mfma_f32_16x16x32_bf16 v[32:35], a[128:131], v[216:219], 0\n"
    "v_exp_f32_e32 v195, v195\n"
    "v_mfma_f32_16x16x32_bf16 v[32:35], a[132:135], v[220:223], v[32:35]\n"
    "s_waitcnt lgkmcnt(0)\n"
    "v_cmp_le_u32_e32 vcc, s76, v253\n"
    "v_cvt_pk_bf16_f32 v209, v194, v195\n"
    "v_mfma_f32_16x16x32_bf16 v[36:39], a[136:139], v[216:219], 0\n"
    "v_exp_f32_e32 v196, v196\n"
    "v_mfma_f32_16x16x32_bf16 v[36:39], a[140:143], v[220:223], v[36:39]\n"
    "v_cvt_pk_bf16_f32 v90, v90, v91\n"
    "v_cvt_pk_bf16_f32 v92, v92, v93\n"
    "v_mfma_f32_16x16x32_bf16 v[236:239], a[212:215], a[48:51], 0\n"
    "s_cbranch_vccz LSPIN3_%=\n"
    "LRET3_%=:\n"
    "v_exp_f32_e32 v197, v197\n"
    "v_mfma_f32_16x16x32_bf16 v[236:239], a[216:219], a[52:55], v[236:239]\n"
    "s_addk_i32 s76, 256\n"
    "v_cvt_pk_bf16_f32 v210, v196, v197\n"
    "v_cvt_pk_bf16_f32 v94, v94, v95\n"
    "v_mfma_f32_16x16x32_bf16 v[236:239], a[220:223], a[56:59], v[236:239]\n"
    "s_add_u32 m0, s62, 0\n"
    "v_exp_f32_e32 v198, v198\n"
    "v_mfma_f32_16x16x32_bf16 v[236:239], a[224:227], a[60:63], v[236:239]\n"
    "buffer_load_dwordx4 v242, s[36:39], 0 offen offset:0 nt lds\n"
    "v_cvt_pk_bf16_f32 v96, v96, v97\n"
    "v_mfma_f32_16x16x32_bf16 v[200:203], a[96:99], a[32:35], v[232:235]\n"
    "v_exp_f32_e32 v199, v199\n"
    "v_mfma_f32_16x16x32_bf16 v[200:203], a[100:103], a[36:39], v[200:203]\n"
    "v_cvt_pk_bf16_f32 v211, v198, v199\n"
    "v_cvt_pk_bf16_f32 v98, v98, v99\n"
    "v_mfma_f32_16x16x32_bf16 v[200:203], a[104:107], a[40:43], v[200:203]\n"
    "v_sub_f32_e32 v239, s89, v236\n"
    "v_sub_f32_e32 v238, s89, v236\n"
    "v_mfma_f32_16x16x32_bf16 v[200:203], a[108:111], a[44:47], v[200:203]\n"
    "v_sub_f32_e32 v237, s89, v236\n"
    "v_sub_f32_e32 v236, s89, v236\n"
    "v_mfma_f32_16x16x32_bf16 v[40:43], a[144:147], v[216:219], 0\n"
    "v_cvt_pk_bf16_f32 v100, v100, v101\n"
    "v_cvt_pk_bf16_f32 v102, v102, v103\n"
    "v_mfma_f32_16x16x32_bf16 v[40:43], a[148:151], v[220:223], v[40:43]\n"
    "v_cvt_pk_bf16_f32 v104, v104, v105\n"
    "v_cvt_pk_bf16_f32 v106, v106, v107\n"
    "v_mfma_f32_16x16x32_bf16 v[44:47], a[152:155], v[216:219], 0\n"
    "v_exp_f32_e32 v200, v200\n"
    "v_mfma_f32_16x16x32_bf16 v[44:47], a[156:159], v[220:223], v[44:47]\n"
    "v_cvt_pk_bf16_f32 v108, v108, v109\n"
    "v_cvt_pk_bf16_f32 v110, v110, v111\n"
    "v_smfmac_f32_16x16x64_bf16 a[196:199], a[208:211], v[216:223], v254\n"
    "v_cvt_pk_bf16_f32 v112, v112, v113\n"
    "v_mfma_f32_16x16x32_bf16 v[204:207], a[112:115], a[32:35], v[232:235]\n"
    "s_sub_u32 m0, s95, 128\n"
    "v_exp_f32_e32 v201, v201\n"
    "v_mfma_f32_16x16x32_bf16 v[204:207], a[116:119], a[36:39], v[204:207]\n"
    "buffer_load_dwordx4 v243, s[44:47], 0 offen offset:128 lds\n"
    "v_cvt_pk_bf16_f32 v212, v200, v201\n"
    "v_mfma_f32_16x16x32_bf16 v[204:207], a[120:123], a[40:43], v[204:207]\n"
    "v_exp_f32_e32 v202, v202\n"
    "v_mfma_f32_16x16x32_bf16 v[204:207], a[124:127], a[44:47], v[204:207]\n"
    "v_cvt_pk_bf16_f32 v114, v114, v115\n"
    "v_cvt_pk_bf16_f32 v116, v116, v117\n"
    "v_mfma_f32_16x16x32_bf16 v[48:51], a[160:163], v[216:219], 0\n"
    "v_exp_f32_e32 v203, v203\n"
    "v_mfma_f32_16x16x32_bf16 v[48:51], a[164:167], v[220:223], v[48:51]\n"
    "v_cvt_pk_bf16_f32 v213, v202, v203\n"
    "v_cvt_pk_bf16_f32 v118, v118, v119\n"
    "v_mfma_f32_16x16x32_bf16 v[52:55], a[168:171], v[216:219], 0\n"
    "v_exp_f32_e32 v204, v204\n"
    "v_mfma_f32_16x16x32_bf16 v[52:55], a[172:175], v[220:223], v[52:55]\n"
    "v_cvt_pk_bf16_f32 v120, v120, v121\n"
    "v_cvt_pk_bf16_f32 v122, v122, v123\n"
    "v_mfma_f32_16x16x32_bf16 v[192:195], a[64:67], a[48:51], v[236:239]\n"
    "v_exp_f32_e32 v205, v205\n"
    "v_mfma_f32_16x16x32_bf16 v[192:195], a[68:71], a[52:55], v[192:195]\n"
    "ds_read_b128 a[64:67], v240 offset:16896\n"
    "v_cvt_pk_bf16_f32 v214, v204, v205\n"
    "v_mfma_f32_16x16x32_bf16 v[192:195], a[72:75], a[56:59], v[192:195]\n"
    "s_sub_u32 m0, s95, 1088\n"
    "v_exp_f32_e32 v206, v206\n"
    "v_mfma_f32_16x16x32_bf16 v[192:195], a[76:79], a[60:63], v[192:195]\n"
    "buffer_load_dwordx4 v243, s[44:47], 0 offen offset:2176 lds\n"
    "ds_read_b128 a[68:71], v240 offset:16960\n"
    "v_mfma_f32_16x16x32_bf16 v[56:59], a[176:179], v[216:219], 0\n"
    "s_add_u32 s44, s44, 16384\n"
    "v_exp_f32_e32 v207, v207\n"
    "v_mfma_f32_16x16x32_bf16 v[56:59], a[180:183], v[220:223], v[56:59]\n"
    "s_addc_u32 s45, s45, 0\n"
    "ds_read_b128 a[72:75], v240 offset:17024\n"
    "v_cvt_pk_bf16_f32 v215, v206, v207\n"
    "v_mfma_f32_16x16x32_bf16 v[60:63], a[184:187], v[216:219], 0\n"
    "s_mul_i32 s46, s53, s56\n"
    "v_exp_f32_e32 v192, v192\n"
    "v_mfma_f32_16x16x32_bf16 v[60:63], a[188:191], v[220:223], v[60:63]\n"
    "ds_read_b128 a[76:79], v240 offset:17088\n"
    "v_cvt_pk_bf16_f32 v124, v124, v125\n"
    "s_lshl_b64 vcc, s[96:97], 128\n"
    "v_cndmask_b32_dpp v160, v72, v64, vcc quad_perm:[1,0,3,2] row_mask:0xf bank_mask:0xf\n"
    "v_cndmask_b32_dpp v161, v74, v66, vcc quad_perm:[1,0,3,2] row_mask:0xf bank_mask:0xf\n"
    "v_cndmask_b32_dpp v162, v76, v68, vcc quad_perm:[1,0,3,2] row_mask:0xf bank_mask:0xf\n"
    "v_cndmask_b32_dpp v163, v78, v70, vcc quad_perm:[1,0,3,2] row_mask:0xf bank_mask:0xf\n"
    "v_cndmask_b32_dpp v168, v88, v80, vcc quad_perm:[1,0,3,2] row_mask:0xf bank_mask:0xf\n"
    "v_cndmask_b32_dpp v169, v90, v82, vcc quad_perm:[1,0,3,2] row_mask:0xf bank_mask:0xf\n"
    "v_cndmask_b32_dpp v170, v92, v84, vcc quad_perm:[1,0,3,2] row_mask:0xf bank_mask:0xf\n"
    "v_cndmask_b32_dpp v171, v94, v86, vcc quad_perm:[1,0,3,2] row_mask:0xf bank_mask:0xf\n"
    "s_lshl_b64 vcc, s[96:97], 129\n"
    "v_cndmask_b32_dpp v164, v64, v72, vcc quad_perm:[1,0,3,2] row_mask:0xf bank_mask:0xf\n"
    "v_cndmask_b32_dpp v165, v66, v74, vcc quad_perm:[1,0,3,2] row_mask:0xf bank_mask:0xf\n"
    "v_cndmask_b32_dpp v166, v68, v76, vcc quad_perm:[1,0,3,2] row_mask:0xf bank_mask:0xf\n"
    "v_cndmask_b32_dpp v167, v70, v78, vcc quad_perm:[1,0,3,2] row_mask:0xf bank_mask:0xf\n"
    "v_cndmask_b32_dpp v172, v80, v88, vcc quad_perm:[1,0,3,2] row_mask:0xf bank_mask:0xf\n"
    "v_cndmask_b32_dpp v173, v82, v90, vcc quad_perm:[1,0,3,2] row_mask:0xf bank_mask:0xf\n"
    "v_cndmask_b32_dpp v174, v84, v92, vcc quad_perm:[1,0,3,2] row_mask:0xf bank_mask:0xf\n"
    "v_cndmask_b32_dpp v175, v86, v94, vcc quad_perm:[1,0,3,2] row_mask:0xf bank_mask:0xf\n"
    "v_mfma_f32_16x16x32_bf16 v[196:199], a[80:83], a[48:51], v[236:239]\n"
    "v_exp_f32_e32 v193, v193\n"
    "v_mfma_f32_16x16x32_bf16 v[196:199], a[84:87], a[52:55], v[196:199]\n"
    "ds_read_b128 a[80:83], v240 offset:17152\n"
    "v_cvt_pk_bf16_f32 v216, v192, v193\n"
    "v_mfma_f32_16x16x32_bf16 v[196:199], a[88:91], a[56:59], v[196:199]\n"
    "v_exp_f32_e32 v194, v194\n"
    "v_mfma_f32_16x16x32_bf16 v[196:199], a[92:95], a[60:63], v[196:199]\n"
    "ds_read_b128 a[84:87], v240 offset:17216\n"
    "ds_read_b128 a[88:91], v240 offset:17280\n"
    "v_mfma_f32_16x16x32_bf16 v[64:67], a[128:131], v[208:211], 0\n"
    "s_add_u32 m0, s62, 32\n"
    "v_exp_f32_e32 v195, v195\n"
    "v_mfma_f32_16x16x32_bf16 v[64:67], a[132:135], v[212:215], v[64:67]\n"
    "buffer_load_dwordx4 v242, s[36:39], 0 offen offset:1024 nt lds\n"
    "ds_read_b128 a[92:95], v240 offset:17344\n"
    "v_mfma_f32_16x16x32_bf16 v[68:71], a[136:139], v[208:211], 0\n"
    "v_exp_f32_e32 v196, v196\n"
    "v_mfma_f32_16x16x32_bf16 v[68:71], a[140:143], v[212:215], v[68:71]\n"
    "v_cvt_pk_bf16_f32 v217, v194, v195\n"
    "v_cvt_pk_bf16_f32 v126, v126, v127\n"
    "v_mfma_f32_16x16x32_bf16 v[200:203], a[96:99], a[48:51], v[236:239]\n"
    "v_exp_f32_e32 v197, v197\n"
    "v_mfma_f32_16x16x32_bf16 v[200:203], a[100:103], a[52:55], v[200:203]\n"
    "s_lshl_b64 vcc, s[96:97], 128\n"
    "ds_read_b128 a[96:99], v240 offset:17408\n"
    "v_cvt_pk_bf16_f32 v218, v196, v197\n"
    "v_mfma_f32_16x16x32_bf16 v[200:203], a[104:107], a[56:59], v[200:203]\n"
    "v_exp_f32_e32 v198, v198\n"
    "v_mfma_f32_16x16x32_bf16 v[200:203], a[108:111], a[60:63], v[200:203]\n"
    "ds_read_b128 a[100:103], v240 offset:17472\n"
    "ds_read_b128 a[104:107], v240 offset:17536\n"
    "v_mfma_f32_16x16x32_bf16 v[72:75], a[144:147], v[208:211], 0\n"
    "v_exp_f32_e32 v199, v199\n"
    "v_mfma_f32_16x16x32_bf16 v[72:75], a[148:151], v[212:215], v[72:75]\n"
    "ds_read_b128 a[108:111], v240 offset:17600\n"
    "v_cvt_pk_bf16_f32 v219, v198, v199\n"
    "v_mfma_f32_16x16x32_bf16 v[76:79], a[152:155], v[208:211], 0\n"
    "s_add_u32 m0, s62, 64\n"
    "v_exp_f32_e32 v200, v200\n"
    "v_mfma_f32_16x16x32_bf16 v[76:79], a[156:159], v[212:215], v[76:79]\n"
    "buffer_load_dwordx4 v242, s[36:39], 0 offen offset:2048 nt lds\n"
    "v_cndmask_b32_dpp v176, v104, v96, vcc quad_perm:[1,0,3,2] row_mask:0xf bank_mask:0xf\n"
    "v_smfmac_f32_16x16x64_bf16 a[200:203], a[208:211], v[208:215], v254\n"
    "v_cndmask_b32_dpp v177, v106, v98, vcc quad_perm:[1,0,3,2] row_mask:0xf bank_mask:0xf\n"
    "v_mfma_f32_16x16x32_bf16 v[204:207], a[112:115], a[48:51], v[236:239]\n"
    "v_exp_f32_e32 v201, v201\n"
    "v_mfma_f32_16x16x32_bf16 v[204:207], a[116:119], a[52:55], v[204:207]\n"
    "ds_read_b128 a[112:115], v240 offset:17664\n"
    "v_cvt_pk_bf16_f32 v220, v200, v201\n"
    "v_mfma_f32_16x16x32_bf16 v[204:207], a[120:123], a[56:59], v[204:207]\n"
    "v_exp_f32_e32 v202, v202\n"
    "v_mfma_f32_16x16x32_bf16 v[204:207], a[124:127], a[60:63], v[204:207]\n"
    "ds_read_b128 a[116:119], v240 offset:17728\n"
    "ds_read_b128 a[120:123], v240 offset:17792\n"
    "v_mfma_f32_16x16x32_bf16 v[80:83], a[160:163], v[208:211], 0\n"
    "v_exp_f32_e32 v203, v203\n"
    "v_mfma_f32_16x16x32_bf16 v[80:83], a[164:167], v[212:215], v[80:83]\n"
    "ds_read_b128 a[124:127], v240 offset:17856\n"
    "v_cvt_pk_bf16_f32 v221, v202, v203\n"
    "v_mfma_f32_16x16x32_bf16 v[84:87], a[168:171], v[208:211], 0\n"
    "v_exp_f32_e32 v204, v204\n"
    "v_mfma_f32_16x16x32_bf16 v[84:87], a[172:175], v[212:215], v[84:87]\n"
    "s_waitcnt lgkmcnt(12)\n"
    "v_cndmask_b32_dpp v178, v108, v100, vcc quad_perm:[1,0,3,2] row_mask:0xf bank_mask:0xf\n"
    "v_cndmask_b32_dpp v179, v110, v102, vcc quad_perm:[1,0,3,2] row_mask:0xf bank_mask:0xf\n"
    "v_mfma_f32_16x16x32_bf16 v[192:195], a[64:67], a[0:3], v[224:227]\n"
    "v_exp_f32_e32 v205, v205\n"
    "v_mfma_f32_16x16x32_bf16 v[192:195], a[68:71], a[4:7], v[192:195]\n"
    "buffer_store_dwordx4 v[140:143], v250, s[48:51], 0 offen offset:384 sc0 sc1 nt\n"
    "v_cvt_pk_bf16_f32 v222, v204, v205\n"
    "v_cndmask_b32_dpp v184, v120, v112, vcc quad_perm:[1,0,3,2] row_mask:0xf bank_mask:0xf\n"
    "v_cndmask_b32_dpp v185, v122, v114, vcc quad_perm:[1,0,3,2] row_mask:0xf bank_mask:0xf\n"
    "v_cndmask_b32_dpp v186, v124, v116, vcc quad_perm:[1,0,3,2] row_mask:0xf bank_mask:0xf\n"
    "v_mfma_f32_16x16x32_bf16 v[192:195], a[72:75], a[8:11], v[192:195]\n"
    "s_add_u32 s48, s48, 16384\n"
    "v_exp_f32_e32 v206, v206\n"
    "v_mfma_f32_16x16x32_bf16 v[192:195], a[76:79], a[12:15], v[192:195]\n"
    "s_addc_u32 s49, s49, 0\n"
    "v_cndmask_b32_dpp v187, v126, v118, vcc quad_perm:[1,0,3,2] row_mask:0xf bank_mask:0xf\n"
    "v_mfma_f32_16x16x32_bf16 v[88:91], a[176:179], v[208:211], 0\n"
    "s_lshl_b64 vcc, s[96:97], 129\n"
    "v_exp_f32_e32 v207, v207\n"
    "v_mfma_f32_16x16x32_bf16 v[88:91], a[180:183], v[212:215], v[88:91]\n"
    "s_mul_i32 s50, s53, s57\n"
    "v_cvt_pk_bf16_f32 v223, v206, v207\n"
    "v_cndmask_b32_dpp v180, v96, v104, vcc quad_perm:[1,0,3,2] row_mask:0xf bank_mask:0xf\n"
    "v_mfma_f32_16x16x32_bf16 v[92:95], a[184:187], v[208:211], 0\n"
    "v_exp_f32_e32 v192, v192\n"
    "v_mfma_f32_16x16x32_bf16 v[92:95], a[188:191], v[212:215], v[92:95]\n"
    "s_waitcnt lgkmcnt(8)\n"
    "v_cndmask_b32_dpp v181, v98, v106, vcc quad_perm:[1,0,3,2] row_mask:0xf bank_mask:0xf\n"
    "v_cndmask_b32_dpp v182, v100, v108, vcc quad_perm:[1,0,3,2] row_mask:0xf bank_mask:0xf\n"
    "v_mfma_f32_16x16x32_bf16 v[196:199], a[80:83], a[0:3], v[224:227]\n"
    "v_exp_f32_e32 v193, v193\n"
    "v_mfma_f32_16x16x32_bf16 v[196:199], a[84:87], a[4:7], v[196:199]\n"
    "v_cvt_pk_bf16_f32 v208, v192, v193\n"
    "v_cndmask_b32_dpp v183, v102, v110, vcc quad_perm:[1,0,3,2] row_mask:0xf bank_mask:0xf\n"
    "v_mfma_f32_16x16x32_bf16 v[196:199], a[88:91], a[8:11], v[196:199]\n"
    "s_add_u32 m0, s62, 96\n"
    "v_exp_f32_e32 v194, v194\n"
    "v_mfma_f32_16x16x32_bf16 v[196:199], a[92:95], a[12:15], v[196:199]\n"
    "buffer_load_dwordx4 v242, s[36:39], 0 offen offset:3072 nt lds\n"
    "v_cndmask_b32_dpp v188, v112, v120, vcc quad_perm:[1,0,3,2] row_mask:0xf bank_mask:0xf\n"
    "v_cndmask_b32_dpp v189, v114, v122, vcc quad_perm:[1,0,3,2] row_mask:0xf bank_mask:0xf\n"
    "v_cndmask_b32_dpp v190, v116, v124, vcc quad_perm:[1,0,3,2] row_mask:0xf bank_mask:0xf\n"
    "v_cndmask_b32_dpp v191, v118, v126, vcc quad_perm:[1,0,3,2] row_mask:0xf bank_mask:0xf\n"
    "v_mfma_f32_16x16x32_bf16 v[96:99], a[128:131], v[216:219], 0\n"
    "s_addk_i32 s59, 16384\n"
    "v_exp_f32_e32 v195, v195\n"
    "v_mfma_f32_16x16x32_bf16 v[96:99], a[132:135], v[220:223], v[96:99]\n"
    "s_xor_b32 s71, s59, s63\n"
    "ds_read_b64_tr_b16 a[128:129], v241 offset:16896\n"
    "v_cvt_pk_bf16_f32 v209, v194, v195\n"
    "v_mfma_f32_16x16x32_bf16 v[100:103], a[136:139], v[216:219], 0\n"
    "s_add_u32 s36, s84, s71\n"
    "v_exp_f32_e32 v196, v196\n"
    "v_mfma_f32_16x16x32_bf16 v[100:103], a[140:143], v[220:223], v[100:103]\n"
    "s_waitcnt lgkmcnt(5)\n"
    "ds_read_b64_tr_b16 a[130:131], v241 offset:17152\n"
    "ds_read_b64_tr_b16 a[132:133], v241 offset:17408\n"
    "v_mfma_f32_16x16x32_bf16 v[200:203], a[96:99], a[0:3], v[224:227]\n"
    "s_addc_u32 s37, s85, 0\n"
    "v_exp_f32_e32 v197, v197\n"
    "v_mfma_f32_16x16x32_bf16 v[200:203], a[100:103], a[4:7], v[200:203]\n"
    "s_addk_i32 s58, -16384\n"
    "ds_read_b64_tr_b16 a[134:135], v241 offset:17664\n"
    "ds_read_b64_tr_b16 a[136:137], v241 offset:16904\n"
    "v_mfma_f32_16x16x32_bf16 v[200:203], a[104:107], a[8:11], v[200:203]\n"
    "s_max_i32 s38, s58, 0\n"
    "v_exp_f32_e32 v198, v198\n"
    "v_mfma_f32_16x16x32_bf16 v[200:203], a[108:111], a[12:15], v[200:203]\n"
    "ds_read_b64_tr_b16 a[138:139], v241 offset:17160\n"
    "ds_read_b64_tr_b16 a[140:141], v241 offset:17416\n"
    "v_mfma_f32_16x16x32_bf16 v[104:107], a[144:147], v[216:219], 0\n"
    "s_add_u32 m0, s62, 67584\n"
    "v_exp_f32_e32 v199, v199\n"
    "v_mfma_f32_16x16x32_bf16 v[104:107], a[148:151], v[220:223], v[104:107]\n"
    "buffer_load_dwordx4 v242, s[40:43], 0 offen offset:0 nt lds\n"
    "ds_read_b64_tr_b16 a[142:143], v241 offset:17672\n"
    "v_mfma_f32_16x16x32_bf16 v[108:111], a[152:155], v[216:219], 0\n"
    "v_exp_f32_e32 v200, v200\n"
    "v_mfma_f32_16x16x32_bf16 v[108:111], a[156:159], v[220:223], v[108:111]\n"
    "v_cvt_pk_bf16_f32 v210, v196, v197\n"
    "v_cvt_pk_bf16_f32 v211, v198, v199\n"
    "v_smfmac_f32_16x16x64_bf16 a[204:207], a[208:211], v[216:223], v254\n"
    "s_waitcnt lgkmcnt(8)\n"
    "ds_read_b64_tr_b16 a[144:145], v241 offset:16912\n"
    "v_mfma_f32_16x16x32_bf16 v[204:207], a[112:115], a[0:3], v[224:227]\n"
    "v_exp_f32_e32 v201, v201\n"
    "v_mfma_f32_16x16x32_bf16 v[204:207], a[116:119], a[4:7], v[204:207]\n"
    "ds_read_b64_tr_b16 a[146:147], v241 offset:17168\n"
    "ds_read_b64_tr_b16 a[148:149], v241 offset:17424\n"
    "v_mfma_f32_16x16x32_bf16 v[204:207], a[120:123], a[8:11], v[204:207]\n"
    "v_exp_f32_e32 v202, v202\n"
    "v_mfma_f32_16x16x32_bf16 v[204:207], a[124:127], a[12:15], v[204:207]\n"
    "ds_read_b64_tr_b16 a[150:151], v241 offset:17680\n"
    "v_cvt_pk_bf16_f32 v212, v200, v201\n"
    "v_mfma_f32_16x16x32_bf16 v[112:115], a[160:163], v[216:219], 0\n"
    "v_exp_f32_e32 v203, v203\n"
    "v_mfma_f32_16x16x32_bf16 v[112:115], a[164:167], v[220:223], v[112:115]\n"
    "ds_read_b64_tr_b16 a[152:153], v241 offset:16920\n"
    "v_cvt_pk_bf16_f32 v213, v202, v203\n"
    "v_mfma_f32_16x16x32_bf16 v[116:119], a[168:171], v[216:219], 0\n"
    "s_add_u32 m0, s62, 67592\n"
    "v_exp_f32_e32 v204, v204\n"
    "v_mfma_f32_16x16x32_bf16 v[116:119], a[172:175], v[220:223], v[116:119]\n"
    "buffer_load_dwordx4 v242, s[40:43], 0 offen offset:1024 nt lds\n"
    "ds_read_b64_tr_b16 a[154:155], v241 offset:17176\n"
    "v_mfma_f32_16x16x32_bf16 v[192:195], a[64:67], a[16:19], v[228:231]\n"
    "v_exp_f32_e32 v205, v205\n"
    "v_mfma_f32_16x16x32_bf16 v[192:195], a[68:71], a[20:23], v[192:195]\n"
    "ds_read_b64_tr_b16 a[156:157], v241 offset:17432\n"
    "v_cvt_pk_bf16_f32 v214, v204, v205\n"
    "v_mfma_f32_16x16x32_bf16 v[192:195], a[72:75], a[24:27], v[192:195]\n"
    "v_exp_f32_e32 v206, v206\n"
    "v_mfma_f32_16x16x32_bf16 v[192:195], a[76:79], a[28:31], v[192:195]\n"
    "ds_read_b64_tr_b16 a[158:159], v241 offset:17688\n"
    "ds_read_b64_tr_b16 a[160:161], v241 offset:17024\n"
    "v_mfma_f32_16x16x32_bf16 v[120:123], a[176:179], v[216:219], 0\n"
    "v_exp_f32_e32 v207, v207\n"
    "v_mfma_f32_16x16x32_bf16 v[120:123], a[180:183], v[220:223], v[120:123]\n"
    "ds_read_b64_tr_b16 a[162:163], v241 offset:17280\n"
    "v_cvt_pk_bf16_f32 v215, v206, v207\n"
    "v_mfma_f32_16x16x32_bf16 v[124:127], a[184:187], v[216:219], 0\n"
    "v_exp_f32_e32 v192, v192\n"
    "v_mfma_f32_16x16x32_bf16 v[124:127], a[188:191], v[220:223], v[124:127]\n"
    "ds_read_b64_tr_b16 a[164:165], v241 offset:17536\n"
    "ds_read_b64_tr_b16 a[166:167], v241 offset:17792\n"
    "v_mfma_f32_16x16x32_bf16 v[196:199], a[80:83], a[16:19], v[228:231]\n"
    "s_add_u32 m0, s62, 67600\n"
    "v_exp_f32_e32 v193, v193\n"
    "v_mfma_f32_16x16x32_bf16 v[196:199], a[84:87], a[20:23], v[196:199]\n"
    "buffer_load_dwordx4 v242, s[40:43], 0 offen offset:2048 nt lds\n"
    "ds_read_b64_tr_b16 a[168:169], v241 offset:17032\n"
    "v_mfma_f32_16x16x32_bf16 v[196:199], a[88:91], a[24:27], v[196:199]\n"
    "LW8_%=:\n"
    "s_cbranch_execz LWR_%=\n"
    "v_exp_f32_e32 v194, v194\n"
    "v_mfma_f32_16x16x32_bf16 v[196:199], a[92:95], a[28:31], v[196:199]\n"
    "s_waitcnt lgkmcnt(14)\n"
    "ds_read_b64_tr_b16 a[170:171], v241 offset:17288\n"
    "ds_read_b64_tr_b16 a[172:173], v241 offset:17544\n"
    "v_mfma_f32_16x16x32_bf16 v[0:3], a[128:131], v[208:211], v[0:3]\n"
    "v_exp_f32_e32 v195, v195\n"
    "v_mfma_f32_16x16x32_bf16 v[0:3], a[132:135], v[212:215], v[0:3]\n"
    "s_waitcnt lgkmcnt(14)\n"
    "ds_read_b64_tr_b16 a[174:175], v241 offset:17800\n"
    "ds_read_b64_tr_b16 a[176:177], v241 offset:17040\n"
    "v_mfma_f32_16x16x32_bf16 v[4:7], a[136:139], v[208:211], v[4:7]\n"
    "v_exp_f32_e32 v196, v196\n"
    "v_mfma_f32_16x16x32_bf16 v[4:7], a[140:143], v[212:215], v[4:7]\n"
    "ds_read_b64_tr_b16 a[178:179], v241 offset:17296\n"
    "ds_read_b64_tr_b16 a[180:181], v241 offset:17552\n"
    "v_mfma_f32_16x16x32_bf16 v[200:203], a[96:99], a[16:19], v[228:231]\n"
    "v_exp_f32_e32 v197, v197\n"
    "v_mfma_f32_16x16x32_bf16 v[200:203], a[100:103], a[20:23], v[200:203]\n"
    "s_add_u32 m0, s62, 67608\n"
    "ds_read_b64_tr_b16 a[182:183], v241 offset:17808\n"
    "ds_read_b64_tr_b16 a[184:185], v241 offset:17048\n"
    "v_mfma_f32_16x16x32_bf16 v[200:203], a[104:107], a[24:27], v[200:203]\n"
    "s_waitcnt lgkmcnt(14)\n"
    "v_exp_f32_e32 v198, v198\n"
    "v_mfma_f32_16x16x32_bf16 v[200:203], a[108:111], a[28:31], v[200:203]\n"
    "buffer_load_dwordx4 v242, s[40:43], 0 offen offset:3072 nt lds\n"
    "ds_read_b64_tr_b16 a[186:187], v241 offset:17304\n"
    "v_mfma_f32_16x16x32_bf16 v[8:11], a[144:147], v[208:211], v[8:11]\n"
    "s_add_u32 s40, s60, s71\n"
    "v_exp_f32_e32 v199, v199\n"
    "v_mfma_f32_16x16x32_bf16 v[8:11], a[148:151], v[212:215], v[8:11]\n"
    "s_waitcnt lgkmcnt(14)\n"
    "ds_read_b64_tr_b16 a[188:189], v241 offset:17560\n"
    "ds_read_b64_tr_b16 a[190:191], v241 offset:17816\n"
    "v_mfma_f32_16x16x32_bf16 v[12:15], a[152:155], v[208:211], v[12:15]\n"
    "s_addc_u32 s41, s61, 0\n"
    "v_exp_f32_e32 v200, v200\n"
    "v_mfma_f32_16x16x32_bf16 v[12:15], a[156:159], v[212:215], v[12:15]\n"
    "s_max_i32 s42, s58, 0\n"
    "v_cvt_pk_bf16_f32 v216, v192, v193\n"
    "v_cvt_pk_bf16_f32 v217, v194, v195\n"
    "v_smfmac_f32_16x16x64_bf16 a[192:195], a[208:211], v[208:215], v254\n"
    "s_waitcnt vmcnt(16)\n"
    "ds_read_b128 v[128:131], v255 offset:2112\n"
    "v_mfma_f32_16x16x32_bf16 v[204:207], a[112:115], a[16:19], v[228:231]\n"
    "v_exp_f32_e32 v201, v201\n"
    "v_mfma_f32_16x16x32_bf16 v[204:207], a[116:119], a[20:23], v[204:207]\n"
    "s_waitcnt vmcnt(24)\n"
    "ds_append v253\n"
    "ds_read_b128 v[132:135], v255 offset:2176\n"
    "v_mfma_f32_16x16x32_bf16 v[204:207], a[120:123], a[24:27], v[204:207]\n"
    "v_exp_f32_e32 v202, v202\n"
    "v_mfma_f32_16x16x32_bf16 v[204:207], a[124:127], a[28:31], v[204:207]\n"
    "s_waitcnt lgkmcnt(14)\n"
    "v_cvt_pk_bf16_f32 v218, v196, v197\n"
    "v_cvt_pk_bf16_f32 v219, v198, v199\n"
    "v_mfma_f32_16x16x32_bf16 v[16:19], a[160:163], v[208:211], v[16:19]\n"
    "v_exp_f32_e32 v203, v203\n"
    "v_mfma_f32_16x16x32_bf16 v[16:19], a[164:167], v[212:215], v[16:19]\n"
    "s_waitcnt lgkmcnt(11)\n"
    "v_cvt_pk_bf16_f32 v220, v200, v201\n"
    "v_cvt_pk_bf16_f32 v221, v202, v203\n"
    "v_mfma_f32_16x16x32_bf16 v[20:23], a[168:171], v[208:211], v[20:23]\n"
    "v_exp_f32_e32 v204, v204\n"
    "v_mfma_f32_16x16x32_bf16 v[20:23], a[172:175], v[212:215], v[20:23]\n"
    "s_waitcnt lgkmcnt(0)\n"
    "v_lshlrev_b32_e32 v244, 16, v128\n"
    "v_and_b32_e32 v245, s67, v128\n"
    "v_mfma_f32_16x16x32_bf16 v[192:195], a[64:67], a[32:35], v[232:235]\n"
    "v_exp_f32_e32 v205, v205\n"
    "v_mfma_f32_16x16x32_bf16 v[192:195], a[68:71], a[36:39], v[192:195]\n"
    "v_cvt_pk_bf16_f32 v222, v204, v205\n"
    "v_mul_f32_e32 v244, s64, v244\n"
    "v_mfma_f32_16x16x32_bf16 v[192:195], a[72:75], a[40:43], v[192:195]\n"
    "v_exp_f32_e32 v206, v206\n"
    "v_mfma_f32_16x16x32_bf16 v[192:195], a[76:79], a[44:47], v[192:195]\n"
    "s_waitcnt lgkmcnt(7)\n"
    "v_mul_f32_e32 v245, s64, v245\n"
    "v_cvt_pk_bf16_f32 v128, v244, v245\n"
    "v_mfma_f32_16x16x32_bf16 v[24:27], a[176:179], v[208:211], v[24:27]\n"
    "v_exp_f32_e32 v207, v207\n"
    "v_mfma_f32_16x16x32_bf16 v[24:27], a[180:183], v[212:215], v[24:27]\n"
    "s_waitcnt lgkmcnt(3)\n"
    "v_cvt_pk_bf16_f32 v223, v206, v207\n"
    "v_lshlrev_b32_e32 v244, 16, v129\n"
    "v_mfma_f32_16x16x32_bf16 v[28:31], a[184:187], v[208:211], v[28:31]\n"
    "v_exp_f32_e32 v192, v192\n"
    "v_mfma_f32_16x16x32_bf16 v[28:31], a[188:191], v[212:215], v[28:31]\n"
    "ds_read_b32 v253, v251\n"
    "v_and_b32_e32 v245, s67, v129\n"
    "v_mfma_f32_16x16x32_bf16 v[196:199], a[80:83], a[32:35], v[232:235]\n"
    "v_exp_f32_e32 v193, v193\n"
    "v_mfma_f32_16x16x32_bf16 v[196:199], a[84:87], a[36:39], v[196:199]\n"
    "buffer_store_dwordx4 v[144:147], v250, s[48:51], 0 offen offset:0 sc0 sc1 nt\n"
    "v_cvt_pk_bf16_f32 v208, v192, v193\n"
    "v_mul_f32_e32 v244, s64, v244\n"
    "v_mul_f32_e32 v245, s64, v245\n"
    "v_cvt_pk_bf16_f32 v129, v244, v245\n"
    "v_mfma_f32_16x16x32_bf16 v[196:199], a[88:91], a[40:43], v[196:199]\n"
    "v_exp_f32_e32 v194, v194\n"
    "v_mfma_f32_16x16x32_bf16 v[196:199], a[92:95], a[44:47], v[196:199]\n"
    "v_lshlrev_b32_e32 v244, 16, v130\n"
    "v_and_b32_e32 v245, s67, v130\n"
    "v_mfma_f32_16x16x32_bf16 v[32:35], a[128:131], v[216:219], v[32:35]\n"
    "v_exp_f32_e32 v195, v195\n"
    "v_mfma_f32_16x16x32_bf16 v[32:35], a[132:135], v[220:223], v[32:35]\n"
    "s_waitcnt lgkmcnt(0)\n"
    "v_cmp_le_u32_e32 vcc, s76, v253\n"
    "v_cvt_pk_bf16_f32 v209, v194, v195\n"
    "v_mfma_f32_16x16x32_bf16 v[36:39], a[136:139], v[216:219], v[36:39]\n"
    "v_exp_f32_e32 v196, v196\n"
    "v_mfma_f32_16x16x32_bf16 v[36:39], a[140:143], v[220:223], v[36:39]\n"
    "v_mul_f32_e32 v244, s64, v244\n"
    "v_mul_f32_e32 v245, s64, v245\n"
    "v_mfma_f32_16x16x32_bf16 v[200:203], a[96:99], a[32:35], v[232:235]\n"
    "s_cbranch_vccz LSPIN4_%=\n"
    "LRET4_%=:\n"
    "v_exp_f32_e32 v197, v197\n"
    "v_mfma_f32_16x16x32_bf16 v[200:203], a[100:103], a[36:39], v[200:203]\n"
    "s_addk_i32 s76, 256\n"
    "v_cvt_pk_bf16_f32 v210, v196, v197\n"
    "v_cvt_pk_bf16_f32 v130, v244, v245\n"
    "v_mfma_f32_16x16x32_bf16 v[200:203], a[104:107], a[40:43], v[200:203]\n"
    "s_add_u32 m0, s62, 16896\n"
    "v_exp_f32_e32 v198, v198\n"
    "v_mfma_f32_16x16x32_bf16 v[200:203], a[108:111], a[44:47], v[200:203]\n"
    "buffer_load_dwordx4 v242, s[36:39], 0 offen offset:0 nt lds\n"
    "v_lshlrev_b32_e32 v244, 16, v131\n"
    "v_mfma_f32_16x16x32_bf16 v[40:43], a[144:147], v[216:219], v[40:43]\n"
    "v_exp_f32_e32 v199, v199\n"
    "v_mfma_f32_16x16x32_bf16 v[40:43], a[148:151], v[220:223], v[40:43]\n"
    "v_cvt_pk_bf16_f32 v211, v198, v199\n"
    "v_and_b32_e32 v245, s67, v131\n"
    "v_mfma_f32_16x16x32_bf16 v[44:47], a[152:155], v[216:219], v[44:47]\n"
    "v_exp_f32_e32 v200, v200\n"
    "v_mfma_f32_16x16x32_bf16 v[44:47], a[156:159], v[220:223], v[44:47]\n"
    "v_mul_f32_e32 v244, s64, v244\n"
    "v_mul_f32_e32 v245, s64, v245\n"
    "v_smfmac_f32_16x16x64_bf16 a[196:199], a[208:211], v[216:223], v254\n"
    "v_cvt_pk_bf16_f32 v131, v244, v245\n"
    "v_mfma_f32_16x16x32_bf16 v[204:207], a[112:115], a[32:35], v[232:235]\n"
    "v_exp_f32_e32 v201, v201\n"
    "v_mfma_f32_16x16x32_bf16 v[204:207], a[116:119], a[36:39], v[204:207]\n"
    "v_cvt_pk_bf16_f32 v212, v200, v201\n"
    "v_lshlrev_b32_e32 v244, 16, v132\n"
    "v_mfma_f32_16x16x32_bf16 v[204:207], a[120:123], a[40:43], v[204:207]\n"
    "v_exp_f32_e32 v202, v202\n"
    "v_mfma_f32_16x16x32_bf16 v[204:207], a[124:127], a[44:47], v[204:207]\n"
    "v_and_b32_e32 v245, s67, v132\n"
    "v_mul_f32_e32 v244, s64, v244\n"
    "v_mfma_f32_16x16x32_bf16 v[48:51], a[160:163], v[216:219], v[48:51]\n"
    "s_add_u32 m0, s95, 2112\n"
    "v_exp_f32_e32 v203, v203\n"
    "v_mfma_f32_16x16x32_bf16 v[48:51], a[164:167], v[220:223], v[48:51]\n"
    "buffer_load_dwordx4 v243, s[44:47], 0 offen offset:0 lds\n"
    "v_cvt_pk_bf16_f32 v213, v202, v203\n"
    "v_mfma_f32_16x16x32_bf16 v[52:55], a[168:171], v[216:219], v[52:55]\n"
    "v_exp_f32_e32 v204, v204\n"
    "v_mfma_f32_16x16x32_bf16 v[52:55], a[172:175], v[220:223], v[52:55]\n"
    "v_mul_f32_e32 v245, s64, v245\n"
    "v_cvt_pk_bf16_f32 v132, v244, v245\n"
    "v_mfma_f32_16x16x32_bf16 v[192:195], a[64:67], a[48:51], v[236:239]\n"
    "v_exp_f32_e32 v205, v205\n"
    "v_mfma_f32_16x16x32_bf16 v[192:195], a[68:71], a[52:55], v[192:195]\n"
    "ds_read_b128 a[64:67], v240 offset:33792\n"
    "v_cvt_pk_bf16_f32 v214, v204, v205\n"
    "v_mfma_f32_16x16x32_bf16 v[192:195], a[72:75], a[56:59], v[192:195]\n"
    "v_exp_f32_e32 v206, v206\n"
    "v_mfma_f32_16x16x32_bf16 v[192:195], a[76:79], a[60:63], v[192:195]\n"
    "ds_read_b128 a[68:71], v240 offset:33856\n"
    "ds_read_b128 a[72:75], v240 offset:33920\n"
    "v_mfma_f32_16x16x32_bf16 v[56:59], a[176:179], v[216:219], v[56:59]\n"
    "v_exp_f32_e32 v207, v207\n"
    "v_mfma_f32_16x16x32_bf16 v[56:59], a[180:183], v[220:223], v[56:59]\n"
    "ds_read_b128 a[76:79], v240 offset:33984\n"
    "v_cvt_pk_bf16_f32 v215, v206, v207\n"
    "v_mfma_f32_16x16x32_bf16 v[60:63], a[184:187], v[216:219], v[60:63]\n"
    "s_add_u32 m0, s95, 1152\n"
    "v_exp_f32_e32 v192, v192\n"
    "v_mfma_f32_16x16x32_bf16 v[60:63], a[188:191], v[220:223], v[60:63]\n"
    "buffer_load_dwordx4 v243, s[44:47], 0 offen offset:2048 lds\n"
    "v_lshlrev_b32_e32 v244, 16, v133\n"
    "v_mfma_f32_16x16x32_bf16 v[196:199], a[80:83], a[48:51], v[236:239]\n"
    "v_exp_f32_e32 v193, v193\n"
    "v_mfma_f32_16x16x32_bf16 v[196:199], a[84:87], a[52:55], v[196:199]\n"
    "ds_read_b128 a[80:83], v240 offset:34048\n"
    "v_cvt_pk_bf16_f32 v216, v192, v193\n"
    "v_mfma_f32_16x16x32_bf16 v[196:199], a[88:91], a[56:59], v[196:199]\n"
    "v_exp_f32_e32 v194, v194\n"
    "v_mfma_f32_16x16x32_bf16 v[196:199], a[92:95], a[60:63], v[196:199]\n"
    "ds_read_b128 a[84:87], v240 offset:34112\n"
    "ds_read_b128 a[88:91], v240 offset:34176\n"
    "v_mfma_f32_16x16x32_bf16 v[64:67], a[128:131], v[208:211], v[64:67]\n"
    "v_exp_f32_e32 v195, v195\n"
    "v_mfma_f32_16x16x32_bf16 v[64:67], a[132:135], v[212:215], v[64:67]\n"
    "ds_read_b128 a[92:95], v240 offset:34240\n"
    "v_cvt_pk_bf16_f32 v217, v194, v195\n"
    "v_mfma_f32_16x16x32_bf16 v[68:71], a[136:139], v[208:211], v[68:71]\n"
    "v_exp_f32_e32 v196, v196\n"
    "v_mfma_f32_16x16x32_bf16 v[68:71], a[140:143], v[212:215], v[68:71]\n"
    "v_and_b32_e32 v245, s67, v133\n"
    "v_mul_f32_e32 v244, s64, v244\n"
    "v_mfma_f32_16x16x32_bf16 v[200:203], a[96:99], a[48:51], v[236:239]\n"
    "s_add_u32 m0, s62, 16928\n"
    "v_exp_f32_e32 v197, v197\n"
    "v_mfma_f32_16x16x32_bf16 v[200:203], a[100:103], a[52:55], v[200:203]\n"
    "buffer_load_dwordx4 v242, s[36:39], 0 offen offset:1024 nt lds\n"
    "ds_read_b128 a[96:99], v240 offset:34304\n"
    "v_mfma_f32_16x16x32_bf16 v[200:203], a[104:107], a[56:59], v[200:203]\n"
    "v_exp_f32_e32 v198, v198\n"
    "v_mfma_f32_16x16x32_bf16 v[200:203], a[108:111], a[60:63], v[200:203]\n"
    "ds_read_b128 a[100:103], v240 offset:34368\n"
    "v_cvt_pk_bf16_f32 v218, v196, v197\n"
    "v_mfma_f32_16x16x32_bf16 v[72:75], a[144:147], v[208:211], v[72:75]\n"
    "v_exp_f32_e32 v199, v199\n"
    "v_mfma_f32_16x16x32_bf16 v[72:75], a[148:151], v[212:215], v[72:75]\n"
    "ds_read_b128 a[104:107], v240 offset:34432\n"
    "ds_read_b128 a[108:111], v240 offset:34496\n"
    "v_mfma_f32_16x16x32_bf16 v[76:79], a[152:155], v[208:211], v[76:79]\n"
    "v_exp_f32_e32 v200, v200\n"
    "v_mfma_f32_16x16x32_bf16 v[76:79], a[156:159], v[212:215], v[76:79]\n"
    "v_cvt_pk_bf16_f32 v219, v198, v199\n"
    "v_mul_f32_e32 v245, s64, v245\n"
    "v_smfmac_f32_16x16x64_bf16 a[200:203], a[208:211], v[208:215], v254\n"
    "v_cvt_pk_bf16_f32 v133, v244, v245\n"
    "v_mfma_f32_16x16x32_bf16 v[204:207], a[112:115], a[48:51], v[236:239]\n"
    "v_exp_f32_e32 v201, v201\n"
    "v_mfma_f32_16x16x32_bf16 v[204:207], a[116:119], a[52:55], v[204:207]\n"
    "ds_read_b128 a[112:115], v240 offset:34560\n"
    "v_cvt_pk_bf16_f32 v220, v200, v201\n"
    "v_mfma_f32_16x16x32_bf16 v[204:207], a[120:123], a[56:59], v[204:207]\n"
    "s_add_u32 m0, s62, 16960\n"
    "v_exp_f32_e32 v202, v202\n"
    "v_mfma_f32_16x16x32_bf16 v[204:207], a[124:127], a[60:63], v[204:207]\n"
    "buffer_load_dwordx4 v242, s[36:39], 0 offen offset:2048 nt lds\n"
    "ds_read_b128 a[116:119], v240 offset:34624\n"
    "v_mfma_f32_16x16x32_bf16 v[80:83], a[160:163], v[208:211], v[80:83]\n"
    "v_exp_f32_e32 v203, v203\n"
    "v_mfma_f32_16x16x32_bf16 v[80:83], a[164:167], v[212:215], v[80:83]\n"
    "ds_read_b128 a[120:123], v240 offset:34688\n"
    "v_cvt_pk_bf16_f32 v221, v202, v203\n"
    "v_mfma_f32_16x16x32_bf16 v[84:87], a[168:171], v[208:211], v[84:87]\n"
    "v_exp_f32_e32 v204, v204\n"
    "v_mfma_f32_16x16x32_bf16 v[84:87], a[172:175], v[212:215], v[84:87]\n"
    "s_waitcnt lgkmcnt(11)\n"
    "ds_read_b128 a[124:127], v240 offset:34752\n"
    "v_lshlrev_b32_e32 v244, 16, v134\n"
    "v_mfma_f32_16x16x32_bf16 v[192:195], a[64:67], a[0:3], v[224:227]\n"
    "v_exp_f32_e32 v205, v205\n"
    "v_mfma_f32_16x16x32_bf16 v[192:195], a[68:71], a[4:7], v[192:195]\n"
    "v_cvt_pk_bf16_f32 v222, v204, v205\n"
    "v_and_b32_e32 v245, s67, v134\n"
    "v_mfma_f32_16x16x32_bf16 v[192:195], a[72:75], a[8:11], v[192:195]\n"
    "v_exp_f32_e32 v206, v206\n"
    "v_mfma_f32_16x16x32_bf16 v[192:195], a[76:79], a[12:15], v[192:195]\n"
    "v_mul_f32_e32 v244, s64, v244\n"
    "v_mul_f32_e32 v245, s64, v245\n"
    "v_mfma_f32_16x16x32_bf16 v[88:91], a[176:179], v[208:211], v[88:91]\n"
    "v_exp_f32_e32 v207, v207\n"
    "v_mfma_f32_16x16x32_bf16 v[88:91], a[180:183], v[212:215], v[88:91]\n"
    "v_cvt_pk_bf16_f32 v223, v206, v207\n"
    "v_cvt_pk_bf16_f32 v134, v244, v245\n"
    "v_mfma_f32_16x16x32_bf16 v[92:95], a[184:187], v[208:211], v[92:95]\n"
    "s_waitcnt lgkmcnt(8)\n"
    "v_exp_f32_e64 v192, v192\n"
    "v_mfma_f32_16x16x32_bf16 v[92:95], a[188:191], v[212:215], v[92:95]\n"
    "buffer_store_dwordx4 v[148:151], v250, s[48:51], 0 offen offset:256 sc0 sc1 nt\n"
    "v_lshlrev_b32_e32 v244, 16, v135\n"
    "v_and_b32_e32 v245, s67, v135\n"
    "v_mul_f32_e32 v244, s64, v244\n"
    "v_mul_f32_e32 v245, s64, v245\n"
    "v_mfma_f32_16x16x32_bf16 v[196:199], a[80:83], a[0:3], v[224:227]\n"
    "v_exp_f32_e32 v193, v193\n"
    "v_mfma_f32_16x16x32_bf16 v[196:199], a[84:87], a[4:7], v[196:199]\n"
    "v_cvt_pk_bf16_f32 v208, v192, v193\n"
    "v_cvt_pk_bf16_f32 v135, v244, v245\n"
    "v_mfma_f32_16x16x32_bf16 v[196:199], a[88:91], a[8:11], v[196:199]\n"
    "v_exp_f32_e32 v194, v194\n"
    "v_mfma_f32_16x16x32_bf16 v[196:199], a[92:95], a[12:15], v[196:199]\n"
    "v_mfma_f32_16x16x32_bf16 v[96:99], a[128:131], v[216:219], v[96:99]\n"
    "v_exp_f32_e32 v195, v195\n"
    "v_mfma_f32_16x16x32_bf16 v[96:99], a[132:135], v[220:223], v[96:99]\n"
    "ds_read_b64_tr_b16 a[128:129], v241 offset:33792\n"
    "v_cvt_pk_bf16_f32 v209, v194, v195\n"
    "v_mfma_f32_16x16x32_bf16 v[100:103], a[136:139], v[216:219], v[100:103]\n"
    "v_exp_f32_e32 v196, v196\n"
    "v_mfma_f32_16x16x32_bf16 v[100:103], a[140:143], v[220:223], v[100:103]\n"
    "s_waitcnt lgkmcnt(5)\n"
    "ds_read_b64_tr_b16 a[130:131], v241 offset:34048\n"
    "ds_read_b64_tr_b16 a[132:133], v241 offset:34304\n"
    "v_mfma_f32_16x16x32_bf16 v[200:203], a[96:99], a[0:3], v[224:227]\n"
    "s_add_u32 m0, s62, 16992\n"
    "v_exp_f32_e32 v197, v197\n"
    "v_mfma_f32_16x16x32_bf16 v[200:203], a[100:103], a[4:7], v[200:203]\n"
    "buffer_load_dwordx4 v242, s[36:39], 0 offen offset:3072 nt lds\n"
    "ds_read_b64_tr_b16 a[134:135], v241 offset:34560\n"
    "v_mfma_f32_16x16x32_bf16 v[200:203], a[104:107], a[8:11], v[200:203]\n"
    "s_addk_i32 s59, 16384\n"
    "v_exp_f32_e32 v198, v198\n"
    "v_mfma_f32_16x16x32_bf16 v[200:203], a[108:111], a[12:15], v[200:203]\n"
    "s_xor_b32 s71, s59, s63\n"
    "ds_read_b64_tr_b16 a[136:137], v241 offset:33800\n"
    "ds_read_b64_tr_b16 a[138:139], v241 offset:34056\n"
    "v_mfma_f32_16x16x32_bf16 v[104:107], a[144:147], v[216:219], v[104:107]\n"
    "s_add_u32 s36, s84, s71\n"
    "v_exp_f32_e32 v199, v199\n"
    "v_mfma_f32_16x16x32_bf16 v[104:107], a[148:151], v[220:223], v[104:107]\n"
    "s_addc_u32 s37, s85, 0\n"
    "ds_read_b64_tr_b16 a[140:141], v241 offset:34312\n"
    "ds_read_b64_tr_b16 a[142:143], v241 offset:34568\n"
    "v_mfma_f32_16x16x32_bf16 v[108:111], a[152:155], v[216:219], v[108:111]\n"
    "s_addk_i32 s58, -16384\n"
    "v_exp_f32_e32 v200, v200\n"
    "v_mfma_f32_16x16x32_bf16 v[108:111], a[156:159], v[220:223], v[108:111]\n"
    "s_max_i32 s38, s58, 0\n"
    "v_cvt_pk_bf16_f32 v210, v196, v197\n"
    "v_cvt_pk_bf16_f32 v211, v198, v199\n"
    "v_smfmac_f32_16x16x64_bf16 a[204:207], a[208:211], v[216:223], v254\n"
    "s_waitcnt lgkmcnt(8)\n"
    "ds_read_b64_tr_b16 a[144:145], v241 offset:33808\n"
    "v_mfma_f32_16x16x32_bf16 v[204:207], a[112:115], a[0:3], v[224:227]\n"
    "v_exp_f32_e32 v201, v201\n"
    "v_mfma_f32_16x16x32_bf16 v[204:207], a[116:119], a[4:7], v[204:207]\n"
    "ds_read_b64_tr_b16 a[146:147], v241 offset:34064\n"
    "ds_read_b64_tr_b16 a[148:149], v241 offset:34320\n"
    "v_mfma_f32_16x16x32_bf16 v[204:207], a[120:123], a[8:11], v[204:207]\n"
    "s_add_u32 m0, s62, 84480\n"
    "v_exp_f32_e32 v202, v202\n"
    "v_mfma_f32_16x16x32_bf16 v[204:207], a[124:127], a[12:15], v[204:207]\n"
    "buffer_load_dwordx4 v242, s[40:43], 0 offen offset:0 nt lds\n"
    "ds_read_b64_tr_b16 a[150:151], v241 offset:34576\n"
    "v_mfma_f32_16x16x32_bf16 v[112:115], a[160:163], v[216:219], v[112:115]\n"
    "v_exp_f32_e32 v203, v203\n"
    "v_mfma_f32_16x16x32_bf16 v[112:115], a[164:167], v[220:223], v[112:115]\n"
    "v_cvt_pk_bf16_f32 v212, v200, v201\n"
    "v_cvt_pk_bf16_f32 v213, v202, v203\n"
    "v_mfma_f32_16x16x32_bf16 v[116:119], a[168:171], v[216:219], v[116:119]\n"
    "v_exp_f32_e32 v204, v204\n"
    "v_mfma_f32_16x16x32_bf16 v[116:119], a[172:175], v[220:223], v[116:119]\n"
    "ds_read_b64_tr_b16 a[152:153], v241 offset:33816\n"
    "ds_read_b64_tr_b16 a[154:155], v241 offset:34072\n"
    "v_mfma_f32_16x16x32_bf16 v[192:195], a[64:67], a[16:19], v[228:231]\n"
    "v_exp_f32_e32 v205, v205\n"
    "v_mfma_f32_16x16x32_bf16 v[192:195], a[68:71], a[20:23], v[192:195]\n"
    "ds_read_b64_tr_b16 a[156:157], v241 offset:34328\n"
    "v_cvt_pk_bf16_f32 v214, v204, v205\n"
    "v_mfma_f32_16x16x32_bf16 v[192:195], a[72:75], a[24:27], v[192:195]\n"
    "v_exp_f32_e32 v206, v206\n"
    "v_mfma_f32_16x16x32_bf16 v[192:195], a[76:79], a[28:31], v[192:195]\n"
    "s_waitcnt vmcnt(17)\n"
    "ds_read_b64_tr_b16 a[158:159], v241 offset:34584\n"
    "ds_read_b128 v[136:139], v255 offset:0\n"
    "v_mfma_f32_16x16x32_bf16 v[120:123], a[176:179], v[216:219], v[120:123]\n"
    "v_exp_f32_e32 v207, v207\n"
    "v_mfma_f32_16x16x32_bf16 v[120:123], a[180:183], v[220:223], v[120:123]\n"
    "ds_read_b128 v[140:143], v255 offset:64\n"
    "v_cvt_pk_bf16_f32 v215, v206, v207\n"
    "v_mfma_f32_16x16x32_bf16 v[124:127], a[184:187], v[216:219], v[124:127]\n"
    "s_add_u32 m0, s62, 84488\n"
    "v_exp_f32_e32 v192, v192\n"
    "v_mfma_f32_16x16x32_bf16 v[124:127], a[188:191], v[220:223], v[124:127]\n"
    "buffer_load_dwordx4 v242, s[40:43], 0 offen offset:1024 nt lds\n"
    "ds_read_b64_tr_b16 a[160:161], v241 offset:33920\n"
    "v_mfma_f32_16x16x32_bf16 v[196:199], a[80:83], a[16:19], v[228:231]\n"
    "v_exp_f32_e32 v193, v193\n"
    "v_mfma_f32_16x16x32_bf16 v[196:199], a[84:87], a[20:23], v[196:199]\n"
    "ds_read_b64_tr_b16 a[162:163], v241 offset:34176\n"
    "ds_read_b64_tr_b16 a[164:165], v241 offset:34432\n"
    "v_mfma_f32_16x16x32_bf16 v[196:199], a[88:91], a[24:27], v[196:199]\n"
    "s_waitcnt lgkmcnt(14)\n"
    "v_exp_f32_e32 v194, v194\n"
    "v_mfma_f32_16x16x32_bf16 v[196:199], a[92:95], a[28:31], v[196:199]\n"
    "s_waitcnt lgkmcnt(3)\n"
    "ds_read_b64_tr_b16 a[166:167], v241 offset:34688\n"
    "ds_read_b64_tr_b16 a[168:169], v241 offset:33928\n"
    "v_mfma_f32_16x16x32_bf16 v[0:3], a[128:131], v[208:211], v[0:3]\n"
    "v_exp_f32_e32 v195, v195\n"
    "v_mfma_f32_16x16x32_bf16 v[0:3], a[132:135], v[212:215], v[0:3]\n"
    "s_waitcnt lgkmcnt(14)\n"
    "ds_read_b64_tr_b16 a[170:171], v241 offset:34184\n"
    "ds_read_b64_tr_b16 a[172:173], v241 offset:34440\n"
    "v_mfma_f32_16x16x32_bf16 v[4:7], a[136:139], v[208:211], v[4:7]\n"
    "v_exp_f32_e32 v196, v196\n"
    "v_mfma_f32_16x16x32_bf16 v[4:7], a[140:143], v[212:215], v[4:7]\n"
    "ds_read_b64_tr_b16 a[174:175], v241 offset:34696\n"
    "ds_read_b64_tr_b16 a[176:177], v241 offset:33936\n"
    "v_mfma_f32_16x16x32_bf16 v[200:203], a[96:99], a[16:19], v[228:231]\n"
    "s_add_u32 m0, s62, 84496\n"
    "v_exp_f32_e32 v197, v197\n"
    "v_mfma_f32_16x16x32_bf16 v[200:203], a[100:103], a[20:23], v[200:203]\n"
    "buffer_load_dwordx4 v242, s[40:43], 0 offen offset:2048 nt lds\n"
    "ds_read_b64_tr_b16 a[178:179], v241 offset:34192\n"
    "v_mfma_f32_16x16x32_bf16 v[200:203], a[104:107], a[24:27], v[200:203]\n"
    "v_exp_f32_e32 v198, v198\n"
    "v_mfma_f32_16x16x32_bf16 v[200:203], a[108:111], a[28:31], v[200:203]\n"
    "s_waitcnt lgkmcnt(14)\n"
    "ds_read_b64_tr_b16 a[180:181], v241 offset:34448\n"
    "ds_read_b64_tr_b16 a[182:183], v241 offset:34704\n"
    "v_mfma_f32_16x16x32_bf16 v[8:11], a[144:147], v[208:211], v[8:11]\n"
    "v_exp_f32_e32 v199, v199\n"
    "v_mfma_f32_16x16x32_bf16 v[8:11], a[148:151], v[212:215], v[8:11]\n"
    "s_waitcnt lgkmcnt(14)\n"
    "ds_read_b64_tr_b16 a[184:185], v241 offset:33944\n"
    "ds_read_b64_tr_b16 a[186:187], v241 offset:34200\n"
    "v_mfma_f32_16x16x32_bf16 v[12:15], a[152:155], v[208:211], v[12:15]\n"
    "v_exp_f32_e32 v200, v200\n"
    "v_mfma_f32_16x16x32_bf16 v[12:15], a[156:159], v[212:215], v[12:15]\n"
    "ds_read_b64_tr_b16 a[188:189], v241 offset:34456\n"
    "ds_read_b64_tr_b16 a[190:191], v241 offset:34712\n"
    "v_smfmac_f32_16x16x64_bf16 a[192:195], a[208:211], v[208:215], v254\n"
    "v_cvt_pk_bf16_f32 v216, v192, v193\n"
    "v_mfma_f32_16x16x32_bf16 v[204:207], a[112:115], a[16:19], v[228:231]\n"
    "s_add_u32 m0, s62, 84504\n"
    "v_exp_f32_e32 v201, v201\n"
    "v_mfma_f32_16x16x32_bf16 v[204:207], a[116:119], a[20:23], v[204:207]\n"
    "s_waitcnt vmcnt(23)\n"
    "ds_append v253\n"
    "v_cvt_pk_bf16_f32 v217, v194, v195\n"
    "v_mfma_f32_16x16x32_bf16 v[204:207], a[120:123], a[24:27], v[204:207]\n"
    "s_waitcnt lgkmcnt(13)\n"
    "v_exp_f32_e32 v202, v202\n"
    "v_mfma_f32_16x16x32_bf16 v[204:207], a[124:127], a[28:31], v[204:207]\n"
    "buffer_load_dwordx4 v242, s[40:43], 0 offen offset:3072 nt lds\n"
    "v_cvt_pk_bf16_f32 v218, v196, v197\n"
    "v_mfma_f32_16x16x32_bf16 v[16:19], a[160:163], v[208:211], v[16:19]\n"
    "s_add_u32 s40, s60, s71\n"
    "v_exp_f32_e32 v203, v203\n"
    "v_mfma_f32_16x16x32_bf16 v[16:19], a[164:167], v[212:215], v[16:19]\n"
    "s_waitcnt lgkmcnt(9)\n"
    "v_cvt_pk_bf16_f32 v219, v198, v199\n"
    "v_cvt_pk_bf16_f32 v220, v200, v201\n"
    "v_mfma_f32_16x16x32_bf16 v[20:23], a[168:171], v[208:211], v[20:23]\n"
    "s_addc_u32 s41, s61, 0\n"
    "v_exp_f32_e32 v204, v204\n"
    "v_mfma_f32_16x16x32_bf16 v[20:23], a[172:175], v[212:215], v[20:23]\n"
    "s_max_i32 s42, s58, 0\n"
    "v_cvt_pk_bf16_f32 v221, v202, v203\n"
    "v_lshlrev_b32_e32 v244, 16, v136\n"
    "v_mfma_f32_16x16x32_bf16 v[192:195], a[64:67], a[32:35], v[232:235]\n"
    "v_exp_f32_e32 v205, v205\n"
    "v_mfma_f32_16x16x32_bf16 v[192:195], a[68:71], a[36:39], v[192:195]\n"
    "v_cvt_pk_bf16_f32 v222, v204, v205\n"
    "v_and_b32_e32 v245, s67, v136\n"
    "v_mfma_f32_16x16x32_bf16 v[192:195], a[72:75], a[40:43], v[192:195]\n"
    "v_exp_f32_e32 v206, v206\n"
    "v_mfma_f32_16x16x32_bf16 v[192:195], a[76:79], a[44:47], v[192:195]\n"
    "s_waitcnt lgkmcnt(5)\n"
    "v_mul_f32_e32 v244, s64, v244\n"
    "v_mul_f32_e32 v245, s64, v245\n"
    "v_mfma_f32_16x16x32_bf16 v[24:27], a[176:179], v[208:211], v[24:27]\n"
    "v_exp_f32_e32 v207, v207\n"
    "v_mfma_f32_16x16x32_bf16 v[24:27], a[180:183], v[212:215], v[24:27]\n"
    "s_waitcnt lgkmcnt(1)\n"
    "v_cvt_pk_bf16_f32 v223, v206, v207\n"
    "v_cvt_pk_bf16_f32 v136, v244, v245\n"
    "v_mfma_f32_16x16x32_bf16 v[28:31], a[184:187], v[208:211], v[28:31]\n"
    "v_exp_f32_e32 v192, v192\n"
    "v_mfma_f32_16x16x32_bf16 v[28:31], a[188:191], v[212:215], v[28:31]\n"
    "ds_read_b32 v253, v251\n"
    "v_lshlrev_b32_e32 v244, 16, v137\n"
    "v_mfma_f32_16x16x32_bf16 v[196:199], a[80:83], a[32:35], v[232:235]\n"
    "v_exp_f32_e32 v193, v193\n"
    "v_mfma_f32_16x16x32_bf16 v[196:199], a[84:87], a[36:39], v[196:199]\n"
    "buffer_store_dwordx4 v[152:155], v250, s[48:51], 0 offen offset:128 sc0 sc1 nt\n"
    "v_cvt_pk_bf16_f32 v208, v192, v193\n"
    "v_and_b32_e32 v245, s67, v137\n"
    "v_mul_f32_e32 v244, s64, v244\n"
    "v_mul_f32_e64 v245, s64, v245\n"
    "v_mfma_f32_16x16x32_bf16 v[196:199], a[88:91], a[40:43], v[196:199]\n"
    "v_exp_f32_e32 v194, v194\n"
    "v_mfma_f32_16x16x32_bf16 v[196:199], a[92:95], a[44:47], v[196:199]\n"
    "v_cvt_pk_bf16_f32 v137, v244, v245\n"
    "v_lshlrev_b32_e32 v244, 16, v138\n"
    "v_mfma_f32_16x16x32_bf16 v[32:35], a[128:131], v[216:219], v[32:35]\n"
    "v_exp_f32_e32 v195, v195\n"
    "v_mfma_f32_16x16x32_bf16 v[32:35], a[132:135], v[220:223], v[32:35]\n"
    "s_waitcnt lgkmcnt(0)\n"
    "v_cmp_le_u32_e32 vcc, s76, v253\n"
    "v_cvt_pk_bf16_f32 v209, v194, v195\n"
    "v_mfma_f32_16x16x32_bf16 v[36:39], a[136:139], v[216:219], v[36:39]\n"
    "v_exp_f32_e32 v196, v196\n"
    "v_mfma_f32_16x16x32_bf16 v[36:39], a[140:143], v[220:223], v[36:39]\n"
    "v_and_b32_e32 v245, s67, v138\n"
    "v_mul_f32_e32 v244, s64, v244\n"
    "v_mfma_f32_16x16x32_bf16 v[200:203], a[96:99], a[32:35], v[232:235]\n"
    "s_cbranch_vccz LSPIN5_%=\n"
    "LRET5_%=:\n"
    "v_exp_f32_e32 v197, v197\n"
    "v_mfma_f32_16x16x32_bf16 v[200:203], a[100:103], a[36:39], v[200:203]\n"
    "s_addk_i32 s76, 256\n"
    "v_cvt_pk_bf16_f32 v210, v196, v197\n"
    "v_mul_f32_e32 v245, s64, v245\n"
    "v_mfma_f32_16x16x32_bf16 v[200:203], a[104:107], a[40:43], v[200:203]\n"
    "s_add_u32 m0, s62, 33792\n"
    "v_exp_f32_e32 v198, v198\n"
    "v_mfma_f32_16x16x32_bf16 v[200:203], a[108:111], a[44:47], v[200:203]\n"
    "buffer_load_dwordx4 v242, s[36:39], 0 offen offset:0 nt lds\n"
    "v_cvt_pk_bf16_f32 v138, v244, v245\n"
    "v_mfma_f32_16x16x32_bf16 v[40:43], a[144:147], v[216:219], v[40:43]\n"
    "v_exp_f32_e32 v199, v199\n"
    "v_mfma_f32_16x16x32_bf16 v[40:43], a[148:151], v[220:223], v[40:43]\n"
    "v_cvt_pk_bf16_f32 v211, v198, v199\n"
    "v_lshlrev_b32_e32 v244, 16, v139\n"
    "v_mfma_f32_16x16x32_bf16 v[44:47], a[152:155], v[216:219], v[44:47]\n"
    "v_exp_f32_e32 v200, v200\n"
    "v_mfma_f32_16x16x32_bf16 v[44:47], a[156:159], v[220:223], v[44:47]\n"
    "v_and_b32_e32 v245, s67, v139\n"
    "v_mul_f32_e32 v244, s64, v244\n"
    "v_smfmac_f32_16x16x64_bf16 a[196:199], a[208:211], v[216:223], v254\n"
    "v_mul_f32_e32 v245, s64, v245\n"
    "v_mfma_f32_16x16x32_bf16 v[204:207], a[112:115], a[32:35], v[232:235]\n"
    "v_exp_f32_e32 v201, v201\n"
    "v_mfma_f32_16x16x32_bf16 v[204:207], a[116:119], a[36:39], v[204:207]\n"
    "v_cvt_pk_bf16_f32 v212, v200, v201\n"
    "v_cvt_pk_bf16_f32 v139, v244, v245\n"
    "v_mfma_f32_16x16x32_bf16 v[204:207], a[120:123], a[40:43], v[204:207]\n"
    "v_exp_f32_e32 v202, v202\n"
    "v_mfma_f32_16x16x32_bf16 v[204:207], a[124:127], a[44:47], v[204:207]\n"
    "v_lshlrev_b32_e32 v244, 16, v140\n"
    "v_and_b32_e32 v245, s67, v140\n"
    "v_mfma_f32_16x16x32_bf16 v[48:51], a[160:163], v[216:219], v[48:51]\n"
    "s_sub_u32 m0, s95, 128\n"
    "v_exp_f32_e32 v203, v203\n"
    "v_mfma_f32_16x16x32_bf16 v[48:51], a[164:167], v[220:223], v[48:51]\n"
    "buffer_load_dwordx4 v243, s[44:47], 0 offen offset:128 lds\n"
    "v_cvt_pk_bf16_f32 v213, v202, v203\n"
    "v_mfma_f32_16x16x32_bf16 v[52:55], a[168:171], v[216:219], v[52:55]\n"
    "v_exp_f32_e32 v204, v204\n"
    "v_mfma_f32_16x16x32_bf16 v[52:55], a[172:175], v[220:223], v[52:55]\n"
    "v_mul_f32_e32 v244, s64, v244\n"
    "v_mul_f32_e32 v245, s64, v245\n"
    "v_mfma_f32_16x16x32_bf16 v[192:195], a[64:67], a[48:51], v[236:239]\n"
    "v_exp_f32_e32 v205, v205\n"
    "v_mfma_f32_16x16x32_bf16 v[192:195], a[68:71], a[52:55], v[192:195]\n"
    "ds_read_b128 a[64:67], v240 offset:50688\n"
    "v_cvt_pk_bf16_f32 v214, v204, v205\n"
    "v_mfma_f32_16x16x32_bf16 v[192:195], a[72:75], a[56:59], v[192:195]\n"
    "v_exp_f32_e32 v206, v206\n"
    "v_mfma_f32_16x16x32_bf16 v[192:195], a[76:79], a[60:63], v[192:195]\n"
    "ds_read_b128 a[68:71], v240 offset:50752\n"
    "ds_read_b128 a[72:75], v240 offset:50816\n"
    "v_mfma_f32_16x16x32_bf16 v[56:59], a[176:179], v[216:219], v[56:59]\n"
    "v_exp_f32_e32 v207, v207\n"
    "v_mfma_f32_16x16x32_bf16 v[56:59], a[180:183], v[220:223], v[56:59]\n"
    "ds_read_b128 a[76:79], v240 offset:50880\n"
    "v_cvt_pk_bf16_f32 v215, v206, v207\n"
    "v_mfma_f32_16x16x32_bf16 v[60:63], a[184:187], v[216:219], v[60:63]\n"
    "s_sub_u32 m0, s95, 1088\n"
    "v_exp_f32_e32 v192, v192\n"
    "v_mfma_f32_16x16x32_bf16 v[60:63], a[188:191], v[220:223], v[60:63]\n"
    "buffer_load_dwordx4 v243, s[44:47], 0 offen offset:2176 lds\n"
    "v_cvt_pk_bf16_f32 v140, v244, v245\n"
    "v_mfma_f32_16x16x32_bf16 v[196:199], a[80:83], a[48:51], v[236:239]\n"
    "s_add_u32 s44, s44, 16384\n"
    "v_exp_f32_e32 v193, v193\n"
    "v_mfma_f32_16x16x32_bf16 v[196:199], a[84:87], a[52:55], v[196:199]\n"
    "s_addc_u32 s45, s45, 0\n"
    "ds_read_b128 a[80:83], v240 offset:50944\n"
    "v_cvt_pk_bf16_f32 v216, v192, v193\n"
    "v_mfma_f32_16x16x32_bf16 v[196:199], a[88:91], a[56:59], v[196:199]\n"
    "s_mul_i32 s46, s54, s56\n"
    "v_exp_f32_e32 v194, v194\n"
    "v_mfma_f32_16x16x32_bf16 v[196:199], a[92:95], a[60:63], v[196:199]\n"
    "ds_read_b128 a[84:87], v240 offset:51008\n"
    "ds_read_b128 a[88:91], v240 offset:51072\n"
    "v_mfma_f32_16x16x32_bf16 v[64:67], a[128:131], v[208:211], v[64:67]\n"
    "v_exp_f32_e32 v195, v195\n"
    "v_mfma_f32_16x16x32_bf16 v[64:67], a[132:135], v[212:215], v[64:67]\n"
    "ds_read_b128 a[92:95], v240 offset:51136\n"
    "v_cvt_pk_bf16_f32 v217, v194, v195\n"
    "v_mfma_f32_16x16x32_bf16 v[68:71], a[136:139], v[208:211], v[68:71]\n"
    "v_exp_f32_e32 v196, v196\n"
    "v_mfma_f32_16x16x32_bf16 v[68:71], a[140:143], v[212:215], v[68:71]\n"
    "v_lshlrev_b32_e32 v244, 16, v141\n"
    "v_and_b32_e32 v245, s67, v141\n"
    "v_mfma_f32_16x16x32_bf16 v[200:203], a[96:99], a[48:51], v[236:239]\n"
    "s_add_u32 m0, s62, 33824\n"
    "v_exp_f32_e32 v197, v197\n"
    "v_mfma_f32_16x16x32_bf16 v[200:203], a[100:103], a[52:55], v[200:203]\n"
    "buffer_load_dwordx4 v242, s[36:39], 0 offen offset:1024 nt lds\n"
    "ds_read_b128 a[96:99], v240 offset:51200\n"
    "v_mfma_f32_16x16x32_bf16 v[200:203], a[104:107], a[56:59], v[200:203]\n"
    "v_exp_f32_e32 v198, v198\n"
    "v_mfma_f32_16x16x32_bf16 v[200:203], a[108:111], a[60:63], v[200:203]\n"
    "ds_read_b128 a[100:103], v240 offset:51264\n"
    "v_cvt_pk_bf16_f32 v218, v196, v197\n"
    "v_mfma_f32_16x16x32_bf16 v[72:75], a[144:147], v[208:211], v[72:75]\n"
    "v_exp_f32_e32 v199, v199\n"
    "v_mfma_f32_16x16x32_bf16 v[72:75], a[148:151], v[212:215], v[72:75]\n"
    "ds_read_b128 a[104:107], v240 offset:51328\n"
    "ds_read_b128 a[108:111], v240 offset:51392\n"
    "v_mfma_f32_16x16x32_bf16 v[76:79], a[152:155], v[208:211], v[76:79]\n"
    "v_exp_f32_e32 v200, v200\n"
    "v_mfma_f32_16x16x32_bf16 v[76:79], a[156:159], v[212:215], v[76:79]\n"
    "v_cvt_pk_bf16_f32 v219, v198, v199\n"
    "v_mul_f32_e32 v244, s64, v244\n"
    "v_smfmac_f32_16x16x64_bf16 a[200:203], a[208:211], v[208:215], v254\n"
    "v_mul_f32_e32 v245, s64, v245\n"
    "v_mfma_f32_16x16x32_bf16 v[204:207], a[112:115], a[48:51], v[236:239]\n"
    "v_exp_f32_e32 v201, v201\n"
    "v_mfma_f32_16x16x32_bf16 v[204:207], a[116:119], a[52:55], v[204:207]\n"
    "ds_read_b128 a[112:115], v240 offset:51456\n"
    "v_cvt_pk_bf16_f32 v220, v200, v201\n"
    "v_mfma_f32_16x16x32_bf16 v[204:207], a[120:123], a[56:59], v[204:207]\n"
    "s_add_u32 m0, s62, 33856\n"
    "v_exp_f32_e32 v202, v202\n"
    "v_mfma_f32_16x16x32_bf16 v[204:207], a[124:127], a[60:63], v[204:207]\n"
    "buffer_load_dwordx4 v242, s[36:39], 0 offen offset:2048 nt lds\n"
    "ds_read_b128 a[116:119], v240 offset:51520\n"
    "v_mfma_f32_16x16x32_bf16 v[80:83], a[160:163], v[208:211], v[80:83]\n"
    "v_exp_f32_e32 v203, v203\n"
    "v_mfma_f32_16x16x32_bf16 v[80:83], a[164:167], v[212:215], v[80:83]\n"
    "ds_read_b128 a[120:123], v240 offset:51584\n"
    "v_cvt_pk_bf16_f32 v221, v202, v203\n"
    "v_mfma_f32_16x16x32_bf16 v[84:87], a[168:171], v[208:211], v[84:87]\n"
    "LW9_%=:\n"
    "s_cbranch_execz LWR_%=\n"
    "v_exp_f32_e32 v204, v204\n"
    "v_mfma_f32_16x16x32_bf16 v[84:87], a[172:175], v[212:215], v[84:87]\n"
    "s_waitcnt lgkmcnt(11)\n"
    "ds_read_b128 a[124:127], v240 offset:51648\n"
    "v_cvt_pk_bf16_f32 v141, v244, v245\n"
    "v_mfma_f32_16x16x32_bf16 v[192:195], a[64:67], a[0:3], v[224:227]\n"
    "v_exp_f32_e32 v205, v205\n"
    "v_mfma_f32_16x16x32_bf16 v[192:195], a[68:71], a[4:7], v[192:195]\n"
    "v_cvt_pk_bf16_f32 v222, v204, v205\n"
    "v_lshlrev_b32_e32 v244, 16, v142\n"
    "v_mfma_f32_16x16x32_bf16 v[192:195], a[72:75], a[8:11], v[192:195]\n"
    "v_exp_f32_e32 v206, v206\n"
    "v_mfma_f32_16x16x32_bf16 v[192:195], a[76:79], a[12:15], v[192:195]\n"
    "v_and_b32_e32 v245, s67, v142\n"
    "v_mul_f32_e32 v244, s64, v244\n"
    "v_mfma_f32_16x16x32_bf16 v[88:91], a[176:179], v[208:211], v[88:91]\n"
    "v_exp_f32_e32 v207, v207\n"
    "v_mfma_f32_16x16x32_bf16 v[88:91], a[180:183], v[212:215], v[88:91]\n"
    "v_cvt_pk_bf16_f32 v223, v206, v207\n"
    "v_mul_f32_e32 v245, s64, v245\n"
    "v_mfma_f32_16x16x32_bf16 v[92:95], a[184:187], v[208:211], v[92:95]\n"
    "s_waitcnt lgkmcnt(8)\n"
    "v_exp_f32_e32 v192, v192\n"
    "v_mfma_f32_16x16x32_bf16 v[92:95], a[188:191], v[212:215], v[92:95]\n"
    "buffer_store_dwordx4 v[156:159], v250, s[48:51], 0 offen offset:384 sc0 sc1 nt\n"
    "v_cvt_pk_bf16_f32 v142, v244, v245\n"
    "v_lshlrev_b32_e32 v244, 16, v143\n"
    "v_and_b32_e32 v245, s67, v143\n"
    "v_mul_f32_e64 v244, s64, v244\n"
    "v_mfma_f32_16x16x32_bf16 v[196:199], a[80:83], a[0:3], v[224:227]\n"
    "s_add_u32 s48, s48, 16384\n"
    "v_exp_f32_e32 v193, v193\n"
    "v_mfma_f32_16x16x32_bf16 v[196:199], a[84:87], a[4:7], v[196:199]\n"
    "s_addc_u32 s49, s49, 0\n"
    "v_cvt_pk_bf16_f32 v208, v192, v193\n"
    "v_mul_f32_e32 v245, s64, v245\n"
    "v_mfma_f32_16x16x32_bf16 v[196:199], a[88:91], a[8:11], v[196:199]\n"
    "s_mul_i32 s50, s54, s57\n"
    "v_exp_f32_e32 v194, v194\n"
    "v_mfma_f32_16x16x32_bf16 v[196:199], a[92:95], a[12:15], v[196:199]\n"
    "v_cvt_pk_bf16_f32 v143, v244, v245\n"
    "v_mfma_f32_16x16x32_bf16 v[96:99], a[128:131], v[216:219], v[96:99]\n"
    "v_exp_f32_e32 v195, v195\n"
    "v_mfma_f32_16x16x32_bf16 v[96:99], a[132:135], v[220:223], v[96:99]\n"
    "ds_read_b64_tr_b16 a[128:129], v241 offset:50688\n"
    "v_cvt_pk_bf16_f32 v209, v194, v195\n"
    "v_mfma_f32_16x16x32_bf16 v[100:103], a[136:139], v[216:219], v[100:103]\n"
    "v_exp_f32_e32 v196, v196\n"
    "v_mfma_f32_16x16x32_bf16 v[100:103], a[140:143], v[220:223], v[100:103]\n"
    "s_waitcnt lgkmcnt(5)\n"
    "ds_read_b64_tr_b16 a[130:131], v241 offset:50944\n"
    "ds_read_b64_tr_b16 a[132:133], v241 offset:51200\n"
    "v_mfma_f32_16x16x32_bf16 v[200:203], a[96:99], a[0:3], v[224:227]\n"
    "s_add_u32 m0, s62, 33888\n"
    "v_exp_f32_e32 v197, v197\n"
    "v_mfma_f32_16x16x32_bf16 v[200:203], a[100:103], a[4:7], v[200:203]\n"
    "buffer_load_dwordx4 v242, s[36:39], 0 offen offset:3072 nt lds\n"
    "ds_read_b64_tr_b16 a[134:135], v241 offset:51456\n"
    "v_mfma_f32_16x16x32_bf16 v[200:203], a[104:107], a[8:11], v[200:203]\n"
    "s_addk_i32 s59, 16384\n"
    "v_exp_f32_e32 v198, v198\n"
    "v_mfma_f32_16x16x32_bf16 v[200:203], a[108:111], a[12:15], v[200:203]\n"
    "s_xor_b32 s71, s59, s63\n"
    "ds_read_b64_tr_b16 a[136:137], v241 offset:50696\n"
    "ds_read_b64_tr_b16 a[138:139], v241 offset:50952\n"
    "v_mfma_f32_16x16x32_bf16 v[104:107], a[144:147], v[216:219], v[104:107]\n"
    "s_add_u32 s36, s84, s71\n"
    "v_exp_f32_e32 v199, v199\n"
    "v_mfma_f32_16x16x32_bf16 v[104:107], a[148:151], v[220:223], v[104:107]\n"
    "s_addc_u32 s37, s85, 0\n"
    "ds_read_b64_tr_b16 a[140:141], v241 offset:51208\n"
    "ds_read_b64_tr_b16 a[142:143], v241 offset:51464\n"
    "v_mfma_f32_16x16x32_bf16 v[108:111], a[152:155], v[216:219], v[108:111]\n"
    "s_addk_i32 s58, -16384\n"
    "v_exp_f32_e32 v200, v200\n"
    "v_mfma_f32_16x16x32_bf16 v[108:111], a[156:159], v[220:223], v[108:111]\n"
    "s_max_i32 s38, s58, 0\n"
    "v_cvt_pk_bf16_f32 v210, v196, v197\n"
    "v_cvt_pk_bf16_f32 v211, v198, v199\n"
    "v_smfmac_f32_16x16x64_bf16 a[204:207], a[208:211], v[216:223], v254\n"
    "s_waitcnt lgkmcnt(8)\n"
    "ds_read_b64_tr_b16 a[144:145], v241 offset:50704\n"
    "v_mfma_f32_16x16x32_bf16 v[204:207], a[112:115], a[0:3], v[224:227]\n"
    "v_exp_f32_e32 v201, v201\n"
    "v_mfma_f32_16x16x32_bf16 v[204:207], a[116:119], a[4:7], v[204:207]\n"
    "ds_read_b64_tr_b16 a[146:147], v241 offset:50960\n"
    "ds_read_b64_tr_b16 a[148:149], v241 offset:51216\n"
    "v_mfma_f32_16x16x32_bf16 v[204:207], a[120:123], a[8:11], v[204:207]\n"
    "s_add_u32 m0, s62, 101376\n"
    "v_exp_f32_e32 v202, v202\n"
    "v_mfma_f32_16x16x32_bf16 v[204:207], a[124:127], a[12:15], v[204:207]\n"
    "buffer_load_dwordx4 v242, s[40:43], 0 offen offset:0 nt lds\n"
    "ds_read_b64_tr_b16 a[150:151], v241 offset:51472\n"
    "v_mfma_f32_16x16x32_bf16 v[112:115], a[160:163], v[216:219], v[112:115]\n"
    "v_exp_f32_e32 v203, v203\n"
    "v_mfma_f32_16x16x32_bf16 v[112:115], a[164:167], v[220:223], v[112:115]\n"
    "v_cvt_pk_bf16_f32 v212, v200, v201\n"
    "v_cvt_pk_bf16_f32 v213, v202, v203\n"
    "v_mfma_f32_16x16x32_bf16 v[116:119], a[168:171], v[216:219], v[116:119]\n"
    "v_exp_f32_e32 v204, v204\n"
    "v_mfma_f32_16x16x32_bf16 v[116:119], a[172:175], v[220:223], v[116:119]\n"
    "ds_read_b64_tr_b16 a[152:153], v241 offset:50712\n"
    "ds_read_b64_tr_b16 a[154:155], v241 offset:50968\n"
    "v_mfma_f32_16x16x32_bf16 v[192:195], a[64:67], a[16:19], v[228:231]\n"
    "v_exp_f32_e32 v205, v205\n"
    "v_mfma_f32_16x16x32_bf16 v[192:195], a[68:71], a[20:23], v[192:195]\n"
    "ds_read_b64_tr_b16 a[156:157], v241 offset:51224\n"
    "v_cvt_pk_bf16_f32 v214, v204, v205\n"
    "v_mfma_f32_16x16x32_bf16 v[192:195], a[72:75], a[24:27], v[192:195]\n"
    "v_exp_f32_e32 v206, v206\n"
    "v_mfma_f32_16x16x32_bf16 v[192:195], a[76:79], a[28:31], v[192:195]\n"
    "ds_read_b64_tr_b16 a[158:159], v241 offset:51480\n"
    "ds_read_b64_tr_b16 a[160:161], v241 offset:50816\n"
    "v_mfma_f32_16x16x32_bf16 v[120:123], a[176:179], v[216:219], v[120:123]\n"
    "v_exp_f32_e32 v207, v207\n"
    "v_mfma_f32_16x16x32_bf16 v[120:123], a[180:183], v[220:223], v[120:123]\n"
    "ds_read_b64_tr_b16 a[162:163], v241 offset:51072\n"
    "v_cvt_pk_bf16_f32 v215, v206, v207\n"
    "v_mfma_f32_16x16x32_bf16 v[124:127], a[184:187], v[216:219], v[124:127]\n"
    "s_add_u32 m0, s62, 101384\n"
    "v_exp_f32_e32 v192, v192\n"
    "v_mfma_f32_16x16x32_bf16 v[124:127], a[188:191], v[220:223], v[124:127]\n"
    "buffer_load_dwordx4 v242, s[40:43], 0 offen offset:1024 nt lds\n"
    "ds_read_b64_tr_b16 a[164:165], v241 offset:51328\n"
    "v_mfma_f32_16x16x32_bf16 v[196:199], a[80:83], a[16:19], v[228:231]\n"
    "v_exp_f32_e32 v193, v193\n"
    "v_mfma_f32_16x16x32_bf16 v[196:199], a[84:87], a[20:23], v[196:199]\n"
    "ds_read_b64_tr_b16 a[166:167], v241 offset:51584\n"
    "ds_read_b64_tr_b16 a[168:169], v241 offset:50824\n"
    "v_mfma_f32_16x16x32_bf16 v[196:199], a[88:91], a[24:27], v[196:199]\n"
    "v_exp_f32_e32 v194, v194\n"
    "v_mfma_f32_16x16x32_bf16 v[196:199], a[92:95], a[28:31], v[196:199]\n"
    "s_waitcnt lgkmcnt(14)\n"
    "ds_read_b64_tr_b16 a[170:171], v241 offset:51080\n"
    "ds_read_b64_tr_b16 a[172:173], v241 offset:51336\n"
    "v_mfma_f32_16x16x32_bf16 v[0:3], a[128:131], v[208:211], v[0:3]\n"
    "s_waitcnt lgkmcnt(14)\n"
    "v_exp_f32_e32 v195, v195\n"
    "v_mfma_f32_16x16x32_bf16 v[0:3], a[132:135], v[212:215], v[0:3]\n"
    "s_waitcnt vmcnt(18)\n"
    "ds_read_b64_tr_b16 a[174:175], v241 offset:51592\n"
    "ds_read_b128 v[144:147], v255 offset:2112\n"
    "v_mfma_f32_16x16x32_bf16 v[4:7], a[136:139], v[208:211], v[4:7]\n"
    "v_exp_f32_e32 v196, v196\n"
    "v_mfma_f32_16x16x32_bf16 v[4:7], a[140:143], v[212:215], v[4:7]\n"
    "ds_read_b128 v[148:151], v255 offset:2176\n"
    "ds_read_b64_tr_b16 a[176:177], v241 offset:50832\n"
    "v_mfma_f32_16x16x32_bf16 v[200:203], a[96:99], a[16:19], v[228:231]\n"
    "s_add_u32 m0, s62, 101392\n"
    "v_exp_f32_e32 v197, v197\n"
    "v_mfma_f32_16x16x32_bf16 v[200:203], a[100:103], a[20:23], v[200:203]\n"
    "buffer_load_dwordx4 v242, s[40:43], 0 offen offset:2048 nt lds\n"
    "ds_read_b64_tr_b16 a[178:179], v241 offset:51088\n"
    "v_mfma_f32_16x16x32_bf16 v[200:203], a[104:107], a[24:27], v[200:203]\n"
    "v_exp_f32_e32 v198, v198\n"
    "v_mfma_f32_16x16x32_bf16 v[200:203], a[108:111], a[28:31], v[200:203]\n"
    "s_waitcnt lgkmcnt(14)\n"
    "ds_read_b64_tr_b16 a[180:181], v241 offset:51344\n"
    "ds_read_b64_tr_b16 a[182:183], v241 offset:51600\n"
    "v_mfma_f32_16x16x32_bf16 v[8:11], a[144:147], v[208:211], v[8:11]\n"
    "s_waitcnt lgkmcnt(14)\n"
    "v_exp_f32_e32 v199, v199\n"
    "v_mfma_f32_16x16x32_bf16 v[8:11], a[148:151], v[212:215], v[8:11]\n"
    "s_waitcnt lgkmcnt(4)\n"
    "ds_read_b64_tr_b16 a[184:185], v241 offset:50840\n"
    "ds_read_b64_tr_b16 a[186:187], v241 offset:51096\n"
    "v_mfma_f32_16x16x32_bf16 v[12:15], a[152:155], v[208:211], v[12:15]\n"
    "v_exp_f32_e32 v200, v200\n"
    "v_mfma_f32_16x16x32_bf16 v[12:15], a[156:159], v[212:215], v[12:15]\n"
    "ds_read_b64_tr_b16 a[188:189], v241 offset:51352\n"
    "ds_read_b64_tr_b16 a[190:191], v241 offset:51608\n"
    "v_smfmac_f32_16x16x64_bf16 a[192:195], a[208:211], v[208:215], v254\n"
    "v_cvt_pk_bf16_f32 v216, v192, v193\n"
    "v_mfma_f32_16x16x32_bf16 v[204:207], a[112:115], a[16:19], v[228:231]\n"
    "s_add_u32 m0, s62, 101400\n"
    "v_exp_f32_e32 v201, v201\n"
    "v_mfma_f32_16x16x32_bf16 v[204:207], a[116:119], a[20:23], v[204:207]\n"
    "s_waitcnt vmcnt(23)\n"
    "ds_append v253\n"
    "v_cvt_pk_bf16_f32 v217, v194, v195\n"
    "v_mfma_f32_16x16x32_bf16 v[204:207], a[120:123], a[24:27], v[204:207]\n"
    "s_waitcnt lgkmcnt(14)\n"
    "v_exp_f32_e32 v202, v202\n"
    "v_mfma_f32_16x16x32_bf16 v[204:207], a[124:127], a[28:31], v[204:207]\n"
    "buffer_load_dwordx4 v242, s[40:43], 0 offen offset:3072 nt lds\n"
    "v_cvt_pk_bf16_f32 v218, v196, v197\n"
    "v_mfma_f32_16x16x32_bf16 v[16:19], a[160:163], v[208:211], v[16:19]\n"
    "s_add_u32 s40, s60, s71\n"
    "v_exp_f32_e32 v203, v203\n"
    "v_mfma_f32_16x16x32_bf16 v[16:19], a[164:167], v[212:215], v[16:19]\n"
    "s_waitcnt lgkmcnt(11)\n"
    "v_cvt_pk_bf16_f32 v219, v198, v199\n"
    "v_cvt_pk_bf16_f32 v220, v200, v201\n"
    "v_mfma_f32_16x16x32_bf16 v[20:23], a[168:171], v[208:211], v[20:23]\n"
    "s_addc_u32 s41, s61, 0\n"
    "v_exp_f32_e32 v204, v204\n"
    "v_mfma_f32_16x16x32_bf16 v[20:23], a[172:175], v[212:215], v[20:23]\n"
    "s_max_i32 s42, s58, 0\n"
    "v_cvt_pk_bf16_f32 v221, v202, v203\n"
    "v_lshlrev_b32_e32 v244, 16, v144\n"
    "v_mfma_f32_16x16x32_bf16 v[192:195], a[64:67], a[32:35], v[232:235]\n"
    "v_exp_f32_e32 v205, v205\n"
    "v_mfma_f32_16x16x32_bf16 v[192:195], a[68:71], a[36:39], v[192:195]\n"
    "v_cvt_pk_bf16_f32 v222, v204, v205\n"
    "v_and_b32_e32 v245, s67, v144\n"
    "v_mfma_f32_16x16x32_bf16 v[192:195], a[72:75], a[40:43], v[192:195]\n"
    "v_exp_f32_e32 v206, v206\n"
    "v_mfma_f32_16x16x32_bf16 v[192:195], a[76:79], a[44:47], v[192:195]\n"
    "s_waitcnt lgkmcnt(5)\n"
    "v_mul_f32_e32 v244, s64, v244\n"
    "v_mul_f32_e32 v245, s64, v245\n"
    "v_mfma_f32_16x16x32_bf16 v[24:27], a[176:179], v[208:211], v[24:27]\n"
    "v_exp_f32_e32 v207, v207\n"
    "v_mfma_f32_16x16x32_bf16 v[24:27], a[180:183], v[212:215], v[24:27]\n"
    "s_waitcnt lgkmcnt(1)\n"
    "v_cvt_pk_bf16_f32 v223, v206, v207\n"
    "v_cvt_pk_bf16_f32 v144, v244, v245\n"
    "v_mfma_f32_16x16x32_bf16 v[28:31], a[184:187], v[208:211], v[28:31]\n"
    "v_exp_f32_e32 v192, v192\n"
    "v_mfma_f32_16x16x32_bf16 v[28:31], a[188:191], v[212:215], v[28:31]\n"
    "ds_read_b32 v253, v251\n"
    "v_lshlrev_b32_e32 v244, 16, v145\n"
    "v_mfma_f32_16x16x32_bf16 v[196:199], a[80:83], a[32:35], v[232:235]\n"
    "v_exp_f32_e64 v193, v193\n"
    "v_mfma_f32_16x16x32_bf16 v[196:199], a[84:87], a[36:39], v[196:199]\n"
    "buffer_store_dwordx4 v[160:163], v250, s[48:51], 0 offen offset:0 sc0 sc1 nt\n"
    "v_cvt_pk_bf16_f32 v208, v192, v193\n"
    "v_and_b32_e32 v245, s67, v145\n"
    "v_mul_f32_e32 v244, s64, v244\n"
    "v_mul_f32_e64 v245, s64, v245\n"
    "v_mfma_f32_16x16x32_bf16 v[196:199], a[88:91], a[40:43], v[196:199]\n"
    "v_exp_f32_e32 v194, v194\n"
    "v_mfma_f32_16x16x32_bf16 v[196:199], a[92:95], a[44:47], v[196:199]\n"
    "v_cvt_pk_bf16_f32 v145, v244, v245\n"
    "v_lshlrev_b32_e32 v244, 16, v146\n"
    "v_mfma_f32_16x16x32_bf16 v[32:35], a[128:131], v[216:219], v[32:35]\n"
    "v_exp_f32_e32 v195, v195\n"
    "v_mfma_f32_16x16x32_bf16 v[32:35], a[132:135], v[220:223], v[32:35]\n"
    "s_waitcnt lgkmcnt(0)\n"
    "v_cmp_le_u32_e32 vcc, s76, v253\n"
    "v_cvt_pk_bf16_f32 v209, v194, v195\n"
    "v_mfma_f32_16x16x32_bf16 v[36:39], a[136:139], v[216:219], v[36:39]\n"
    "v_exp_f32_e32 v196, v196\n"
    "v_mfma_f32_16x16x32_bf16 v[36:39], a[140:143], v[220:223], v[36:39]\n"
    "v_and_b32_e32 v245, s67, v146\n"
    "v_mul_f32_e32 v244, s64, v244\n"
    "v_mfma_f32_16x16x32_bf16 v[200:203], a[96:99], a[32:35], v[232:235]\n"
    "s_cbranch_vccz LSPIN6_%=\n"
    "LRET6_%=:\n"
    "v_exp_f32_e32 v197, v197\n"
    "v_mfma_f32_16x16x32_bf16 v[200:203], a[100:103], a[36:39], v[200:203]\n"
    "s_addk_i32 s76, 256\n"
    "v_cvt_pk_bf16_f32 v210, v196, v197\n"
    "v_mul_f32_e32 v245, s64, v245\n"
    "v_mfma_f32_16x16x32_bf16 v[200:203], a[104:107], a[40:43], v[200:203]\n"
    "s_add_u32 m0, s62, 50688\n"
    "v_exp_f32_e32 v198, v198\n"
    "v_mfma_f32_16x16x32_bf16 v[200:203], a[108:111], a[44:47], v[200:203]\n"
    "buffer_load_dwordx4 v242, s[36:39], 0 offen offset:0 nt lds\n"
    "v_cvt_pk_bf16_f32 v146, v244, v245\n"
    "v_mfma_f32_16x16x32_bf16 v[40:43], a[144:147], v[216:219], v[40:43]\n"
    "v_exp_f32_e32 v199, v199\n"
    "v_mfma_f32_16x16x32_bf16 v[40:43], a[148:151], v[220:223], v[40:43]\n"
    "v_cvt_pk_bf16_f32 v211, v198, v199\n"
    "v_lshlrev_b32_e32 v244, 16, v147\n"
    "v_mfma_f32_16x16x32_bf16 v[44:47], a[152:155], v[216:219], v[44:47]\n"
    "v_exp_f32_e32 v200, v200\n"
    "v_mfma_f32_16x16x32_bf16 v[44:47], a[156:159], v[220:223], v[44:47]\n"
    "v_and_b32_e32 v245, s67, v147\n"
    "v_mul_f32_e32 v244, s64, v244\n"
    "v_smfmac_f32_16x16x64_bf16 a[196:199], a[208:211], v[216:223], v254\n"
    "v_mul_f32_e32 v245, s64, v245\n"
    "v_mfma_f32_16x16x32_bf16 v[204:207], a[112:115], a[32:35], v[232:235]\n"
    "v_exp_f32_e32 v201, v201\n"
    "v_mfma_f32_16x16x32_bf16 v[204:207], a[116:119], a[36:39], v[204:207]\n"
    "v_cvt_pk_bf16_f32 v212, v200, v201\n"
    "v_cvt_pk_bf16_f32 v147, v244, v245\n"
    "v_mfma_f32_16x16x32_bf16 v[204:207], a[120:123], a[40:43], v[204:207]\n"
    "v_exp_f32_e32 v202, v202\n"
    "v_mfma_f32_16x16x32_bf16 v[204:207], a[124:127], a[44:47], v[204:207]\n"
    "v_lshlrev_b32_e32 v244, 16, v148\n"
    "v_and_b32_e32 v245, s67, v148\n"
    "v_mfma_f32_16x16x32_bf16 v[48:51], a[160:163], v[216:219], v[48:51]\n"
    "s_add_u32 m0, s95, 2112\n"
    "v_exp_f32_e32 v203, v203\n"
    "v_mfma_f32_16x16x32_bf16 v[48:51], a[164:167], v[220:223], v[48:51]\n"
    "buffer_load_dwordx4 v243, s[44:47], 0 offen offset:0 lds\n"
    "v_cvt_pk_bf16_f32 v213, v202, v203\n"
    "v_mfma_f32_16x16x32_bf16 v[52:55], a[168:171], v[216:219], v[52:55]\n"
    "v_exp_f32_e32 v204, v204\n"
    "v_mfma_f32_16x16x32_bf16 v[52:55], a[172:175], v[220:223], v[52:55]\n"
    "v_mul_f32_e32 v244, s64, v244\n"
    "v_mul_f32_e32 v245, s64, v245\n"
    "v_mfma_f32_16x16x32_bf16 v[192:195], a[64:67], a[48:51], v[236:239]\n"
    "v_exp_f32_e32 v205, v205\n"
    "v_mfma_f32_16x16x32_bf16 v[192:195], a[68:71], a[52:55], v[192:195]\n"
    "ds_read_b128 a[64:67], v240 offset:0\n"
    "v_cvt_pk_bf16_f32 v214, v204, v205\n"
    "v_mfma_f32_16x16x32_bf16 v[192:195], a[72:75], a[56:59], v[192:195]\n"
    "v_exp_f32_e32 v206, v206\n"
    "v_mfma_f32_16x16x32_bf16 v[192:195], a[76:79], a[60:63], v[192:195]\n"
    "ds_read_b128 a[68:71], v240 offset:64\n"
    "ds_read_b128 a[72:75], v240 offset:128\n"
    "v_mfma_f32_16x16x32_bf16 v[56:59], a[176:179], v[216:219], v[56:59]\n"
    "v_exp_f32_e32 v207, v207\n"
    "v_mfma_f32_16x16x32_bf16 v[56:59], a[180:183], v[220:223], v[56:59]\n"
    "ds_read_b128 a[76:79], v240 offset:192\n"
    "v_cvt_pk_bf16_f32 v215, v206, v207\n"
    "v_mfma_f32_16x16x32_bf16 v[60:63], a[184:187], v[216:219], v[60:63]\n"
    "s_add_u32 m0, s95, 1152\n"
    "v_exp_f32_e32 v192, v192\n"
    "v_mfma_f32_16x16x32_bf16 v[60:63], a[188:191], v[220:223], v[60:63]\n"
    "buffer_load_dwordx4 v243, s[44:47], 0 offen offset:2048 lds\n"
    "v_cvt_pk_bf16_f32 v148, v244, v245\n"
    "v_mfma_f32_16x16x32_bf16 v[196:199], a[80:83], a[48:51], v[236:239]\n"
    "v_exp_f32_e32 v193, v193\n"
    "v_mfma_f32_16x16x32_bf16 v[196:199], a[84:87], a[52:55], v[196:199]\n"
    "ds_read_b128 a[80:83], v240 offset:256\n"
    "v_cvt_pk_bf16_f32 v216, v192, v193\n"
    "v_mfma_f32_16x16x32_bf16 v[196:199], a[88:91], a[56:59], v[196:199]\n"
    "v_exp_f32_e32 v194, v194\n"
    "v_mfma_f32_16x16x32_bf16 v[196:199], a[92:95], a[60:63], v[196:199]\n"
    "ds_read_b128 a[84:87], v240 offset:320\n"
    "ds_read_b128 a[88:91], v240 offset:384\n"
    "v_mfma_f32_16x16x32_bf16 v[64:67], a[128:131], v[208:211], v[64:67]\n"
    "v_exp_f32_e32 v195, v195\n"
    "v_mfma_f32_16x16x32_bf16 v[64:67], a[132:135], v[212:215], v[64:67]\n"
    "ds_read_b128 a[92:95], v240 offset:448\n"
    "v_cvt_pk_bf16_f32 v217, v194, v195\n"
    "v_mfma_f32_16x16x32_bf16 v[68:71], a[136:139], v[208:211], v[68:71]\n"
    "v_exp_f32_e32 v196, v196\n"
    "v_mfma_f32_16x16x32_bf16 v[68:71], a[140:143], v[212:215], v[68:71]\n"
    "v_lshlrev_b32_e32 v244, 16, v149\n"
    "v_and_b32_e32 v245, s67, v149\n"
    "v_mfma_f32_16x16x32_bf16 v[200:203], a[96:99], a[48:51], v[236:239]\n"
    "s_add_u32 m0, s62, 50720\n"
    "v_exp_f32_e32 v197, v197\n"
    "v_mfma_f32_16x16x32_bf16 v[200:203], a[100:103], a[52:55], v[200:203]\n"
    "buffer_load_dwordx4 v242, s[36:39], 0 offen offset:1024 nt lds\n"
    "ds_read_b128 a[96:99], v240 offset:512\n"
    "v_mfma_f32_16x16x32_bf16 v[200:203], a[104:107], a[56:59], v[200:203]\n"
    "v_exp_f32_e32 v198, v198\n"
    "v_mfma_f32_16x16x32_bf16 v[200:203], a[108:111], a[60:63], v[200:203]\n"
    "ds_read_b128 a[100:103], v240 offset:576\n"
    "v_cvt_pk_bf16_f32 v218, v196, v197\n"
    "v_mfma_f32_16x16x32_bf16 v[72:75], a[144:147], v[208:211], v[72:75]\n"
    "v_exp_f32_e32 v199, v199\n"
    "v_mfma_f32_16x16x32_bf16 v[72:75], a[148:151], v[212:215], v[72:75]\n"
    "ds_read_b128 a[104:107], v240 offset:640\n"
    "ds_read_b128 a[108:111], v240 offset:704\n"
    "v_mfma_f32_16x16x32_bf16 v[76:79], a[152:155], v[208:211], v[76:79]\n"
    "v_exp_f32_e32 v200, v200\n"
    "v_mfma_f32_16x16x32_bf16 v[76:79], a[156:159], v[212:215], v[76:79]\n"
    "v_cvt_pk_bf16_f32 v219, v198, v199\n"
    "v_mul_f32_e32 v244, s64, v244\n"
    "v_smfmac_f32_16x16x64_bf16 a[200:203], a[208:211], v[208:215], v254\n"
    "v_mul_f32_e32 v245, s64, v245\n"
    "v_mfma_f32_16x16x32_bf16 v[204:207], a[112:115], a[48:51], v[236:239]\n"
    "v_exp_f32_e32 v201, v201\n"
    "v_mfma_f32_16x16x32_bf16 v[204:207], a[116:119], a[52:55], v[204:207]\n"
    "ds_read_b128 a[112:115], v240 offset:768\n"
    "v_cvt_pk_bf16_f32 v220, v200, v201\n"
    "v_mfma_f32_16x16x32_bf16 v[204:207], a[120:123], a[56:59], v[204:207]\n"
    "s_add_u32 m0, s62, 50752\n"
    "v_exp_f32_e32 v202, v202\n"
    "v_mfma_f32_16x16x32_bf16 v[204:207], a[124:127], a[60:63], v[204:207]\n"
    "buffer_load_dwordx4 v242, s[36:39], 0 offen offset:2048 nt lds\n"
    "ds_read_b128 a[116:119], v240 offset:832\n"
    "v_mfma_f32_16x16x32_bf16 v[80:83], a[160:163], v[208:211], v[80:83]\n"
    "v_exp_f32_e32 v203, v203\n"
    "v_mfma_f32_16x16x32_bf16 v[80:83], a[164:167], v[212:215], v[80:83]\n"
    "ds_read_b128 a[120:123], v240 offset:896\n"
    "v_cvt_pk_bf16_f32 v221, v202, v203\n"
    "v_mfma_f32_16x16x32_bf16 v[84:87], a[168:171], v[208:211], v[84:87]\n"
    "v_exp_f32_e32 v204, v204\n"
    "v_mfma_f32_16x16x32_bf16 v[84:87], a[172:175], v[212:215], v[84:87]\n"
    "s_waitcnt lgkmcnt(11)\n"
    "ds_read_b128 a[124:127], v240 offset:960\n"
    "v_cvt_pk_bf16_f32 v149, v244, v245\n"
    "v_mfma_f32_16x16x32_bf16 v[192:195], a[64:67], a[0:3], v[224:227]\n"
    "v_exp_f32_e32 v205, v205\n"
    "v_mfma_f32_16x16x32_bf16 v[192:195], a[68:71], a[4:7], v[192:195]\n"
    "v_cvt_pk_bf16_f32 v222, v204, v205\n"
    "v_lshlrev_b32_e32 v244, 16, v150\n"
    "v_mfma_f32_16x16x32_bf16 v[192:195], a[72:75], a[8:11], v[192:195]\n"
    "v_exp_f32_e32 v206, v206\n"
    "v_mfma_f32_16x16x32_bf16 v[192:195], a[76:79], a[12:15], v[192:195]\n"
    "v_and_b32_e32 v245, s67, v150\n"
    "v_mul_f32_e32 v244, s64, v244\n"
    "v_mfma_f32_16x16x32_bf16 v[88:91], a[176:179], v[208:211], v[88:91]\n"
    "v_exp_f32_e32 v207, v207\n"
    "v_mfma_f32_16x16x32_bf16 v[88:91], a[180:183], v[212:215], v[88:91]\n"
    "v_cvt_pk_bf16_f32 v223, v206, v207\n"
    "v_mul_f32_e32 v245, s64, v245\n"
    "v_mfma_f32_16x16x32_bf16 v[92:95], a[184:187], v[208:211], v[92:95]\n"
    "s_waitcnt lgkmcnt(8)\n"
    "v_exp_f32_e64 v192, v192\n"
    "v_mfma_f32_16x16x32_bf16 v[92:95], a[188:191], v[212:215], v[92:95]\n"
    "buffer_store_dwordx4 v[164:167], v250, s[48:51], 0 offen offset:256 sc0 sc1 nt\n"
    "v_cvt_pk_bf16_f32 v150, v244, v245\n"
    "v_lshlrev_b32_e32 v244, 16, v151\n"
    "v_and_b32_e32 v245, s67, v151\n"
    "v_mul_f32_e64 v244, s64, v244\n"
    "v_mfma_f32_16x16x32_bf16 v[196:199], a[80:83], a[0:3], v[224:227]\n"
    "v_exp_f32_e32 v193, v193\n"
    "v_mfma_f32_16x16x32_bf16 v[196:199], a[84:87], a[4:7], v[196:199]\n"
    "v_cvt_pk_bf16_f32 v208, v192, v193\n"
    "v_mul_f32_e32 v245, s64, v245\n"
    "v_mfma_f32_16x16x32_bf16 v[196:199], a[88:91], a[8:11], v[196:199]\n"
    "v_exp_f32_e32 v194, v194\n"
    "v_mfma_f32_16x16x32_bf16 v[196:199], a[92:95], a[12:15], v[196:199]\n"
    "v_cvt_pk_bf16_f32 v151, v244, v245\n"
    "v_mfma_f32_16x16x32_bf16 v[96:99], a[128:131], v[216:219], v[96:99]\n"
    "v_exp_f32_e32 v195, v195\n"
    "v_mfma_f32_16x16x32_bf16 v[96:99], a[132:135], v[220:223], v[96:99]\n"
    "ds_read_b64_tr_b16 a[128:129], v241 offset:0\n"
    "v_cvt_pk_bf16_f32 v209, v194, v195\n"
    "v_mfma_f32_16x16x32_bf16 v[100:103], a[136:139], v[216:219], v[100:103]\n"
    "v_exp_f32_e32 v196, v196\n"
    "v_mfma_f32_16x16x32_bf16 v[100:103], a[140:143], v[220:223], v[100:103]\n"
    "s_waitcnt lgkmcnt(5)\n"
    "ds_read_b64_tr_b16 a[130:131], v241 offset:256\n"
    "ds_read_b64_tr_b16 a[132:133], v241 offset:512\n"
    "v_mfma_f32_16x16x32_bf16 v[200:203], a[96:99], a[0:3], v[224:227]\n"
    "s_add_u32 m0, s62, 50784\n"
    "v_exp_f32_e32 v197, v197\n"
    "v_mfma_f32_16x16x32_bf16 v[200:203], a[100:103], a[4:7], v[200:203]\n"
    "buffer_load_dwordx4 v242, s[36:39], 0 offen offset:3072 nt lds\n"
    "ds_read_b64_tr_b16 a[134:135], v241 offset:768\n"
    "v_mfma_f32_16x16x32_bf16 v[200:203], a[104:107], a[8:11], v[200:203]\n"
    "s_addk_i32 s59, 16384\n"
    "v_exp_f32_e32 v198, v198\n"
    "v_mfma_f32_16x16x32_bf16 v[200:203], a[108:111], a[12:15], v[200:203]\n"
    "s_xor_b32 s71, s59, s63\n"
    "ds_read_b64_tr_b16 a[136:137], v241 offset:8\n"
    "ds_read_b64_tr_b16 a[138:139], v241 offset:264\n"
    "v_mfma_f32_16x16x32_bf16 v[104:107], a[144:147], v[216:219], v[104:107]\n"
    "s_add_u32 s36, s84, s71\n"
    "v_exp_f32_e32 v199, v199\n"
    "v_mfma_f32_16x16x32_bf16 v[104:107], a[148:151], v[220:223], v[104:107]\n"
    "s_addc_u32 s37, s85, 0\n"
    "ds_read_b64_tr_b16 a[140:141], v241 offset:520\n"
    "ds_read_b64_tr_b16 a[142:143], v241 offset:776\n"
    "v_mfma_f32_16x16x32_bf16 v[108:111], a[152:155], v[216:219], v[108:111]\n"
    "s_addk_i32 s58, -16384\n"
    "v_exp_f32_e32 v200, v200\n"
    "v_mfma_f32_16x16x32_bf16 v[108:111], a[156:159], v[220:223], v[108:111]\n"
    "s_max_i32 s38, s58, 0\n"
    "v_cvt_pk_bf16_f32 v210, v196, v197\n"
    "v_cvt_pk_bf16_f32 v211, v198, v199\n"
    "v_smfmac_f32_16x16x64_bf16 a[204:207], a[208:211], v[216:223], v254\n"
    "s_waitcnt lgkmcnt(8)\n"
    "ds_read_b64_tr_b16 a[144:145], v241 offset:16\n"
    "v_mfma_f32_16x16x32_bf16 v[204:207], a[112:115], a[0:3], v[224:227]\n"
    "v_exp_f32_e32 v201, v201\n"
    "v_mfma_f32_16x16x32_bf16 v[204:207], a[116:119], a[4:7], v[204:207]\n"
    "ds_read_b64_tr_b16 a[146:147], v241 offset:272\n"
    "ds_read_b64_tr_b16 a[148:149], v241 offset:528\n"
    "v_mfma_f32_16x16x32_bf16 v[204:207], a[120:123], a[8:11], v[204:207]\n"
    "s_add_u32 m0, s62, 118272\n"
    "v_exp_f32_e32 v202, v202\n"
    "v_mfma_f32_16x16x32_bf16 v[204:207], a[124:127], a[12:15], v[204:207]\n"
    "buffer_load_dwordx4 v242, s[40:43], 0 offen offset:0 nt lds\n"
    "ds_read_b64_tr_b16 a[150:151], v241 offset:784\n"
    "v_mfma_f32_16x16x32_bf16 v[112:115], a[160:163], v[216:219], v[112:115]\n"
    "v_exp_f32_e32 v203, v203\n"
    "v_mfma_f32_16x16x32_bf16 v[112:115], a[164:167], v[220:223], v[112:115]\n"
    "v_cvt_pk_bf16_f32 v212, v200, v201\n"
    "v_cvt_pk_bf16_f32 v213, v202, v203\n"
    "v_mfma_f32_16x16x32_bf16 v[116:119], a[168:171], v[216:219], v[116:119]\n"
    "v_exp_f32_e32 v204, v204\n"
    "v_mfma_f32_16x16x32_bf16 v[116:119], a[172:175], v[220:223], v[116:119]\n"
    "ds_read_b64_tr_b16 a[152:153], v241 offset:24\n"
    "ds_read_b64_tr_b16 a[154:155], v241 offset:280\n"
    "v_mfma_f32_16x16x32_bf16 v[192:195], a[64:67], a[16:19], v[228:231]\n"
    "v_exp_f32_e32 v205, v205\n"
    "v_mfma_f32_16x16x32_bf16 v[192:195], a[68:71], a[20:23], v[192:195]\n"
    "ds_read_b64_tr_b16 a[156:157], v241 offset:536\n"
    "v_cvt_pk_bf16_f32 v214, v204, v205\n"
    "v_mfma_f32_16x16x32_bf16 v[192:195], a[72:75], a[24:27], v[192:195]\n"
    "v_exp_f32_e32 v206, v206\n"
    "v_mfma_f32_16x16x32_bf16 v[192:195], a[76:79], a[28:31], v[192:195]\n"
    "ds_read_b64_tr_b16 a[158:159], v241 offset:792\n"
    "ds_read_b64_tr_b16 a[160:161], v241 offset:128\n"
    "v_mfma_f32_16x16x32_bf16 v[120:123], a[176:179], v[216:219], v[120:123]\n"
    "v_exp_f32_e32 v207, v207\n"
    "v_mfma_f32_16x16x32_bf16 v[120:123], a[180:183], v[220:223], v[120:123]\n"
    "ds_read_b64_tr_b16 a[162:163], v241 offset:384\n"
    "v_cvt_pk_bf16_f32 v215, v206, v207\n"
    "v_mfma_f32_16x16x32_bf16 v[124:127], a[184:187], v[216:219], v[124:127]\n"
    "s_add_u32 m0, s62, 118280\n"
    "v_exp_f32_e32 v192, v192\n"
    "v_mfma_f32_16x16x32_bf16 v[124:127], a[188:191], v[220:223], v[124:127]\n"
    "buffer_load_dwordx4 v242, s[40:43], 0 offen offset:1024 nt lds\n"
    "ds_read_b64_tr_b16 a[164:165], v241 offset:640\n"
    "v_mfma_f32_16x16x32_bf16 v[196:199], a[80:83], a[16:19], v[228:231]\n"
    "v_exp_f32_e32 v193, v193\n"
    "v_mfma_f32_16x16x32_bf16 v[196:199], a[84:87], a[20:23], v[196:199]\n"
    "ds_read_b64_tr_b16 a[166:167], v241 offset:896\n"
    "ds_read_b64_tr_b16 a[168:169], v241 offset:136\n"
    "v_mfma_f32_16x16x32_bf16 v[196:199], a[88:91], a[24:27], v[196:199]\n"
    "v_exp_f32_e32 v194, v194\n"
    "v_mfma_f32_16x16x32_bf16 v[196:199], a[92:95], a[28:31], v[196:199]\n"
    "s_waitcnt lgkmcnt(14)\n"
    "ds_read_b64_tr_b16 a[170:171], v241 offset:392\n"
    "ds_read_b64_tr_b16 a[172:173], v241 offset:648\n"
    "v_mfma_f32_16x16x32_bf16 v[0:3], a[128:131], v[208:211], v[0:3]\n"
    "s_waitcnt lgkmcnt(14)\n"
    "v_exp_f32_e32 v195, v195\n"
    "v_mfma_f32_16x16x32_bf16 v[0:3], a[132:135], v[212:215], v[0:3]\n"
    "s_waitcnt vmcnt(18)\n"
    "ds_read_b64_tr_b16 a[174:175], v241 offset:904\n"
    "ds_read_b128 v[152:155], v255 offset:0\n"
    "v_mfma_f32_16x16x32_bf16 v[4:7], a[136:139], v[208:211], v[4:7]\n"
    "v_exp_f32_e32 v196, v196\n"
    "v_mfma_f32_16x16x32_bf16 v[4:7], a[140:143], v[212:215], v[4:7]\n"
    "ds_read_b128 v[156:159], v255 offset:64\n"
    "ds_read_b64_tr_b16 a[176:177], v241 offset:144\n"
    "v_mfma_f32_16x16x32_bf16 v[200:203], a[96:99], a[16:19], v[228:231]\n"
    "s_add_u32 m0, s62, 118288\n"
    "v_exp_f32_e32 v197, v197\n"
    "v_mfma_f32_16x16x32_bf16 v[200:203], a[100:103], a[20:23], v[200:203]\n"
    "buffer_load_dwordx4 v242, s[40:43], 0 offen offset:2048 nt lds\n"
    "ds_read_b64_tr_b16 a[178:179], v241 offset:400\n"
    "v_mfma_f32_16x16x32_bf16 v[200:203], a[104:107], a[24:27], v[200:203]\n"
    "v_exp_f32_e32 v198, v198\n"
    "v_mfma_f32_16x16x32_bf16 v[200:203], a[108:111], a[28:31], v[200:203]\n"
    "s_waitcnt lgkmcnt(14)\n"
    "ds_read_b64_tr_b16 a[180:181], v241 offset:656\n"
    "ds_read_b64_tr_b16 a[182:183], v241 offset:912\n"
    "v_mfma_f32_16x16x32_bf16 v[8:11], a[144:147], v[208:211], v[8:11]\n"
    "s_waitcnt lgkmcnt(14)\n"
    "v_exp_f32_e32 v199, v199\n"
    "v_mfma_f32_16x16x32_bf16 v[8:11], a[148:151], v[212:215], v[8:11]\n"
    "s_waitcnt lgkmcnt(4)\n"
    "ds_read_b64_tr_b16 a[184:185], v241 offset:152\n"
    "ds_read_b64_tr_b16 a[186:187], v241 offset:408\n"
    "v_mfma_f32_16x16x32_bf16 v[12:15], a[152:155], v[208:211], v[12:15]\n"
    "v_exp_f32_e32 v200, v200\n"
    "v_mfma_f32_16x16x32_bf16 v[12:15], a[156:159], v[212:215], v[12:15]\n"
    "ds_read_b64_tr_b16 a[188:189], v241 offset:664\n"
    "ds_read_b64_tr_b16 a[190:191], v241 offset:920\n"
    "v_smfmac_f32_16x16x64_bf16 a[192:195], a[208:211], v[208:215], v254\n"
    "v_cvt_pk_bf16_f32 v216, v192, v193\n"
    "v_mfma_f32_16x16x32_bf16 v[204:207], a[112:115], a[16:19], v[228:231]\n"
    "s_add_u32 m0, s62, 118296\n"
    "v_exp_f32_e32 v201, v201\n"
    "v_mfma_f32_16x16x32_bf16 v[204:207], a[116:119], a[20:23], v[204:207]\n"
    "s_waitcnt vmcnt(23)\n"
    "ds_append v253\n"
    "v_cvt_pk_bf16_f32 v217, v194, v195\n"
    "v_mfma_f32_16x16x32_bf16 v[204:207], a[120:123], a[24:27], v[204:207]\n"
    "s_waitcnt lgkmcnt(14)\n"
    "v_exp_f32_e32 v202, v202\n"
    "v_mfma_f32_16x16x32_bf16 v[204:207], a[124:127], a[28:31], v[204:207]\n"
    "buffer_load_dwordx4 v242, s[40:43], 0 offen offset:3072 nt lds\n"
    "v_cvt_pk_bf16_f32 v218, v196, v197\n"
    "v_mfma_f32_16x16x32_bf16 v[16:19], a[160:163], v[208:211], v[16:19]\n"
    "s_add_u32 s40, s60, s71\n"
    "v_exp_f32_e32 v203, v203\n"
    "v_mfma_f32_16x16x32_bf16 v[16:19], a[164:167], v[212:215], v[16:19]\n"
    "s_waitcnt lgkmcnt(11)\n"
    "v_cvt_pk_bf16_f32 v219, v198, v199\n"
    "v_cvt_pk_bf16_f32 v220, v200, v201\n"
    "v_mfma_f32_16x16x32_bf16 v[20:23], a[168:171], v[208:211], v[20:23]\n"
    "s_addc_u32 s41, s61, 0\n"
    "v_exp_f32_e32 v204, v204\n"
    "v_mfma_f32_16x16x32_bf16 v[20:23], a[172:175], v[212:215], v[20:23]\n"
    "s_max_i32 s42, s58, 0\n"
    "v_cvt_pk_bf16_f32 v221, v202, v203\n"
    "v_lshlrev_b32_e32 v244, 16, v152\n"
    "v_mfma_f32_16x16x32_bf16 v[192:195], a[64:67], a[32:35], v[232:235]\n"
    "v_exp_f32_e32 v205, v205\n"
    "v_mfma_f32_16x16x32_bf16 v[192:195], a[68:71], a[36:39], v[192:195]\n"
    "v_cvt_pk_bf16_f32 v222, v204, v205\n"
    "v_and_b32_e32 v245, s67, v152\n"
    "v_mfma_f32_16x16x32_bf16 v[192:195], a[72:75], a[40:43], v[192:195]\n"
    "v_exp_f32_e32 v206, v206\n"
    "v_mfma_f32_16x16x32_bf16 v[192:195], a[76:79], a[44:47], v[192:195]\n"
    "s_waitcnt lgkmcnt(5)\n"
    "v_mul_f32_e32 v244, s64, v244\n"
    "v_mul_f32_e32 v245, s64, v245\n"
    "v_mfma_f32_16x16x32_bf16 v[24:27], a[176:179], v[208:211], v[24:27]\n"
    "v_exp_f32_e32 v207, v207\n"
    "v_mfma_f32_16x16x32_bf16 v[24:27], a[180:183], v[212:215], v[24:27]\n"
    "s_waitcnt lgkmcnt(1)\n"
    "v_cvt_pk_bf16_f32 v223, v206, v207\n"
    "v_cvt_pk_bf16_f32 v152, v244, v245\n"
    "LBODY_%=:\n"
    "v_mfma_f32_16x16x32_bf16 v[28:31], a[184:187], v[208:211], v[28:31]\n"
    "LW10_%=:\n"
    "s_cbranch_execz LWR_%=\n"
    "v_exp_f32_e32 v192, v192\n"
    "v_mfma_f32_16x16x32_bf16 v[28:31], a[188:191], v[212:215], v[28:31]\n"
    "ds_read_b32 v253, v251\n"
    "v_lshlrev_b32_e32 v244, 16, v153\n"
    "v_mfma_f32_16x16x32_bf16 v[196:199], a[80:83], a[32:35], v[232:235]\n"
    "v_exp_f32_e32 v193, v193\n"
    "v_mfma_f32_16x16x32_bf16 v[196:199], a[84:87], a[36:39], v[196:199]\n"
    "buffer_store_dwordx4 v[168:171], v250, s[48:51], 0 offen offset:128 sc0 sc1 nt\n"
    "v_cvt_pk_bf16_f32 v208, v192, v193\n"
    "v_and_b32_e32 v245, s67, v153\n"
    "v_mul_f32_e32 v244, s64, v244\n"
    "v_mul_f32_e64 v245, s64, v245\n"
    "v_mfma_f32_16x16x32_bf16 v[196:199], a[88:91], a[40:43], v[196:199]\n"
    "v_exp_f32_e32 v194, v194\n"
    "v_mfma_f32_16x16x32_bf16 v[196:199], a[92:95], a[44:47], v[196:199]\n"
    "v_cvt_pk_bf16_f32 v153, v244, v245\n"
    "v_lshlrev_b32_e32 v244, 16, v154\n"
    "v_mfma_f32_16x16x32_bf16 v[32:35], a[128:131], v[216:219], v[32:35]\n"
    "v_exp_f32_e32 v195, v195\n"
    "v_mfma_f32_16x16x32_bf16 v[32:35], a[132:135], v[220:223], v[32:35]\n"
    "s_waitcnt lgkmcnt(0)\n"
    "v_cmp_le_u32_e32 vcc, s76, v253\n"
    "v_cvt_pk_bf16_f32 v209, v194, v195\n"
    "v_mfma_f32_16x16x32_bf16 v[36:39], a[136:139], v[216:219], v[36:39]\n"
    "LW11_%=:\n"
    "s_cbranch_execz LWR_%=\n"
    "v_exp_f32_e32 v196, v196\n"
    "v_mfma_f32_16x16x32_bf16 v[36:39], a[140:143], v[220:223], v[36:39]\n"
    "v_and_b32_e32 v245, s67, v154\n"
    "v_mul_f32_e32 v244, s64, v244\n"
    "v_mfma_f32_16x16x32_bf16 v[200:203], a[96:99], a[32:35], v[232:235]\n"
    "s_cbranch_vccz LSPIN7_%=\n"
    "LRET7_%=:\n"
    "v_exp_f32_e32 v197, v197\n"
    "v_mfma_f32_16x16x32_bf16 v[200:203], a[100:103], a[36:39], v[200:203]\n"
    "s_addk_i32 s76, 256\n"
    "v_cvt_pk_bf16_f32 v210, v196, v197\n"
    "v_mul_f32_e32 v245, s64, v245\n"
    "v_mfma_f32_16x16x32_bf16 v[200:203], a[104:107], a[40:43], v[200:203]\n"
    "s_add_u32 m0, s62, 0\n"
    "v_exp_f32_e32 v198, v198\n"
    "v_mfma_f32_16x16x32_bf16 v[200:203], a[108:111], a[44:47], v[200:203]\n"
    "buffer_load_dwordx4 v242, s[36:39], 0 offen offset:0 nt lds\n"
    "v_cvt_pk_bf16_f32 v154, v244, v245\n"
    "v_mfma_f32_16x16x32_bf16 v[40:43], a[144:147], v[216:219], v[40:43]\n"
    "v_exp_f32_e32 v199, v199\n"
    "v_mfma_f32_16x16x32_bf16 v[40:43], a[148:151], v[220:223], v[40:43]\n"
    "v_cvt_pk_bf16_f32 v211, v198, v199\n"
    "v_lshlrev_b32_e32 v244, 16, v155\n"
    "v_mfma_f32_16x16x32_bf16 v[44:47], a[152:155], v[216:219], v[44:47]\n"
    "v_exp_f32_e32 v200, v200\n"
    "v_mfma_f32_16x16x32_bf16 v[44:47], a[156:159], v[220:223], v[44:47]\n"
    "v_and_b32_e32 v245, s67, v155\n"
    "v_mul_f32_e32 v244, s64, v244\n"
    "v_smfmac_f32_16x16x64_bf16 a[196:199], a[208:211], v[216:223], v254\n"
    "v_mul_f32_e32 v245, s64, v245\n"
    "v_mfma_f32_16x16x32_bf16 v[204:207], a[112:115], a[32:35], v[232:235]\n"
    "v_exp_f32_e32 v201, v201\n"
    "v_mfma_f32_16x16x32_bf16 v[204:207], a[116:119], a[36:39], v[204:207]\n"
    "v_cvt_pk_bf16_f32 v212, v200, v201\n"
    "v_cvt_pk_bf16_f32 v155, v244, v245\n"
    "v_mfma_f32_16x16x32_bf16 v[204:207], a[120:123], a[40:43], v[204:207]\n"
    "v_exp_f32_e32 v202, v202\n"
    "v_mfma_f32_16x16x32_bf16 v[204:207], a[124:127], a[44:47], v[204:207]\n"
    "v_lshlrev_b32_e32 v244, 16, v156\n"
    "v_and_b32_e32 v245, s67, v156\n"
    "v_mfma_f32_16x16x32_bf16 v[48:51], a[160:163], v[216:219], v[48:51]\n"
    "s_sub_u32 m0, s95, 128\n"
    "v_exp_f32_e32 v203, v203\n"
    "v_mfma_f32_16x16x32_bf16 v[48:51], a[164:167], v[220:223], v[48:51]\n"
    "buffer_load_dwordx4 v243, s[44:47], 0 offen offset:128 lds\n"
    "v_cvt_pk_bf16_f32 v213, v202, v203\n"
    "v_mfma_f32_16x16x32_bf16 v[52:55], a[168:171], v[216:219], v[52:55]\n"
    "v_exp_f32_e32 v204, v204\n"
    "v_mfma_f32_16x16x32_bf16 v[52:55], a[172:175], v[220:223], v[52:55]\n"
    "v_mul_f32_e32 v244, s64, v244\n"
    "v_mul_f32_e32 v245, s64, v245\n"
    "v_mfma_f32_16x16x32_bf16 v[192:195], a[64:67], a[48:51], v[236:239]\n"
    "v_exp_f32_e32 v205, v205\n"
    "v_mfma_f32_16x16x32_bf16 v[192:195], a[68:71], a[52:55], v[192:195]\n"
    "ds_read_b128 a[64:67], v240 offset:16896\n"
    "v_cvt_pk_bf16_f32 v214, v204, v205\n"
    "v_mfma_f32_16x16x32_bf16 v[192:195], a[72:75], a[56:59], v[192:195]\n"
    "v_exp_f32_e32 v206, v206\n"
    "v_mfma_f32_16x16x32_bf16 v[192:195], a[76:79], a[60:63], v[192:195]\n"
    "ds_read_b128 a[68:71], v240 offset:16960\n"
    "ds_read_b128 a[72:75], v240 offset:17024\n"
    "v_mfma_f32_16x16x32_bf16 v[56:59], a[176:179], v[216:219], v[56:59]\n"
    "v_exp_f32_e32 v207, v207\n"
    "v_mfma_f32_16x16x32_bf16 v[56:59], a[180:183], v[220:223], v[56:59]\n"
    "ds_read_b128 a[76:79], v240 offset:17088\n"
    "v_cvt_pk_bf16_f32 v215, v206, v207\n"
    "v_mfma_f32_16x16x32_bf16 v[60:63], a[184:187], v[216:219], v[60:63]\n"
    "s_sub_u32 m0, s95, 1088\n"
    "v_exp_f32_e32 v192, v192\n"
    "v_mfma_f32_16x16x32_bf16 v[60:63], a[188:191], v[220:223], v[60:63]\n"
    "buffer_load_dwordx4 v243, s[44:47], 0 offen offset:2176 lds\n"
    "v_cvt_pk_bf16_f32 v156, v244, v245\n"
    "v_mfma_f32_16x16x32_bf16 v[196:199], a[80:83], a[48:51], v[236:239]\n"
    "s_add_u32 s44, s44, 16384\n"
    "v_exp_f32_e32 v193, v193\n"
    "v_mfma_f32_16x16x32_bf16 v[196:199], a[84:87], a[52:55], v[196:199]\n"
    "s_addc_u32 s45, s45, 0\n"
    "ds_read_b128 a[80:83], v240 offset:17152\n"
    "v_cvt_pk_bf16_f32 v216, v192, v193\n"
    "v_mfma_f32_16x16x32_bf16 v[196:199], a[88:91], a[56:59], v[196:199]\n"
    "s_mul_i32 s46, s55, s56\n"
    "v_exp_f32_e32 v194, v194\n"
    "v_mfma_f32_16x16x32_bf16 v[196:199], a[92:95], a[60:63], v[196:199]\n"
    "ds_read_b128 a[84:87], v240 offset:17216\n"
    "ds_read_b128 a[88:91], v240 offset:17280\n"
    "v_mfma_f32_16x16x32_bf16 v[64:67], a[128:131], v[208:211], v[64:67]\n"
    "v_exp_f32_e32 v195, v195\n"
    "v_mfma_f32_16x16x32_bf16 v[64:67], a[132:135], v[212:215], v[64:67]\n"
    "ds_read_b128 a[92:95], v240 offset:17344\n"
    "v_cvt_pk_bf16_f32 v217, v194, v195\n"
    "v_mfma_f32_16x16x32_bf16 v[68:71], a[136:139], v[208:211], v[68:71]\n"
    "v_exp_f32_e32 v196, v196\n"
    "v_mfma_f32_16x16x32_bf16 v[68:71], a[140:143], v[212:215], v[68:71]\n"
    "v_lshlrev_b32_e32 v244, 16, v157\n"
    "v_and_b32_e32 v245, s67, v157\n"
    "v_mfma_f32_16x16x32_bf16 v[200:203], a[96:99], a[48:51], v[236:239]\n"
    "s_add_u32 m0, s62, 32\n"
    "v_exp_f32_e32 v197, v197\n"
    "v_mfma_f32_16x16x32_bf16 v[200:203], a[100:103], a[52:55], v[200:203]\n"
    "buffer_load_dwordx4 v242, s[36:39], 0 offen offset:1024 nt lds\n"
    "ds_read_b128 a[96:99], v240 offset:17408\n"
    "v_mfma_f32_16x16x32_bf16 v[200:203], a[104:107], a[56:59], v[200:203]\n"
    "v_exp_f32_e32 v198, v198\n"
    "v_mfma_f32_16x16x32_bf16 v[200:203], a[108:111], a[60:63], v[200:203]\n"
    "ds_read_b128 a[100:103], v240 offset:17472\n"
    "v_cvt_pk_bf16_f32 v218, v196, v197\n"
    "v_mfma_f32_16x16x32_bf16 v[72:75], a[144:147], v[208:211], v[72:75]\n"
    "v_exp_f32_e32 v199, v199\n"
    "v_mfma_f32_16x16x32_bf16 v[72:75], a[148:151], v[212:215], v[72:75]\n"
    "ds_read_b128 a[104:107], v240 offset:17536\n"
    "ds_read_b128 a[108:111], v240 offset:17600\n"
    "v_mfma_f32_16x16x32_bf16 v[76:79], a[152:155], v[208:211], v[76:79]\n"
    "v_exp_f32_e32 v200, v200\n"
    "v_mfma_f32_16x16x32_bf16 v[76:79], a[156:159], v[212:215], v[76:79]\n"
    "v_cvt_pk_bf16_f32 v219, v198, v199\n"
    "v_mul_f32_e32 v244, s64, v244\n"
    "v_smfmac_f32_16x16x64_bf16 a[200:203], a[208:211], v[208:215], v254\n"
    "v_mul_f32_e32 v245, s64, v245\n"
    "v_mfma_f32_16x16x32_bf16 v[204:207], a[112:115], a[48:51], v[236:239]\n"
    "v_exp_f32_e32 v201, v201\n"
    "v_mfma_f32_16x16x32_bf16 v[204:207], a[116:119], a[52:55], v[204:207]\n"
    "ds_read_b128 a[112:115], v240 offset:17664\n"
    "v_cvt_pk_bf16_f32 v220, v200, v201\n"
    "v_mfma_f32_16x16x32_bf16 v[204:207], a[120:123], a[56:59], v[204:207]\n"
    "s_add_u32 m0, s62, 64\n"
    "v_exp_f32_e32 v202, v202\n"
    "v_mfma_f32_16x16x32_bf16 v[204:207], a[124:127], a[60:63], v[204:207]\n"
    "buffer_load_dwordx4 v242, s[36:39], 0 offen offset:2048 nt lds\n"
    "ds_read_b128 a[116:119], v240 offset:17728\n"
    "v_mfma_f32_16x16x32_bf16 v[80:83], a[160:163], v[208:211], v[80:83]\n"
    "v_exp_f32_e32 v203, v203\n"
    "v_mfma_f32_16x16x32_bf16 v[80:83], a[164:167], v[212:215], v[80:83]\n"
    "ds_read_b128 a[120:123], v240 offset:17792\n"
    "v_cvt_pk_bf16_f32 v221, v202, v203\n"
    "v_mfma_f32_16x16x32_bf16 v[84:87], a[168:171], v[208:211], v[84:87]\n"
    "v_exp_f32_e32 v204, v204\n"
    "v_mfma_f32_16x16x32_bf16 v[84:87], a[172:175], v[212:215], v[84:87]\n"
    "s_waitcnt lgkmcnt(11)\n"
    "ds_read_b128 a[124:127], v240 offset:17856\n"
    "v_cvt_pk_bf16_f32 v157, v244, v245\n"
    "v_mfma_f32_16x16x32_bf16 v[192:195], a[64:67], a[0:3], v[224:227]\n"
    "v_exp_f32_e32 v205, v205\n"
    "v_mfma_f32_16x16x32_bf16 v[192:195], a[68:71], a[4:7], v[192:195]\n"
    "v_cvt_pk_bf16_f32 v222, v204, v205\n"
    "v_lshlrev_b32_e32 v244, 16, v158\n"
    "v_mfma_f32_16x16x32_bf16 v[192:195], a[72:75], a[8:11], v[192:195]\n"
    "v_exp_f32_e32 v206, v206\n"
    "v_mfma_f32_16x16x32_bf16 v[192:195], a[76:79], a[12:15], v[192:195]\n"
    "v_and_b32_e32 v245, s67, v158\n"
    "v_mul_f32_e32 v244, s64, v244\n"
    "v_mfma_f32_16x16x32_bf16 v[88:91], a[176:179], v[208:211], v[88:91]\n"
    "v_exp_f32_e32 v207, v207\n"
    "v_mfma_f32_16x16x32_bf16 v[88:91], a[180:183], v[212:215], v[88:91]\n"
    "v_cvt_pk_bf16_f32 v223, v206, v207\n"
    "v_mul_f32_e32 v245, s64, v245\n"
    "v_mfma_f32_16x16x32_bf16 v[92:95], a[184:187], v[208:211], v[92:95]\n"
    "s_waitcnt lgkmcnt(8)\n"
    "v_exp_f32_e64 v192, v192\n"
    "v_mfma_f32_16x16x32_bf16 v[92:95], a[188:191], v[212:215], v[92:95]\n"
    "buffer_store_dwordx4 v[172:175], v250, s[48:51], 0 offen offset:384 sc0 sc1 nt\n"
    "v_cvt_pk_bf16_f32 v158, v244, v245\n"
    "v_lshlrev_b32_e32 v244, 16, v159\n"
    "v_and_b32_e32 v245, s67, v159\n"
    "v_mul_f32_e64 v244, s64, v244\n"
    "v_mfma_f32_16x16x32_bf16 v[196:199], a[80:83], a[0:3], v[224:227]\n"
    "s_add_u32 s48, s48, 16384\n"
    "v_exp_f32_e32 v193, v193\n"
    "v_mfma_f32_16x16x32_bf16 v[196:199], a[84:87], a[4:7], v[196:199]\n"
    "s_addc_u32 s49, s49, 0\n"
    "v_cvt_pk_bf16_f32 v208, v192, v193\n"
    "v_mul_f32_e32 v245, s64, v245\n"
    "v_mfma_f32_16x16x32_bf16 v[196:199], a[88:91], a[8:11], v[196:199]\n"
    "s_mul_i32 s50, s55, s57\n"
    "v_exp_f32_e32 v194, v194\n"
    "v_mfma_f32_16x16x32_bf16 v[196:199], a[92:95], a[12:15], v[196:199]\n"
    "v_cvt_pk_bf16_f32 v159, v244, v245\n"
    "v_mfma_f32_16x16x32_bf16 v[96:99], a[128:131], v[216:219], v[96:99]\n"
    "v_exp_f32_e32 v195, v195\n"
    "v_mfma_f32_16x16x32_bf16 v[96:99], a[132:135], v[220:223], v[96:99]\n"
    "ds_read_b64_tr_b16 a[128:129], v241 offset:16896\n"
    "v_cvt_pk_bf16_f32 v209, v194, v195\n"
    "v_mfma_f32_16x16x32_bf16 v[100:103], a[136:139], v[216:219], v[100:103]\n"
    "v_exp_f32_e32 v196, v196\n"
    "v_mfma_f32_16x16x32_bf16 v[100:103], a[140:143], v[220:223], v[100:103]\n"
    "s_waitcnt lgkmcnt(5)\n"
    "ds_read_b64_tr_b16 a[130:131], v241 offset:17152\n"
    "ds_read_b64_tr_b16 a[132:133], v241 offset:17408\n"
    "v_mfma_f32_16x16x32_bf16 v[200:203], a[96:99], a[0:3], v[224:227]\n"
    "s_add_u32 m0, s62, 96\n"
    "v_exp_f32_e32 v197, v197\n"
    "v_mfma_f32_16x16x32_bf16 v[200:203], a[100:103], a[4:7], v[200:203]\n"
    "buffer_load_dwordx4 v242, s[36:39], 0 offen offset:3072 nt lds\n"
    "ds_read_b64_tr_b16 a[134:135], v241 offset:17664\n"
    "v_mfma_f32_16x16x32_bf16 v[200:203], a[104:107], a[8:11], v[200:203]\n"
    "s_addk_i32 s59, 16384\n"
    "v_exp_f32_e32 v198, v198\n"
    "v_mfma_f32_16x16x32_bf16 v[200:203], a[108:111], a[12:15], v[200:203]\n"
    "s_xor_b32 s71, s59, s63\n"
    "ds_read_b64_tr_b16 a[136:137], v241 offset:16904\n"
    "ds_read_b64_tr_b16 a[138:139], v241 offset:17160\n"
    "v_mfma_f32_16x16x32_bf16 v[104:107], a[144:147], v[216:219], v[104:107]\n"
    "s_add_u32 s36, s84, s71\n"
    "v_exp_f32_e32 v199, v199\n"
    "v_mfma_f32_16x16x32_bf16 v[104:107], a[148:151], v[220:223], v[104:107]\n"
    "s_addc_u32 s37, s85, 0\n"
    "ds_read_b64_tr_b16 a[140:141], v241 offset:17416\n"
    "ds_read_b64_tr_b16 a[142:143], v241 offset:17672\n"
    "v_mfma_f32_16x16x32_bf16 v[108:111], a[152:155], v[216:219], v[108:111]\n"
    "s_addk_i32 s58, -16384\n"
    "v_exp_f32_e32 v200, v200\n"
    "v_mfma_f32_16x16x32_bf16 v[108:111], a[156:159], v[220:223], v[108:111]\n"
    "s_max_i32 s38, s58, 0\n"
    "v_cvt_pk_bf16_f32 v210, v196, v197\n"
    "v_cvt_pk_bf16_f32 v211, v198, v199\n"
    "v_smfmac_f32_16x16x64_bf16 a[204:207], a[208:211], v[216:223], v254\n"
    "s_waitcnt lgkmcnt(8)\n"
    "ds_read_b64_tr_b16 a[144:145], v241 offset:16912\n"
    "v_mfma_f32_16x16x32_bf16 v[204:207], a[112:115], a[0:3], v[224:227]\n"
    "v_exp_f32_e32 v201, v201\n"
    "v_mfma_f32_16x16x32_bf16 v[204:207], a[116:119], a[4:7], v[204:207]\n"
    "ds_read_b64_tr_b16 a[146:147], v241 offset:17168\n"
    "ds_read_b64_tr_b16 a[148:149], v241 offset:17424\n"
    "v_mfma_f32_16x16x32_bf16 v[204:207], a[120:123], a[8:11], v[204:207]\n"
    "s_add_u32 m0, s62, 67584\n"
    "v_exp_f32_e32 v202, v202\n"
    "v_mfma_f32_16x16x32_bf16 v[204:207], a[124:127], a[12:15], v[204:207]\n"
    "buffer_load_dwordx4 v242, s[40:43], 0 offen offset:0 nt lds\n"
    "ds_read_b64_tr_b16 a[150:151], v241 offset:17680\n"
    "v_mfma_f32_16x16x32_bf16 v[112:115], a[160:163], v[216:219], v[112:115]\n"
    "v_exp_f32_e32 v203, v203\n"
    "v_mfma_f32_16x16x32_bf16 v[112:115], a[164:167], v[220:223], v[112:115]\n"
    "v_cvt_pk_bf16_f32 v212, v200, v201\n"
    "v_cvt_pk_bf16_f32 v213, v202, v203\n"
    "v_mfma_f32_16x16x32_bf16 v[116:119], a[168:171], v[216:219], v[116:119]\n"
    "v_exp_f32_e32 v204, v204\n"
    "v_mfma_f32_16x16x32_bf16 v[116:119], a[172:175], v[220:223], v[116:119]\n"
    "ds_read_b64_tr_b16 a[152:153], v241 offset:16920\n"
    "ds_read_b64_tr_b16 a[154:155], v241 offset:17176\n"
    "v_mfma_f32_16x16x32_bf16 v[192:195], a[64:67], a[16:19], v[228:231]\n"
    "v_exp_f32_e32 v205, v205\n"
    "v_mfma_f32_16x16x32_bf16 v[192:195], a[68:71], a[20:23], v[192:195]\n"
    "ds_read_b64_tr_b16 a[156:157], v241 offset:17432\n"
    "v_cvt_pk_bf16_f32 v214, v204, v205\n"
    "v_mfma_f32_16x16x32_bf16 v[192:195], a[72:75], a[24:27], v[192:195]\n"
    "v_exp_f32_e32 v206, v206\n"
    "v_mfma_f32_16x16x32_bf16 v[192:195], a[76:79], a[28:31], v[192:195]\n"
    "ds_read_b64_tr_b16 a[158:159], v241 offset:17688\n"
    "ds_read_b64_tr_b16 a[160:161], v241 offset:17024\n"
    "v_mfma_f32_16x16x32_bf16 v[120:123], a[176:179], v[216:219], v[120:123]\n"
    "v_exp_f32_e32 v207, v207\n"
    "v_mfma_f32_16x16x32_bf16 v[120:123], a[180:183], v[220:223], v[120:123]\n"
    "ds_read_b64_tr_b16 a[162:163], v241 offset:17280\n"
    "v_cvt_pk_bf16_f32 v215, v206, v207\n"
    "v_mfma_f32_16x16x32_bf16 v[124:127], a[184:187], v[216:219], v[124:127]\n"
    "s_add_u32 m0, s62, 67592\n"
    "v_exp_f32_e32 v192, v192\n"
    "v_mfma_f32_16x16x32_bf16 v[124:127], a[188:191], v[220:223], v[124:127]\n"
    "buffer_load_dwordx4 v242, s[40:43], 0 offen offset:1024 nt lds\n"
    "ds_read_b64_tr_b16 a[164:165], v241 offset:17536\n"
    "v_mfma_f32_16x16x32_bf16 v[196:199], a[80:83], a[16:19], v[228:231]\n"
    "v_exp_f32_e32 v193, v193\n"
    "v_mfma_f32_16x16x32_bf16 v[196:199], a[84:87], a[20:23], v[196:199]\n"
    "ds_read_b64_tr_b16 a[166:167], v241 offset:17792\n"
    "ds_read_b64_tr_b16 a[168:169], v241 offset:17032\n"
    "v_mfma_f32_16x16x32_bf16 v[196:199], a[88:91], a[24:27], v[196:199]\n"
    "v_exp_f32_e32 v194, v194\n"
    "v_mfma_f32_16x16x32_bf16 v[196:199], a[92:95], a[28:31], v[196:199]\n"
    "s_waitcnt lgkmcnt(14)\n"
    "ds_read_b64_tr_b16 a[170:171], v241 offset:17288\n"
    "ds_read_b64_tr_b16 a[172:173], v241 offset:17544\n"
    "v_mfma_f32_16x16x32_bf16 v[0:3], a[128:131], v[208:211], v[0:3]\n"
    "s_waitcnt lgkmcnt(14)\n"
    "v_exp_f32_e32 v195, v195\n"
    "v_mfma_f32_16x16x32_bf16 v[0:3], a[132:135], v[212:215], v[0:3]\n"
    "s_waitcnt vmcnt(17)\n"
    "ds_read_b64_tr_b16 a[174:175], v241 offset:17800\n"
    "ds_read_b128 v[160:163], v255 offset:2112\n"
    "v_mfma_f32_16x16x32_bf16 v[4:7], a[136:139], v[208:211], v[4:7]\n"
    "v_exp_f32_e32 v196, v196\n"
    "v_mfma_f32_16x16x32_bf16 v[4:7], a[140:143], v[212:215], v[4:7]\n"
    "ds_read_b128 v[164:167], v255 offset:2176\n"
    "ds_read_b64_tr_b16 a[176:177], v241 offset:17040\n"
    "v_mfma_f32_16x16x32_bf16 v[200:203], a[96:99], a[16:19], v[228:231]\n"
    "s_add_u32 m0, s62, 67600\n"
    "v_exp_f32_e32 v197, v197\n"
    "v_mfma_f32_16x16x32_bf16 v[200:203], a[100:103], a[20:23], v[200:203]\n"
    "buffer_load_dwordx4 v242, s[40:43], 0 offen offset:2048 nt lds\n"
    "ds_read_b64_tr_b16 a[178:179], v241 offset:17296\n"
    "v_mfma_f32_16x16x32_bf16 v[200:203], a[104:107], a[24:27], v[200:203]\n"
    "v_exp_f32_e32 v198, v198\n"
    "v_mfma_f32_16x16x32_bf16 v[200:203], a[108:111], a[28:31], v[200:203]\n"
    "s_waitcnt lgkmcnt(14)\n"
    "ds_read_b64_tr_b16 a[180:181], v241 offset:17552\n"
    "ds_read_b64_tr_b16 a[182:183], v241 offset:17808\n"
    "v_mfma_f32_16x16x32_bf16 v[8:11], a[144:147], v[208:211], v[8:11]\n"
    "s_waitcnt lgkmcnt(14)\n"
    "v_exp_f32_e32 v199, v199\n"
    "v_mfma_f32_16x16x32_bf16 v[8:11], a[148:151], v[212:215], v[8:11]\n"
    "s_waitcnt lgkmcnt(4)\n"
    "ds_read_b64_tr_b16 a[184:185], v241 offset:17048\n"
    "ds_read_b64_tr_b16 a[186:187], v241 offset:17304\n"
    "v_mfma_f32_16x16x32_bf16 v[12:15], a[152:155], v[208:211], v[12:15]\n"
    "v_exp_f32_e32 v200, v200\n"
    "v_mfma_f32_16x16x32_bf16 v[12:15], a[156:159], v[212:215], v[12:15]\n"
    "ds_read_b64_tr_b16 a[188:189], v241 offset:17560\n"
    "ds_read_b64_tr_b16 a[190:191], v241 offset:17816\n"
    "v_smfmac_f32_16x16x64_bf16 a[192:195], a[208:211], v[208:215], v254\n"
    "v_cvt_pk_bf16_f32 v216, v192, v193\n"
    "v_mfma_f32_16x16x32_bf16 v[204:207], a[112:115], a[16:19], v[228:231]\n"
    "s_add_u32 m0, s62, 67608\n"
    "v_exp_f32_e32 v201, v201\n"
    "v_mfma_f32_16x16x32_bf16 v[204:207], a[116:119], a[20:23], v[204:207]\n"
    "s_waitcnt vmcnt(21)\n"
    "ds_append v253\n"
    "v_cvt_pk_bf16_f32 v217, v194, v195\n"
    "v_mfma_f32_16x16x32_bf16 v[204:207], a[120:123], a[24:27], v[204:207]\n"
    "s_waitcnt lgkmcnt(14)\n"
    "v_exp_f32_e32 v202, v202\n"
    "v_mfma_f32_16x16x32_bf16 v[204:207], a[124:127], a[28:31], v[204:207]\n"
    "buffer_load_dwordx4 v242, s[40:43], 0 offen offset:3072 nt lds\n"
    "v_cvt_pk_bf16_f32 v218, v196, v197\n"
    "v_mfma_f32_16x16x32_bf16 v[16:19], a[160:163], v[208:211], v[16:19]\n"
    "s_add_u32 s40, s60, s71\n"
    "v_exp_f32_e32 v203, v203\n"
    "v_mfma_f32_16x16x32_bf16 v[16:19], a[164:167], v[212:215], v[16:19]\n"
    "s_waitcnt lgkmcnt(11)\n"
    "v_cvt_pk_bf16_f32 v219, v198, v199\n"
    "v_cvt_pk_bf16_f32 v220, v200, v201\n"
    "v_mfma_f32_16x16x32_bf16 v[20:23], a[168:171], v[208:211], v[20:23]\n"
    "s_addc_u32 s41, s61, 0\n"
    "v_exp_f32_e32 v204, v204\n"
    "v_mfma_f32_16x16x32_bf16 v[20:23], a[172:175], v[212:215], v[20:23]\n"
    "s_max_i32 s42, s58, 0\n"
    "v_cvt_pk_bf16_f32 v221, v202, v203\n"
    "v_lshlrev_b32_e32 v244, 16, v160\n"
    "v_mfma_f32_16x16x32_bf16 v[192:195], a[64:67], a[32:35], v[232:235]\n"
    "v_exp_f32_e32 v205, v205\n"
    "v_mfma_f32_16x16x32_bf16 v[192:195], a[68:71], a[36:39], v[192:195]\n"
    "v_cvt_pk_bf16_f32 v222, v204, v205\n"
    "v_and_b32_e32 v245, s67, v160\n"
    "v_mfma_f32_16x16x32_bf16 v[192:195], a[72:75], a[40:43], v[192:195]\n"
    "v_exp_f32_e32 v206, v206\n"
    "v_mfma_f32_16x16x32_bf16 v[192:195], a[76:79], a[44:47], v[192:195]\n"
    "s_waitcnt lgkmcnt(5)\n"
    "v_mul_f32_e32 v244, s64, v244\n"
    "v_mul_f32_e32 v245, s64, v245\n"
    "v_mfma_f32_16x16x32_bf16 v[24:27], a[176:179], v[208:211], v[24:27]\n"
    "v_exp_f32_e32 v207, v207\n"
    "v_mfma_f32_16x16x32_bf16 v[24:27], a[180:183], v[212:215], v[24:27]\n"
    "s_waitcnt lgkmcnt(1)\n"
    "v_cvt_pk_bf16_f32 v223, v206, v207\n"
    "v_cvt_pk_bf16_f32 v160, v244, v245\n"
    "v_mfma_f32_16x16x32_bf16 v[28:31], a[184:187], v[208:211], v[28:31]\n"
    "v_exp_f32_e32 v192, v192\n"
    "v_mfma_f32_16x16x32_bf16 v[28:31], a[188:191], v[212:215], v[28:31]\n"
    "ds_read_b32 v253, v251\n"
    "v_lshlrev_b32_e32 v244, 16, v161\n"
    "v_mfma_f32_16x16x32_bf16 v[196:199], a[80:83], a[32:35], v[232:235]\n"
    "v_exp_f32_e64 v193, v193\n"
    "v_mfma_f32_16x16x32_bf16 v[196:199], a[84:87], a[36:39], v[196:199]\n"
    "buffer_store_dwordx4 v[176:179], v250, s[48:51], 0 offen offset:0 sc0 sc1 nt\n"
    "v_cvt_pk_bf16_f32 v208, v192, v193\n"
    "v_and_b32_e32 v245, s67, v161\n"
    "v_mul_f32_e32 v244, s64, v244\n"
    "v_mul_f32_e64 v245, s64, v245\n"
    "v_mfma_f32_16x16x32_bf16 v[196:199], a[88:91], a[40:43], v[196:199]\n"
    "v_exp_f32_e32 v194, v194\n"
    "v_mfma_f32_16x16x32_bf16 v[196:199], a[92:95], a[44:47], v[196:199]\n"
    "v_cvt_pk_bf16_f32 v161, v244, v245\n"
    "v_lshlrev_b32_e32 v244, 16, v162\n"
    "v_mfma_f32_16x16x32_bf16 v[32:35], a[128:131], v[216:219], v[32:35]\n"
    "v_exp_f32_e32 v195, v195\n"
    "v_mfma_f32_16x16x32_bf16 v[32:35], a[132:135], v[220:223], v[32:35]\n"
    "s_waitcnt lgkmcnt(0)\n"
    "v_cmp_le_u32_e32 vcc, s76, v253\n"
    "v_cvt_pk_bf16_f32 v209, v194, v195\n"
    "v_mfma_f32_16x16x32_bf16 v[36:39], a[136:139], v[216:219], v[36:39]\n"
    "v_exp_f32_e32 v196, v196\n"
    "v_mfma_f32_16x16x32_bf16 v[36:39], a[140:143], v[220:223], v[36:39]\n"
    "v_and_b32_e32 v245, s67, v162\n"
    "v_mul_f32_e32 v244, s64, v244\n"
    "v_mfma_f32_16x16x32_bf16 v[200:203], a[96:99], a[32:35], v[232:235]\n"
    "s_cbranch_vccz LSPIN8_%=\n"
    "LRET8_%=:\n"
    "v_exp_f32_e32 v197, v197\n"
    "v_mfma_f32_16x16x32_bf16 v[200:203], a[100:103], a[36:39], v[200:203]\n"
    "s_addk_i32 s76, 256\n"
    "v_cvt_pk_bf16_f32 v210, v196, v197\n"
    "v_mul_f32_e32 v245, s64, v245\n"
    "v_mfma_f32_16x16x32_bf16 v[200:203], a[104:107], a[40:43], v[200:203]\n"
    "s_add_u32 m0, s62, 16896\n"
    "v_exp_f32_e32 v198, v198\n"
    "v_mfma_f32_16x16x32_bf16 v[200:203], a[108:111], a[44:47], v[200:203]\n"
    "buffer_load_dwordx4 v242, s[36:39], 0 offen offset:0 nt lds\n"
    "v_cvt_pk_bf16_f32 v162, v244, v245\n"
    "v_mfma_f32_16x16x32_bf16 v[40:43], a[144:147], v[216:219], v[40:43]\n"
    "v_exp_f32_e32 v199, v199\n"
    "v_mfma_f32_16x16x32_bf16 v[40:43], a[148:151], v[220:223], v[40:43]\n"
    "v_cvt_pk_bf16_f32 v211, v198, v199\n"
    "v_lshlrev_b32_e32 v244, 16, v163\n"
    "v_mfma_f32_16x16x32_bf16 v[44:47], a[152:155], v[216:219], v[44:47]\n"
    "v_exp_f32_e32 v200, v200\n"
    "v_mfma_f32_16x16x32_bf16 v[44:47], a[156:159], v[220:223], v[44:47]\n"
    "v_and_b32_e32 v245, s67, v163\n"
    "v_mul_f32_e32 v244, s64, v244\n"
    "v_smfmac_f32_16x16x64_bf16 a[196:199], a[208:211], v[216:223], v254\n"
    "v_mul_f32_e32 v245, s64, v245\n"
    "v_mfma_f32_16x16x32_bf16 v[204:207], a[112:115], a[32:35], v[232:235]\n"
    "v_exp_f32_e32 v201, v201\n"
    "v_mfma_f32_16x16x32_bf16 v[204:207], a[116:119], a[36:39], v[204:207]\n"
    "v_cvt_pk_bf16_f32 v212, v200, v201\n"
    "v_cvt_pk_bf16_f32 v163, v244, v245\n"
    "v_mfma_f32_16x16x32_bf16 v[204:207], a[120:123], a[40:43], v[204:207]\n"
    "v_exp_f32_e32 v202, v202\n"
    "v_mfma_f32_16x16x32_bf16 v[204:207], a[124:127], a[44:47], v[204:207]\n"
    "v_lshlrev_b32_e32 v244, 16, v164\n"
    "v_and_b32_e32 v245, s67, v164\n"
    "v_mfma_f32_16x16x32_bf16 v[48:51], a[160:163], v[216:219], v[48:51]\n"
    "s_add_u32 m0, s95, 2112\n"
    "v_exp_f32_e32 v203, v203\n"
    "v_mfma_f32_16x16x32_bf16 v[48:51], a[164:167], v[220:223], v[48:51]\n"
    "buffer_load_dwordx4 v243, s[44:47], 0 offen offset:0 lds\n"
    "v_cvt_pk_bf16_f32 v213, v202, v203\n"
    "v_mfma_f32_16x16x32_bf16 v[52:55], a[168:171], v[216:219], v[52:55]\n"
    "v_exp_f32_e32 v204, v204\n"
    "v_mfma_f32_16x16x32_bf16 v[52:55], a[172:175], v[220:223], v[52:55]\n"
    "v_mul_f32_e32 v244, s64, v244\n"
    "v_mul_f32_e32 v245, s64, v245\n"
    "v_mfma_f32_16x16x32_bf16 v[192:195], a[64:67], a[48:51], v[236:239]\n"
    "v_exp_f32_e32 v205, v205\n"
    "v_mfma_f32_16x16x32_bf16 v[192:195], a[68:71], a[52:55], v[192:195]\n"
    "ds_read_b128 a[64:67], v240 offset:33792\n"
    "v_cvt_pk_bf16_f32 v214, v204, v205\n"
    "v_mfma_f32_16x16x32_bf16 v[192:195], a[72:75], a[56:59], v[192:195]\n"
    "v_exp_f32_e32 v206, v206\n"
    "v_mfma_f32_16x16x32_bf16 v[192:195], a[76:79], a[60:63], v[192:195]\n"
    "ds_read_b128 a[68:71], v240 offset:33856\n"
    "ds_read_b128 a[72:75], v240 offset:33920\n"
    "v_mfma_f32_16x16x32_bf16 v[56:59], a[176:179], v[216:219], v[56:59]\n"
    "v_exp_f32_e32 v207, v207\n"
    "v_mfma_f32_16x16x32_bf16 v[56:59], a[180:183], v[220:223], v[56:59]\n"
    "ds_read_b128 a[76:79], v240 offset:33984\n"
    "v_cvt_pk_bf16_f32 v215, v206, v207\n"
    "v_mfma_f32_16x16x32_bf16 v[60:63], a[184:187], v[216:219], v[60:63]\n"
    "s_add_u32 m0, s95, 1152\n"
    "v_exp_f32_e32 v192, v192\n"
    "v_mfma_f32_16x16x32_bf16 v[60:63], a[188:191], v[220:223], v[60:63]\n"
    "buffer_load_dwordx4 v243, s[44:47], 0 offen offset:2048 lds\n"
    "v_cvt_pk_bf16_f32 v164, v244, v245\n"
    "v_mfma_f32_16x16x32_bf16 v[196:199], a[80:83], a[48:51], v[236:239]\n"
    "v_exp_f32_e32 v193, v193\n"
    "v_mfma_f32_16x16x32_bf16 v[196:199], a[84:87], a[52:55], v[196:199]\n"
    "ds_read_b128 a[80:83], v240 offset:34048\n"
    "v_cvt_pk_bf16_f32 v216, v192, v193\n"
    "v_mfma_f32_16x16x32_bf16 v[196:199], a[88:91], a[56:59], v[196:199]\n"
    "v_exp_f32_e32 v194, v194\n"
    "v_mfma_f32_16x16x32_bf16 v[196:199], a[92:95], a[60:63], v[196:199]\n"
    "ds_read_b128 a[84:87], v240 offset:34112\n"
    "ds_read_b128 a[88:91], v240 offset:34176\n"
    "v_mfma_f32_16x16x32_bf16 v[64:67], a[128:131], v[208:211], v[64:67]\n"
    "v_exp_f32_e32 v195, v195\n"
    "v_mfma_f32_16x16x32_bf16 v[64:67], a[132:135], v[212:215], v[64:67]\n"
    "ds_read_b128 a[92:95], v240 offset:34240\n"
    "v_cvt_pk_bf16_f32 v217, v194, v195\n"
    "v_mfma_f32_16x16x32_bf16 v[68:71], a[136:139], v[208:211], v[68:71]\n"
    "v_exp_f32_e32 v196, v196\n"
    "v_mfma_f32_16x16x32_bf16 v[68:71], a[140:143], v[212:215], v[68:71]\n"
    "v_lshlrev_b32_e32 v244, 16, v165\n"
    "v_and_b32_e32 v245, s67, v165\n"
    "v_mfma_f32_16x16x32_bf16 v[200:203], a[96:99], a[48:51], v[236:239]\n"
    "s_add_u32 m0, s62, 16928\n"
    "v_exp_f32_e32 v197, v197\n"
    "v_mfma_f32_16x16x32_bf16 v[200:203], a[100:103], a[52:55], v[200:203]\n"
    "buffer_load_dwordx4 v242, s[36:39], 0 offen offset:1024 nt lds\n"
    "ds_read_b128 a[96:99], v240 offset:34304\n"
    "v_mfma_f32_16x16x32_bf16 v[200:203], a[104:107], a[56:59], v[200:203]\n"
    "v_exp_f32_e32 v198, v198\n"
    "v_mfma_f32_16x16x32_bf16 v[200:203], a[108:111], a[60:63], v[200:203]\n"
    "ds_read_b128 a[100:103], v240 offset:34368\n"
    "v_cvt_pk_bf16_f32 v218, v196, v197\n"
    "v_mfma_f32_16x16x32_bf16 v[72:75], a[144:147], v[208:211], v[72:75]\n"
    "v_exp_f32_e32 v199, v199\n"
    "v_mfma_f32_16x16x32_bf16 v[72:75], a[148:151], v[212:215], v[72:75]\n"
    "ds_read_b128 a[104:107], v240 offset:34432\n"
    "ds_read_b128 a[108:111], v240 offset:34496\n"
    "v_mfma_f32_16x16x32_bf16 v[76:79], a[152:155], v[208:211], v[76:79]\n"
    "v_exp_f32_e32 v200, v200\n"
    "v_mfma_f32_16x16x32_bf16 v[76:79], a[156:159], v[212:215], v[76:79]\n"
    "v_cvt_pk_bf16_f32 v219, v198, v199\n"
    "v_mul_f32_e32 v244, s64, v244\n"
    "v_smfmac_f32_16x16x64_bf16 a[200:203], a[208:211], v[208:215], v254\n"
    "v_mul_f32_e32 v245, s64, v245\n"
    "v_mfma_f32_16x16x32_bf16 v[204:207], a[112:115], a[48:51], v[236:239]\n"
    "v_exp_f32_e32 v201, v201\n"
    "v_mfma_f32_16x16x32_bf16 v[204:207], a[116:119], a[52:55], v[204:207]\n"
    "ds_read_b128 a[112:115], v240 offset:34560\n"
    "v_cvt_pk_bf16_f32 v220, v200, v201\n"
    "v_mfma_f32_16x16x32_bf16 v[204:207], a[120:123], a[56:59], v[204:207]\n"
    "s_add_u32 m0, s62, 16960\n"
    "v_exp_f32_e32 v202, v202\n"
    "v_mfma_f32_16x16x32_bf16 v[204:207], a[124:127], a[60:63], v[204:207]\n"
    "buffer_load_dwordx4 v242, s[36:39], 0 offen offset:2048 nt lds\n"
    "ds_read_b128 a[116:119], v240 offset:34624\n"
    "v_mfma_f32_16x16x32_bf16 v[80:83], a[160:163], v[208:211], v[80:83]\n"
    "v_exp_f32_e32 v203, v203\n"
    "v_mfma_f32_16x16x32_bf16 v[80:83], a[164:167], v[212:215], v[80:83]\n"
    "ds_read_b128 a[120:123], v240 offset:34688\n"
    "v_cvt_pk_bf16_f32 v221, v202, v203\n"
    "v_mfma_f32_16x16x32_bf16 v[84:87], a[168:171], v[208:211], v[84:87]\n"
    "v_exp_f32_e32 v204, v204\n"
    "v_mfma_f32_16x16x32_bf16 v[84:87], a[172:175], v[212:215], v[84:87]\n"
    "s_waitcnt lgkmcnt(11)\n"
    "ds_read_b128 a[124:127], v240 offset:34752\n"
    "v_cvt_pk_bf16_f32 v165, v244, v245\n"
    "s_cmp_eq_u32 s70, 3\n"
    "s_cbranch_scc0 LTOP_%=\n"
    "s_branch LTAIL_%=\n"
    "LEND_%=:\n"
    : [bad0] "=&s"(bad0), [bad1] "=&s"(bad1), [bad2] "=&s"(bad2), [bad3] "=&s"(bad3), [ka] "+s"(ka)
    : [wv] "s"(wv), [wg] "s"(w)
    : "v0", "v1", "v2", "v3", "v4", "v5", "v6", "v7", "v8", "v9", "v10", "v11", "v12", "v13", "v14", "v15", "v16", "v17", "v18", "v19", "v20", "v21", "v22", "v23", "v24", "v25", "v26", "v27", "v28", "v29", "v30", "v31", "v32", "v33", "v34", "v35", "v36", "v37", "v38", "v39", "v40", "v41", "v42", "v43", "v44", "v45", "v46", "v47", "v48", "v49", "v50", "v51", "v52", "v53", "v54", "v55", "v56", "v57", "v58", "v59", "v60", "v61", "v62", "v63", "v64", "v65", "v66", "v67", "v68", "v69", "v70", "v71", "v72", "v73", "v74", "v75", "v76", "v77", "v78", "v79", "v80", "v81", "v82", "v83", "v84", "v85", "v86", "v87", "v88", "v89", "v90", "v91", "v92", "v93", "v94", "v95", "v96", "v97", "v98", "v99", "v100", "v101", "v102", "v103", "v104", "v105", "v106", "v107", "v108", "v109", "v110", "v111", "v112", "v113", "v114", "v115", "v116", "v117", "v118", "v119", "v120", "v121", "v122", "v123", "v124", "v125", "v126", "v127", "v128", "v129", "v130", "v131", "v132", "v133", "v134", "v135", "v136", "v137", "v138", "v139", "v140", "v141", "v142", "v143", "v144", "v145", "v146", "v147", "v148", "v149", "v150", "v151", "v152", "v153", "v154", "v155", "v156", "v157", "v158", "v159", "v160", "v161", "v162", "v163", "v164", "v165", "v166", "v167", "v168", "v169", "v170", "v171", "v172", "v173", "v174", "v175", "v176", "v177", "v178", "v179", "v180", "v181", "v182", "v183", "v184", "v185", "v186", "v187", "v188", "v189", "v190", "v191", "v192", "v193", "v194", "v195", "v196", "v197", "v198", "v199", "v200", "v201", "v202", "v203", "v204", "v205", "v206", "v207", "v208", "v209", "v210", "v211", "v212", "v213", "v214", "v215", "v216", "v217", "v218", "v219", "v220", "v221", "v222", "v223", "v224", "v225", "v226", "v227", "v228", "v229", "v230", "v231", "v232", "v233", "v234", "v235", "v236", "v237", "v238", "v239", "v240", "v241", "v242", "v243", "v244", "v245", "v246", "v247", "v248", "v249", "v250", "v251", "v252", "v253", "v254", "v255", "a0", "a1", "a2", "a3", "a4", "a5", "a6", "a7", "a8", "a9", "a10", "a11", "a12", "a13", "a14", "a15", "a16", "a17", "a18", "a19", "a20", "a21", "a22", "a23", "a24", "a25", "a26", "a27", "a28", "a29", "a30", "a31", "a32", "a33", "a34", "a35", "a36", "a37", "a38", "a39", "a40", "a41", "a42", "a43", "a44", "a45", "a46", "a47", "a48", "a49", "a50", "a51", "a52", "a53", "a54", "a55", "a56", "a57", "a58", "a59", "a60", "a61", "a62", "a63", "a64", "a65", "a66", "a67", "a68", "a69", "a70", "a71", "a72", "a73", "a74", "a75", "a76", "a77", "a78", "a79", "a80", "a81", "a82", "a83", "a84", "a85", "a86", "a87", "a88", "a89", "a90", "a91", "a92", "a93", "a94", "a95", "a96", "a97", "a98", "a99", "a100", "a101", "a102", "a103", "a104", "a105", "a106", "a107", "a108", "a109", "a110", "a111", "a112", "a113", "a114", "a115", "a116", "a117", "a118", "a119", "a120", "a121", "a122", "a123", "a124", "a125", "a126", "a127", "a128", "a129", "a130", "a131", "a132", "a133", "a134", "a135", "a136", "a137", "a138", "a139", "a140", "a141", "a142", "a143", "a144", "a145", "a146", "a147", "a148", "a149", "a150", "a151", "a152", "a153", "a154", "a155", "a156", "a157", "a158", "a159", "a160", "a161", "a162", "a163", "a164", "a165", "a166", "a167", "a168", "a169", "a170", "a171", "a172", "a173", "a174", "a175", "a176", "a177", "a178", "a179", "a180", "a181", "a182", "a183", "a184", "a185", "a186", "a187", "a188", "a189", "a190", "a191", "a192", "a193", "a194", "a195", "a196", "a197", "a198", "a199", "a200", "a201", "a202", "a203", "a204", "a205", "a206", "a207", "a208", "a209", "a210", "a211", "a212", "a213", "a214", "a215", "a216", "a217", "a218", "a219", "a220", "a221", "a222", "a223", "a224", "a225", "a226", "a227", "a228", "a229", "a230", "a231", "a232", "a233", "a234", "a235", "a236", "a237", "a238", "a239", "a240", "a241", "a242", "a243", "a244", "a245", "a246", "a247", "a248", "a249", "a250", "a251", "a252", "a253", "a254", "a255", "s36", "s37", "s38", "s39", "s40", "s41", "s42", "s43", "s44", "s45", "s46", "s47", "s48", "s49", "s50", "s51", "s52", "s53", "s54", "s55", "s56", "s57", "s58", "s59", "s60", "s61", "s62", "s63", "s64", "s65", "s66", "s67", "s68", "s69", "s70", "s71", "s72", "s73", "s74", "s75", "s76", "s77", "s78", "s79", "s80", "s81", "s82", "s83", "s84", "s85", "s86", "s87", "s88", "s89", "s90", "s91", "s92", "s93", "s94", "s95", "s96", "s97", "s98", "s99", "vcc", "scc", "memory");
  typedef const __attribute__((address_space(1))) char* gp;
  const char* Qp = (const char*)(gp)ka[0]; const char* Kp = (const char*)(gp)ka[1]; const char* Vp = (const char*)(gp)ka[2]; char* Op = (char*)(gp)ka[3];
  const unsigned c = (w >> 3) & 3, head0 = ((w >> 5) * 8 + (w & 7)) * 4;
  const unsigned lane = __builtin_amdgcn_mbcnt_hi(~0u, __builtin_amdgcn_mbcnt_lo(~0u, 0u));
#pragma unroll 1
  for (unsigned n = 0; n < 256; ++n) {
    const unsigned r = n >> 6, i = n & 63, j = i >> 4, row = c * 256 + (i >> 4) * 64 + wv * 16 + (i & 15), h = head0 + r;
    const u64 bad = j == 0 ? bad0 : j == 1 ? bad1 : j == 2 ? bad2 : bad3;
    if (row < 992 && ((bad >> (16 * (3 - r) + (i & 15))) & 1))
      exact_row(Qp + ((size_t)h * 992 + row) * 256, Kp + (size_t)h * 131072, Vp + (size_t)h * 131072, Op + ((size_t)h * 992 + row) * 256, lane);
  }
}

extern "C" void kernel_launch(void* const* d_in, const int* in_sizes, int n_in, void* d_out, int out_size, void* d_ws, size_t ws_size, hipStream_t stream) {
  (void)hipFuncSetAttribute((const void*)gqa_attn_kernel, hipFuncAttributeMaxDynamicSharedMemorySize, LDS_BYTES);
  hipLaunchKernelGGL(gqa_attn_kernel, dim3(256), dim3(256), LDS_BYTES, stream, (const char*)d_in[0], (const char*)d_in[1], (const char*)d_in[2], (char*)d_out
  );
}
```
